```python
import torch

def _softmax(x, axis=-1):
    return torch.softmax(x, dim=axis)
def _gelu(x):
    return torch.nn.functional.gelu(x, approximate="tanh")
def _silu(x):
    return torch.nn.functional.silu(x)
def _sigmoid(x):
    return torch.sigmoid(x)
def _layernorm(x, axis=-1, eps=1e-5):
    m = x.mean(dim=axis, keepdim=True)
    v = x.var(dim=axis, keepdim=True, unbiased=False)
    return (x - m) / torch.sqrt(v + eps)
def _scatter_add(tgt, idx, src, axis=0):
    return tgt.clone().index_add_(axis, idx, src)
def _segment_sum(src, seg, n):
    out = torch.zeros((n,) + src.shape[1:], dtype=src.dtype)
    return out.index_add_(0, seg, src)
def _pad(x, pw):
    flat = tuple(v for pair in reversed(pw) for v in pair)
    return torch.nn.functional.pad(x, flat)

kernel_name = 'standalone__mha_softmax__near_ridge'

def setup_inputs():

    _w = (lambda a: (lambda t: t.to(torch.bfloat16) if t.is_floating_point() else t)(torch.as_tensor(a)))
    B, H, Sq, Sk, D = 8, 16, 624, 1024, 256
    return {
        "q": _w((lambda *s: torch.randn(*s, dtype=torch.bfloat16) * 0.8)(B, H, Sq, D)),
        "k": _w((lambda *s: torch.randn(*s, dtype=torch.bfloat16) * 0.8)(B, H, Sk, D)),
        "v": _w((lambda *s: torch.randn(*s, dtype=torch.bfloat16) * 0.05)(B, H, Sk, D)),
    }

def reference(q, k, v):
    scale = q.shape[-1] ** -0.5
    scores = torch.einsum("bhqd,bhkd->bhqk", q, k) * scale
    p = _softmax(scores, axis=-1)
    return torch.einsum("bhqk,bhkd->bhqd", p, v)
```

```cpp
#include <hip/hip_runtime.h>
#include <hip/hip_bf16.h>

__global__ __launch_bounds__(256, 1) __attribute__((aligned(4096))) void mha_fwd_kernel(const char* Q, const char* K, const char* V, char* O) {
  __shared__ __attribute__((aligned(16))) char lds[163840];
  const unsigned g = blockIdx.x, grp = (g & 7u) * 8u + (g >> 5), qd = (g >> 3) & 3u;
  const unsigned wid = __builtin_amdgcn_readfirstlane(threadIdx.x >> 6);
  const unsigned ldsb = (unsigned)(size_t)lds;
  asm volatile(
    "s_load_dwordx8 s[36:43], %[ka], 0\n"
    ".p2align 6\n"
    "s_getpc_b64 s[22:23]\n"
    "s_lshl_b32 s62, %[loc], 2\n"
    "s_add_u32 s62, s62, %[qd]\n"
    "s_lshl_b32 s62, s62, 2\n"
    "s_add_u32 s62, s62, %[wid]\n"
    "s_lshl_b32 s62, s62, 7\n"
    "s_add_u32 s22, s22, s62\n"
    "s_addc_u32 s23, s23, 0\n"
    "s_mov_b32 s46, 319488\n"
    "s_mov_b32 s47, 0x20000\n"
    "s_mov_b32 s50, 0\n"
    "s_mov_b32 s51, 0x20000\n"
    "s_lshl_b32 s52, %[wid], 12\n"
    "s_mov_b32 s53, s52\n"
    "s_add_u32 s54, s52, %[lds]\n"
    "s_mul_i32 s62, %[qd], 81920\n"
    "s_lshl_b32 s63, %[wid], 14\n"
    "s_add_u32 s55, s62, s63\n"
    "s_add_u32 s56, s55, 8192\n"
    "s_lshr_b32 s63, %[wid], 1\n"
    "s_lshl_b32 s63, s63, 13\n"
    "s_add_u32 s63, s63, 65536\n"
    "s_add_u32 s57, s62, s63\n"
    "s_mov_b32 s58, 0x3db8aa3b\n"
    "s_mov_b32 s64, 0xbdb8aa3b\n"
    "s_mov_b32 s59, 0x43800000\n"
    "s_mov_b32 s61, 0\n"
    "s_and_b32 s62, %[wid], 1\n"
    "v_mbcnt_lo_u32_b32 v64, -1, 0\n"
    "v_mbcnt_hi_u32_b32 v64, -1, v64\n"
    "v_lshrrev_b32 v65, 2, v64\n"
    "v_and_b32 v65, 7, v65\n"
    "v_lshlrev_b32 v66, 9, v65\n"
    "v_lshrrev_b32 v67, 5, v64\n"
    "v_lshl_add_u32 v66, v67, 6, v66\n"
    "v_and_b32 v67, 3, v64\n"
    "s_lshl_b32 s63, s62, 1\n"
    "v_xor_b32 v68, s63, v67\n"
    "v_lshl_add_u32 v210, v68, 4, v66\n"
    "v_lshrrev_b32 v68, 2, v65\n"
    "v_lshlrev_b32 v68, 1, v68\n"
    "v_xor_b32 v68, v68, v67\n"
    "v_lshl_add_u32 v211, v68, 4, v66\n"
    "s_add_u32 s69, s55, 4096\n"
    "s_add_u32 s70, s56, 4096\n"
    "s_add_u32 s71, s57, 4096\n"
    "s_lshl_b32 s63, %[wid], 13\n"
    "s_add_u32 s63, s63, 131072\n"
    "s_add_u32 s68, s63, %[lds]\n"
    "s_mov_b32 s65, s68\n"
    "s_lshl_b32 s63, s62, 8\n"
    "s_add_u32 s24, s57, s63\n"
    "s_add_u32 s25, s24, 128\n"
    "s_add_u32 s26, s24, 4096\n"
    "s_add_u32 s27, s24, 4224\n"
    "s_mov_b32 s28, s55\n"
    "s_add_u32 s29, s55, 128\n"
    "s_add_u32 s30, s55, 4096\n"
    "s_add_u32 s31, s55, 4224\n"
    "s_waitcnt lgkmcnt(0)\n"
    "s_mul_i32 s63, %[grp], 638976\n"
    "s_add_u32 s44, s36, s63\n"
    "s_addc_u32 s45, s37, 0\n"
    "s_add_u32 s48, s42, s63\n"
    "s_addc_u32 s49, s43, 0\n"
    "s_lshl_b32 s63, %[grp], 20\n"
    "s_add_u32 s36, s38, s63\n"
    "s_addc_u32 s37, s39, 0\n"
    "s_add_u32 s40, s40, s63\n"
    "s_addc_u32 s41, s41, 0\n"
    "s_and_b32 s37, s37, 0xffff\n"
    "s_and_b32 s41, s41, 0xffff\n"
    "s_and_b32 s45, s45, 0xffff\n"
    "s_and_b32 s49, s49, 0xffff\n"
    "s_mov_b32 s38, 0x100000\n"
    "s_mov_b32 s39, 0x20000\n"
    "s_mov_b32 s42, 0x100000\n"
    "s_mov_b32 s43, 0x20000\n"
    "s_load_dword s60, s[36:37], 0\n"
    "s_load_dword s60, s[44:45], 0\n"
    "s_load_dword s60, s[40:41], 0\n"
    "s_load_dword s60, s[40:41], 1048572\n"
    "s_load_dword s60, s[36:37], 1048572\n"
    "s_load_dword s60, s[44:45], 638972\n"
    "s_load_dword s60, s[22:23], 0\n"
    "s_load_dword s60, s[22:23], 16384\n"
    "s_load_dword s60, s[22:23], 32768\n"
    "s_add_u32 m0, s54, 65536\n"
    "s_nop 0\n"
    "buffer_load_dwordx4 v210, s[44:47], s55 offen offset:0 nt lds\n"
    "s_add_u32 m0, s54, 0\n"
    "s_nop 0\n"
    "buffer_load_dwordx4 v210, s[36:39], s52 offen offset:0 lds\n"
    "s_add_u32 m0, s54, 66432\n"
    "s_nop 0\n"
    "buffer_load_dwordx4 v210, s[44:47], s55 offen offset:128 nt lds\n"
    "s_add_u32 m0, s54, 896\n"
    "s_nop 0\n"
    "buffer_load_dwordx4 v210, s[36:39], s52 offen offset:128 lds\n"
    "s_add_u32 m0, s54, 67328\n"
    "s_nop 0\n"
    "buffer_load_dwordx4 v210, s[44:47], s55 offen offset:256 nt lds\n"
    "s_add_u32 m0, s54, 1792\n"
    "s_nop 0\n"
    "buffer_load_dwordx4 v210, s[36:39], s52 offen offset:256 lds\n"
    "s_add_u32 m0, s54, 68224\n"
    "s_nop 0\n"
    "buffer_load_dwordx4 v210, s[44:47], s55 offen offset:384 nt lds\n"
    "s_add_u32 m0, s54, 2688\n"
    "s_nop 0\n"
    "buffer_load_dwordx4 v210, s[36:39], s52 offen offset:384 lds\n"
    "s_add_u32 s52, s52, 16384\n"
    "v_and_b32 v65, 15, v64\n"
    "v_lshrrev_b32 v66, 4, v64\n"
    "v_lshrrev_b32 v69, 1, v66\n"
    "v_lshrrev_b32 v67, 3, v64\n"
    "v_and_b32 v68, 7, v64\n"
    "v_lshlrev_b32 v70, 9, v67\n"
    "v_lshl_add_u32 v70, v68, 4, v70\n"
    "s_lshl_b32 s63, s62, 8\n"
    "v_add_u32 v215, s63, v70\n"
    "v_xor_b32 v216, 0x100, v215\n"
    "v_xor_b32 v68, v68, v67\n"
    "v_lshlrev_b32 v68, 4, v68\n"
    "v_lshl_add_u32 v68, v67, 7, v68\n"
    "v_add_u32 v225, s68, v68\n"
    "v_and_b32 v67, 7, v65\n"
    "v_xor_b32 v67, v67, v69\n"
    "v_lshlrev_b32 v67, 4, v67\n"
    "v_lshl_add_u32 v67, v65, 7, v67\n"
    "v_and_b32 v68, 1, v66\n"
    "v_lshl_add_u32 v67, v68, 3, v67\n"
    "v_add_u32 v221, s68, v67\n"
    "v_xor_b32 v222, 32, v221\n"
    "v_xor_b32 v223, 64, v221\n"
    "v_xor_b32 v224, 0x60, v221\n"
    "v_and_b32 v67, 7, v65\n"
    "v_lshlrev_b32 v67, 6, v67\n"
    "v_lshl_add_u32 v67, v66, 4, v67\n"
    "v_lshrrev_b32 v68, 3, v65\n"
    "v_lshl_add_u32 v70, v68, 11, v67\n"
    "s_and_b32 s63, s68, 0xffffdfff\n"
    "v_add_u32 v213, s63, v70\n"
    "s_lshl_b32 s63, s62, 5\n"
    "v_xor_b32 v70, s63, v70\n"
    "v_xor_b32 v67, s63, v67\n"
    "s_add_u32 s63, s68, 4096\n"
    "v_add_u32 v214, s63, v70\n"
    "v_lshl_add_u32 v67, v68, 14, v67\n"
    "v_add_u32 v212, s54, v67\n"
    "v_lshrrev_b32 v67, 3, v65\n"
    "v_lshlrev_b32 v68, 1, v67\n"
    "v_xor_b32 v68, v68, v66\n"
    "v_lshlrev_b32 v68, 4, v68\n"
    "v_lshl_add_u32 v68, v67, 12, v68\n"
    "v_and_b32 v67, 7, v65\n"
    "v_lshl_add_u32 v68, v67, 6, v68\n"
    "s_lshl_b32 s63, s62, 14\n"
    "v_xor_b32 v68, s63, v68\n"
    "v_add_u32 v200, %[lds], v68\n"
    "v_xor_b32 v68, 0x4000, v68\n"
    "v_add_u32 v201, %[lds], v68\n"
    "v_and_b32 v67, 1, v66\n"
    "v_lshrrev_b32 v68, 2, v64\n"
    "v_and_b32 v68, 7, v68\n"
    "v_lshlrev_b32 v68, 6, v68\n"
    "v_lshl_add_u32 v68, v69, 12, v68\n"
    "v_lshl_add_u32 v68, v67, 5, v68\n"
    "v_and_b32 v67, 3, v64\n"
    "v_lshl_add_u32 v68, v67, 3, v68\n"
    "s_mul_i32 s63, s62, 0x4800\n"
    "v_xor_b32 v68, s63, v68\n"
    "s_add_u32 s63, %[lds], 0x10000\n"
    "v_xor_b32 v67, 0x0, v68\n"
    "v_add_u32 v202, s63, v67\n"
    "v_xor_b32 v67, 0x800, v68\n"
    "v_add_u32 v203, s63, v67\n"
    "v_xor_b32 v67, 0x20, v68\n"
    "v_add_u32 v204, s63, v67\n"
    "v_xor_b32 v67, 0x820, v68\n"
    "v_add_u32 v205, s63, v67\n"
    "v_xor_b32 v67, 0x4000, v68\n"
    "v_add_u32 v206, s63, v67\n"
    "v_xor_b32 v67, 0x4800, v68\n"
    "v_add_u32 v207, s63, v67\n"
    "v_xor_b32 v67, 0x4020, v68\n"
    "v_add_u32 v208, s63, v67\n"
    "v_xor_b32 v67, 0x4820, v68\n"
    "v_add_u32 v209, s63, v67\n"
    "s_add_u32 s63, s54, 65536\n"
    "v_lshl_add_u32 v218, v64, 4, s63\n"
    "v_xor_b32 v217, 0x1000, v218\n"
    "v_lshl_add_u32 v220, v64, 3, s68\n"
    "v_xor_b32 v219, 0x2000, v220\n"
    "v_mov_b32 v226, v225\n"
    "v_add_u32 v212, 0x10000, v212\n"
    "s_add_u32 m0, s54, 81920\n"
    "s_nop 0\n"
    "buffer_load_dwordx4 v210, s[44:47], s69 offen offset:0 nt lds\n"
    "s_add_u32 m0, s54, 16384\n"
    "s_nop 0\n"
    "buffer_load_dwordx4 v210, s[36:39], s52 offen offset:0 lds\n"
    "s_add_u32 m0, s54, 82816\n"
    "s_nop 0\n"
    "buffer_load_dwordx4 v210, s[44:47], s69 offen offset:128 nt lds\n"
    "s_add_u32 m0, s54, 17280\n"
    "s_nop 0\n"
    "buffer_load_dwordx4 v210, s[36:39], s52 offen offset:128 lds\n"
    "s_add_u32 m0, s54, 83712\n"
    "s_nop 0\n"
    "buffer_load_dwordx4 v210, s[44:47], s69 offen offset:256 nt lds\n"
    "s_add_u32 m0, s54, 18176\n"
    "s_nop 0\n"
    "buffer_load_dwordx4 v210, s[36:39], s52 offen offset:256 lds\n"
    "s_add_u32 m0, s54, 84608\n"
    "s_nop 0\n"
    "buffer_load_dwordx4 v210, s[44:47], s69 offen offset:384 nt lds\n"
    "s_add_u32 m0, s54, 19072\n"
    "s_nop 0\n"
    "buffer_load_dwordx4 v210, s[36:39], s52 offen offset:384 lds\n"
    "s_add_u32 s52, s52, 16384\n"
    "s_mov_b64 exec, 0\n"
    "s_cmp_eq_u32 %[wid], 0\n"
    "s_cbranch_scc1 Lpad0_%=\n"
    "s_cmp_eq_u32 %[wid], 1\n"
    "s_cbranch_scc1 Lpad1_%=\n"
    "s_cmp_eq_u32 %[wid], 2\n"
    "s_cbranch_scc1 Lpad2_%=\n"
    "s_cmp_eq_u32 %[wid], 3\n"
    "s_cbranch_scc1 Lpad3_%=\n"
    "Lhopd0_%=:\n"
    "s_mov_b64 exec, -1\n"
    "s_branch Lgap_%=\n"
    "s_nop 0\n"
    "s_nop 0\n"
    "s_nop 0\n"
    "s_nop 0\n"
    "s_nop 0\n"
    "s_nop 0\n"
    "s_nop 0\n"
    "s_nop 0\n"
    "s_nop 0\n"
    "s_nop 0\n"
    "s_nop 0\n"
    "s_nop 0\n"
    "s_nop 0\n"
    "s_nop 0\n"
    "s_nop 0\n"
    "s_nop 0\n"
    "s_nop 0\n"
    "s_nop 0\n"
    "s_nop 0\n"
    "s_nop 0\n"
    "s_nop 0\n"
    "s_nop 0\n"
    "s_nop 0\n"
    "s_nop 0\n"
    "s_nop 0\n"
    "s_nop 0\n"
    "s_nop 0\n"
    "s_nop 0\n"
    "s_nop 0\n"
    "s_nop 0\n"
    "s_nop 0\n"
    "s_nop 0\n"
    "s_nop 0\n"
    "s_nop 0\n"
    "s_nop 0\n"
    "s_nop 0\n"
    "s_nop 0\n"
    "s_nop 0\n"
    "s_nop 0\n"
    "s_nop 0\n"
    "s_nop 0\n"
    "s_nop 0\n"
    "s_nop 0\n"
    "s_nop 0\n"
    "s_nop 0\n"
    "s_nop 0\n"
    "s_nop 0\n"
    "s_nop 0\n"
    "s_nop 0\n"
    "s_nop 0\n"
    "s_nop 0\n"
    "s_nop 0\n"
    "s_nop 0\n"
    "s_nop 0\n"
    "s_nop 0\n"
    "s_nop 0\n"
    "s_nop 0\n"
    "s_nop 0\n"
    "s_nop 0\n"
    "s_nop 0\n"
    "s_nop 0\n"
    "s_nop 0\n"
    "s_nop 0\n"
    "s_nop 0\n"
    "s_nop 0\n"
    "s_nop 0\n"
    "s_nop 0\n"
    "s_nop 0\n"
    "s_nop 0\n"
    "s_nop 0\n"
    "s_nop 0\n"
    "s_nop 0\n"
    "s_nop 0\n"
    "s_nop 0\n"
    "s_nop 0\n"
    "s_nop 0\n"
    "s_nop 0\n"
    "s_nop 0\n"
    "s_nop 0\n"
    "s_nop 0\n"
    "s_nop 0\n"
    "s_nop 0\n"
    "s_nop 0\n"
    "s_nop 0\n"
    "s_nop 0\n"
    "s_nop 0\n"
    "s_nop 0\n"
    "s_nop 0\n"
    "s_nop 0\n"
    "s_nop 0\n"
    "s_nop 0\n"
    "s_nop 0\n"
    "s_nop 0\n"
    "s_nop 0\n"
    "s_nop 0\n"
    "s_nop 0\n"
    "s_nop 0\n"
    "s_nop 0\n"
    "s_nop 0\n"
    "s_nop 0\n"
    "s_nop 0\n"
    "s_nop 0\n"
    "s_nop 0\n"
    "s_nop 0\n"
    "s_nop 0\n"
    "s_nop 0\n"
    "s_nop 0\n"
    "s_nop 0\n"
    "s_nop 0\n"
    "s_nop 0\n"
    "s_nop 0\n"
    "s_nop 0\n"
    "s_nop 0\n"
    "s_nop 0\n"
    "s_nop 0\n"
    "s_nop 0\n"
    "s_nop 0\n"
    "s_nop 0\n"
    "s_nop 0\n"
    "s_nop 0\n"
    "s_nop 0\n"
    "s_nop 0\n"
    "s_nop 0\n"
    "s_nop 0\n"
    "s_nop 0\n"
    "s_nop 0\n"
    "s_nop 0\n"
    "s_nop 0\n"
    "s_nop 0\n"
    "s_nop 0\n"
    "s_nop 0\n"
    "s_nop 0\n"
    "s_nop 0\n"
    "s_nop 0\n"
    "s_nop 0\n"
    "s_nop 0\n"
    "s_nop 0\n"
    "s_nop 0\n"
    "s_nop 0\n"
    "s_nop 0\n"
    "s_nop 0\n"
    "s_nop 0\n"
    "s_nop 0\n"
    "s_nop 0\n"
    "s_nop 0\n"
    "s_nop 0\n"
    "s_nop 0\n"
    "s_nop 0\n"
    "s_nop 0\n"
    "s_nop 0\n"
    "s_nop 0\n"
    "s_nop 0\n"
    "s_nop 0\n"
    "s_nop 0\n"
    "s_nop 0\n"
    "s_nop 0\n"
    "s_nop 0\n"
    "s_nop 0\n"
    "s_nop 0\n"
    "s_nop 0\n"
    "s_nop 0\n"
    "s_nop 0\n"
    "s_nop 0\n"
    "s_nop 0\n"
    "s_nop 0\n"
    "s_nop 0\n"
    "s_nop 0\n"
    "s_nop 0\n"
    "s_nop 0\n"
    "s_nop 0\n"
    "s_nop 0\n"
    "s_nop 0\n"
    "s_nop 0\n"
    "s_nop 0\n"
    "s_nop 0\n"
    "s_nop 0\n"
    "s_nop 0\n"
    "s_nop 0\n"
    "s_nop 0\n"
    "s_nop 0\n"
    "s_nop 0\n"
    "s_nop 0\n"
    "s_nop 0\n"
    "s_nop 0\n"
    "s_nop 0\n"
    "s_nop 0\n"
    "s_nop 0\n"
    "s_nop 0\n"
    "s_nop 0\n"
    "s_nop 0\n"
    "s_nop 0\n"
    "s_nop 0\n"
    "s_nop 0\n"
    "s_nop 0\n"
    "s_nop 0\n"
    "s_nop 0\n"
    "s_nop 0\n"
    "s_nop 0\n"
    "s_nop 0\n"
    "s_nop 0\n"
    "s_nop 0\n"
    "s_nop 0\n"
    "s_nop 0\n"
    "s_nop 0\n"
    "s_nop 0\n"
    "s_nop 0\n"
    "s_nop 0\n"
    "s_nop 0\n"
    "s_nop 0\n"
    "s_nop 0\n"
    "s_nop 0\n"
    "s_nop 0\n"
    "s_nop 0\n"
    "s_nop 0\n"
    "s_nop 0\n"
    "s_nop 0\n"
    "s_nop 0\n"
    "s_nop 0\n"
    "s_nop 0\n"
    "s_nop 0\n"
    "s_nop 0\n"
    "s_nop 0\n"
    "s_nop 0\n"
    "s_nop 0\n"
    "s_nop 0\n"
    "s_nop 0\n"
    "s_nop 0\n"
    "s_nop 0\n"
    "s_nop 0\n"
    "s_nop 0\n"
    "s_nop 0\n"
    "s_nop 0\n"
    "s_nop 0\n"
    "s_nop 0\n"
    "s_nop 0\n"
    "s_nop 0\n"
    "s_nop 0\n"
    "s_nop 0\n"
    "s_nop 0\n"
    "s_nop 0\n"
    "s_nop 0\n"
    "s_nop 0\n"
    "s_nop 0\n"
    "s_nop 0\n"
    "s_nop 0\n"
    "s_nop 0\n"
    "s_nop 0\n"
    "s_nop 0\n"
    "s_nop 0\n"
    "s_nop 0\n"
    "s_nop 0\n"
    "s_nop 0\n"
    "s_nop 0\n"
    "s_nop 0\n"
    "s_nop 0\n"
    "s_nop 0\n"
    "s_nop 0\n"
    "s_nop 0\n"
    "s_nop 0\n"
    "s_nop 0\n"
    "s_nop 0\n"
    "s_nop 0\n"
    "s_nop 0\n"
    "s_nop 0\n"
    "s_nop 0\n"
    "s_nop 0\n"
    "s_nop 0\n"
    "s_nop 0\n"
    "s_nop 0\n"
    "s_nop 0\n"
    "s_nop 0\n"
    "s_nop 0\n"
    "s_nop 0\n"
    "s_nop 0\n"
    "s_nop 0\n"
    "s_nop 0\n"
    "s_nop 0\n"
    "s_nop 0\n"
    "s_nop 0\n"
    "s_nop 0\n"
    "s_nop 0\n"
    "s_nop 0\n"
    "s_nop 0\n"
    "s_nop 0\n"
    "s_nop 0\n"
    "s_nop 0\n"
    "s_nop 0\n"
    "s_nop 0\n"
    "s_nop 0\n"
    "s_nop 0\n"
    "s_nop 0\n"
    "s_nop 0\n"
    "s_nop 0\n"
    "s_nop 0\n"
    "s_nop 0\n"
    "s_nop 0\n"
    "s_nop 0\n"
    "s_nop 0\n"
    "s_nop 0\n"
    "s_nop 0\n"
    "s_nop 0\n"
    "s_nop 0\n"
    "s_nop 0\n"
    "s_nop 0\n"
    "s_nop 0\n"
    "s_nop 0\n"
    "s_nop 0\n"
    "s_nop 0\n"
    "s_nop 0\n"
    "s_nop 0\n"
    "s_nop 0\n"
    "s_nop 0\n"
    "s_nop 0\n"
    "s_nop 0\n"
    "s_nop 0\n"
    "s_nop 0\n"
    "s_nop 0\n"
    "s_nop 0\n"
    "s_nop 0\n"
    "s_nop 0\n"
    "s_nop 0\n"
    "s_nop 0\n"
    "s_nop 0\n"
    "s_nop 0\n"
    "s_nop 0\n"
    "s_nop 0\n"
    "s_nop 0\n"
    "s_nop 0\n"
    "s_nop 0\n"
    "s_nop 0\n"
    "s_nop 0\n"
    "s_nop 0\n"
    "s_nop 0\n"
    "s_nop 0\n"
    "s_nop 0\n"
    "s_nop 0\n"
    "s_nop 0\n"
    "s_nop 0\n"
    "s_nop 0\n"
    "s_nop 0\n"
    "s_nop 0\n"
    "s_nop 0\n"
    "s_nop 0\n"
    "s_nop 0\n"
    "s_nop 0\n"
    "s_nop 0\n"
    "s_nop 0\n"
    "s_nop 0\n"
    "s_nop 0\n"
    "s_nop 0\n"
    "s_nop 0\n"
    "s_nop 0\n"
    "s_nop 0\n"
    "s_nop 0\n"
    "s_nop 0\n"
    "s_nop 0\n"
    "s_nop 0\n"
    "s_nop 0\n"
    "s_nop 0\n"
    "s_nop 0\n"
    "s_nop 0\n"
    "s_nop 0\n"
    "s_nop 0\n"
    "s_nop 0\n"
    "s_nop 0\n"
    "s_nop 0\n"
    "s_nop 0\n"
    "s_nop 0\n"
    "s_nop 0\n"
    "s_nop 0\n"
    "s_nop 0\n"
    "s_nop 0\n"
    "s_nop 0\n"
    "s_nop 0\n"
    "s_nop 0\n"
    "s_nop 0\n"
    "s_nop 0\n"
    "s_nop 0\n"
    "s_nop 0\n"
    "s_nop 0\n"
    "s_nop 0\n"
    "s_nop 0\n"
    "s_nop 0\n"
    "s_nop 0\n"
    "s_nop 0\n"
    "s_nop 0\n"
    "s_nop 0\n"
    "s_nop 0\n"
    "s_nop 0\n"
    "s_nop 0\n"
    "s_nop 0\n"
    "s_nop 0\n"
    "s_nop 0\n"
    "s_nop 0\n"
    "s_nop 0\n"
    "s_nop 0\n"
    "s_nop 0\n"
    "s_nop 0\n"
    "s_nop 0\n"
    "s_nop 0\n"
    "s_nop 0\n"
    "s_nop 0\n"
    "s_nop 0\n"
    "s_nop 0\n"
    "s_nop 0\n"
    "s_nop 0\n"
    "s_nop 0\n"
    "s_nop 0\n"
    "s_nop 0\n"
    "s_nop 0\n"
    "s_nop 0\n"
    "s_nop 0\n"
    "s_nop 0\n"
    "s_nop 0\n"
    "s_nop 0\n"
    "s_nop 0\n"
    "s_nop 0\n"
    "s_nop 0\n"
    "s_nop 0\n"
    "s_nop 0\n"
    "s_nop 0\n"
    "s_nop 0\n"
    "s_nop 0\n"
    "s_nop 0\n"
    "s_nop 0\n"
    "s_nop 0\n"
    "s_nop 0\n"
    "s_nop 0\n"
    "s_nop 0\n"
    "s_nop 0\n"
    "s_nop 0\n"
    "s_nop 0\n"
    "s_nop 0\n"
    "s_nop 0\n"
    "s_nop 0\n"
    "s_nop 0\n"
    "s_nop 0\n"
    "s_nop 0\n"
    "s_nop 0\n"
    "s_nop 0\n"
    "s_nop 0\n"
    "s_nop 0\n"
    "s_nop 0\n"
    "s_nop 0\n"
    "s_nop 0\n"
    "s_nop 0\n"
    "s_nop 0\n"
    "s_nop 0\n"
    "s_nop 0\n"
    "s_nop 0\n"
    "s_nop 0\n"
    "s_nop 0\n"
    "s_nop 0\n"
    "s_nop 0\n"
    "s_nop 0\n"
    "s_nop 0\n"
    "s_nop 0\n"
    "s_nop 0\n"
    "s_nop 0\n"
    "s_nop 0\n"
    "s_nop 0\n"
    "s_nop 0\n"
    "s_nop 0\n"
    "s_nop 0\n"
    "s_nop 0\n"
    "s_nop 0\n"
    "s_nop 0\n"
    "s_nop 0\n"
    "s_nop 0\n"
    "s_nop 0\n"
    "s_nop 0\n"
    "s_nop 0\n"
    "s_nop 0\n"
    "s_nop 0\n"
    "s_nop 0\n"
    "s_nop 0\n"
    "s_nop 0\n"
    "s_nop 0\n"
    "s_nop 0\n"
    "s_nop 0\n"
    "s_nop 0\n"
    "s_nop 0\n"
    "s_nop 0\n"
    "s_nop 0\n"
    "s_nop 0\n"
    "s_nop 0\n"
    "s_nop 0\n"
    "s_nop 0\n"
    "s_nop 0\n"
    "s_nop 0\n"
    "s_nop 0\n"
    "s_nop 0\n"
    "s_nop 0\n"
    "s_nop 0\n"
    "s_nop 0\n"
    "s_nop 0\n"
    "s_nop 0\n"
    "s_nop 0\n"
    "s_nop 0\n"
    "s_nop 0\n"
    "s_nop 0\n"
    "s_nop 0\n"
    "s_nop 0\n"
    "s_nop 0\n"
    "s_nop 0\n"
    "s_nop 0\n"
    "s_nop 0\n"
    "s_nop 0\n"
    "s_nop 0\n"
    "s_nop 0\n"
    "s_nop 0\n"
    "s_nop 0\n"
    "s_nop 0\n"
    "s_nop 0\n"
    "s_nop 0\n"
    "s_nop 0\n"
    "s_nop 0\n"
    "s_nop 0\n"
    "s_nop 0\n"
    "s_nop 0\n"
    "s_nop 0\n"
    "s_nop 0\n"
    "s_nop 0\n"
    "s_nop 0\n"
    "s_nop 0\n"
    "s_nop 0\n"
    "s_nop 0\n"
    "s_nop 0\n"
    "s_nop 0\n"
    "s_nop 0\n"
    "s_nop 0\n"
    "s_nop 0\n"
    "s_nop 0\n"
    "s_nop 0\n"
    "s_nop 0\n"
    "s_nop 0\n"
    "s_nop 0\n"
    "s_nop 0\n"
    "s_nop 0\n"
    "s_nop 0\n"
    "s_nop 0\n"
    "s_nop 0\n"
    "s_nop 0\n"
    "s_nop 0\n"
    "s_nop 0\n"
    "s_nop 0\n"
    "s_nop 0\n"
    "s_nop 0\n"
    "s_nop 0\n"
    "s_nop 0\n"
    "s_nop 0\n"
    "s_nop 0\n"
    "s_nop 0\n"
    "s_nop 0\n"
    "s_nop 0\n"
    "s_nop 0\n"
    "s_nop 0\n"
    "s_nop 0\n"
    "s_nop 0\n"
    "s_nop 0\n"
    "s_nop 0\n"
    "s_nop 0\n"
    "s_nop 0\n"
    "s_nop 0\n"
    "s_nop 0\n"
    "s_nop 0\n"
    "s_nop 0\n"
    "s_nop 0\n"
    "s_nop 0\n"
    "s_nop 0\n"
    "s_nop 0\n"
    "s_nop 0\n"
    "s_nop 0\n"
    "s_nop 0\n"
    "s_nop 0\n"
    "s_nop 0\n"
    "s_nop 0\n"
    "s_nop 0\n"
    "s_nop 0\n"
    "s_nop 0\n"
    "s_nop 0\n"
    "s_nop 0\n"
    "s_nop 0\n"
    "s_nop 0\n"
    "s_nop 0\n"
    "s_nop 0\n"
    "s_nop 0\n"
    "s_nop 0\n"
    "s_nop 0\n"
    "s_nop 0\n"
    "s_nop 0\n"
    "s_nop 0\n"
    "s_nop 0\n"
    "s_nop 0\n"
    "s_nop 0\n"
    "s_nop 0\n"
    "s_nop 0\n"
    "s_nop 0\n"
    "s_nop 0\n"
    "s_nop 0\n"
    "s_nop 0\n"
    "s_nop 0\n"
    "s_nop 0\n"
    "s_nop 0\n"
    "s_nop 0\n"
    "s_nop 0\n"
    "s_nop 0\n"
    "s_nop 0\n"
    "s_nop 0\n"
    "s_nop 0\n"
    "s_nop 0\n"
    "s_nop 0\n"
    "s_nop 0\n"
    "s_nop 0\n"
    "s_nop 0\n"
    "s_nop 0\n"
    "s_nop 0\n"
    "s_nop 0\n"
    "s_nop 0\n"
    "s_nop 0\n"
    "s_nop 0\n"
    "s_nop 0\n"
    "s_nop 0\n"
    "s_nop 0\n"
    "s_nop 0\n"
    "s_nop 0\n"
    "s_nop 0\n"
    "s_nop 0\n"
    "s_nop 0\n"
    "s_nop 0\n"
    "s_nop 0\n"
    "s_nop 0\n"
    "s_nop 0\n"
    "s_nop 0\n"
    "s_nop 0\n"
    "s_nop 0\n"
    "s_nop 0\n"
    "s_nop 0\n"
    "s_nop 0\n"
    "s_nop 0\n"
    "s_nop 0\n"
    "s_nop 0\n"
    "s_nop 0\n"
    "s_nop 0\n"
    "s_nop 0\n"
    "s_nop 0\n"
    "s_nop 0\n"
    "s_nop 0\n"
    "s_nop 0\n"
    "s_nop 0\n"
    "s_nop 0\n"
    "s_nop 0\n"
    "s_nop 0\n"
    "s_nop 0\n"
    "s_nop 0\n"
    "s_nop 0\n"
    "s_nop 0\n"
    "s_nop 0\n"
    "s_nop 0\n"
    "s_nop 0\n"
    "s_nop 0\n"
    "s_nop 0\n"
    "s_nop 0\n"
    "s_nop 0\n"
    "s_nop 0\n"
    "s_nop 0\n"
    "s_nop 0\n"
    "s_nop 0\n"
    "s_nop 0\n"
    "s_nop 0\n"
    "Lpad0_%=:\n"
    "s_cbranch_execz Lhopd0_%=\n"
    "s_nop 0\n"
    "s_nop 0\n"
    "s_nop 0\n"
    "s_nop 0\n"
    "s_nop 0\n"
    "s_nop 0\n"
    "s_nop 0\n"
    "s_nop 0\n"
    "s_nop 0\n"
    "s_nop 0\n"
    "s_nop 0\n"
    "s_nop 0\n"
    "s_nop 0\n"
    "s_nop 0\n"
    "s_nop 0\n"
    "s_nop 0\n"
    "s_nop 0\n"
    "s_nop 0\n"
    "s_nop 0\n"
    "s_nop 0\n"
    "s_nop 0\n"
    "s_nop 0\n"
    "s_nop 0\n"
    "s_nop 0\n"
    "s_nop 0\n"
    "s_nop 0\n"
    "s_nop 0\n"
    "s_nop 0\n"
    "s_nop 0\n"
    "s_nop 0\n"
    "s_nop 0\n"
    "s_nop 0\n"
    "s_nop 0\n"
    "s_nop 0\n"
    "s_nop 0\n"
    "s_nop 0\n"
    "s_nop 0\n"
    "s_nop 0\n"
    "s_nop 0\n"
    "s_nop 0\n"
    "s_nop 0\n"
    "s_nop 0\n"
    "s_nop 0\n"
    "s_nop 0\n"
    "s_nop 0\n"
    "s_nop 0\n"
    "s_nop 0\n"
    "s_nop 0\n"
    "s_nop 0\n"
    "s_nop 0\n"
    "s_nop 0\n"
    "s_nop 0\n"
    "s_nop 0\n"
    "s_nop 0\n"
    "s_nop 0\n"
    "s_nop 0\n"
    "s_nop 0\n"
    "s_nop 0\n"
    "s_nop 0\n"
    "s_nop 0\n"
    "s_nop 0\n"
    "s_nop 0\n"
    "s_nop 0\n"
    "s_nop 0\n"
    "s_nop 0\n"
    "s_nop 0\n"
    "s_nop 0\n"
    "s_nop 0\n"
    "s_nop 0\n"
    "s_nop 0\n"
    "s_nop 0\n"
    "s_nop 0\n"
    "s_nop 0\n"
    "s_nop 0\n"
    "s_nop 0\n"
    "s_nop 0\n"
    "s_nop 0\n"
    "s_nop 0\n"
    "s_nop 0\n"
    "s_nop 0\n"
    "s_nop 0\n"
    "s_nop 0\n"
    "Lgap_%=:\n"
    "s_add_u32 m0, s54, 98304\n"
    "s_nop 0\n"
    "buffer_load_dwordx4 v210, s[44:47], s56 offen offset:0 nt lds\n"
    "s_add_u32 m0, s54, 99200\n"
    "s_nop 0\n"
    "buffer_load_dwordx4 v210, s[44:47], s56 offen offset:128 nt lds\n"
    "s_add_u32 m0, s54, 100096\n"
    "s_nop 0\n"
    "buffer_load_dwordx4 v210, s[44:47], s56 offen offset:256 nt lds\n"
    "s_add_u32 m0, s54, 100992\n"
    "s_nop 0\n"
    "buffer_load_dwordx4 v210, s[44:47], s56 offen offset:384 nt lds\n"
    "s_add_u32 m0, s54, 114688\n"
    "s_nop 0\n"
    "buffer_load_dwordx4 v210, s[44:47], s70 offen offset:0 nt lds\n"
    "s_add_u32 m0, s54, 115584\n"
    "s_nop 0\n"
    "buffer_load_dwordx4 v210, s[44:47], s70 offen offset:128 nt lds\n"
    "s_add_u32 m0, s54, 116480\n"
    "s_nop 0\n"
    "buffer_load_dwordx4 v210, s[44:47], s70 offen offset:256 nt lds\n"
    "s_add_u32 m0, s54, 117376\n"
    "s_nop 0\n"
    "buffer_load_dwordx4 v210, s[44:47], s70 offen offset:384 nt lds\n"
    "s_mov_b64 exec, 0\n"
    "s_cmp_eq_u32 %[wid], 0\n"
    "s_cbranch_scc1 Lpad4_%=\n"
    "s_cmp_eq_u32 %[wid], 1\n"
    "s_cbranch_scc1 Lpad5_%=\n"
    "s_cmp_eq_u32 %[wid], 2\n"
    "s_cbranch_scc1 Lpad6_%=\n"
    "s_cmp_eq_u32 %[wid], 3\n"
    "s_cbranch_scc1 Lpad7_%=\n"
    "Lhopd1_%=:\n"
    "s_mov_b64 exec, -1\n"
    "s_waitcnt vmcnt(8)\n"
    "s_barrier\n"
    "ds_read_b128 a[224:227], v200 offset:0\n"
    "ds_read_b128 a[228:231], v200 offset:512\n"
    "ds_read_b128 a[232:235], v200 offset:1024\n"
    "ds_read_b128 a[236:239], v200 offset:1536\n"
    "ds_read_b128 a[240:243], v200 offset:2048\n"
    "ds_read_b128 a[244:247], v200 offset:2560\n"
    "s_waitcnt vmcnt(9)\n"
    "ds_read_b128 a[128:131], v212 offset:0\n"
    "ds_read_b128 a[132:135], v212 offset:512\n"
    "ds_read_b128 a[136:139], v212 offset:1024\n"
    "ds_read_b128 a[140:143], v212 offset:1536\n"
    "ds_read_b128 a[144:147], v212 offset:2048\n"
    "ds_read_b128 a[148:151], v212 offset:2560\n"
    "ds_read_b128 a[152:155], v212 offset:3072\n"
    "ds_read_b128 a[156:159], v212 offset:3584\n"
    "s_waitcnt lgkmcnt(0)\n"
    "v_mfma_f32_16x16x32_bf16 v[64:67], a[224:227], a[128:131], 0\n"
    "s_add_u32 m0, s68, 0\n"
    "ds_read_b128 a[248:251], v200 offset:3072\n"
    "ds_read_b128 a[252:255], v200 offset:3584\n"
    "ds_read_b128 a[224:227], v200 offset:8192\n"
    "v_mfma_f32_16x16x32_bf16 v[64:67], a[228:231], a[132:135], v[64:67]\n"
    "s_waitcnt lgkmcnt(13)\n"
    "buffer_load_dwordx4 v210, s[44:47], s24 offen offset:0 nt lds\n"
    "ds_read_b128 a[228:231], v200 offset:8704\n"
    "v_mfma_f32_16x16x32_bf16 v[64:67], a[232:235], a[136:139], v[64:67]\n"
    "ds_read_b128 a[232:235], v200 offset:9216\n"
    "v_mfma_f32_16x16x32_bf16 v[64:67], a[236:239], a[140:143], v[64:67]\n"
    "s_waitcnt lgkmcnt(13)\n"
    "ds_read_b128 a[236:239], v200 offset:9728\n"
    "v_mfma_f32_16x16x32_bf16 v[64:67], a[240:243], a[144:147], v[64:67]\n"
    "s_add_u32 m0, s68, 1024\n"
    "ds_read_b128 a[240:243], v200 offset:10240\n"
    "v_mfma_f32_16x16x32_bf16 v[64:67], a[244:247], a[148:151], v[64:67]\n"
    "s_waitcnt lgkmcnt(5)\n"
    "ds_read_b128 a[244:247], v200 offset:10752\n"
    "v_mfma_f32_16x16x32_bf16 v[64:67], a[248:251], a[152:155], v[64:67]\n"
    "s_nop 0\n"
    "buffer_load_dwordx4 v210, s[44:47], s25 offen offset:0 nt lds\n"
    "ds_read_b128 a[248:251], v200 offset:11264\n"
    "v_mfma_f32_16x16x32_bf16 v[64:67], a[252:255], a[156:159], v[64:67]\n"
    "s_waitcnt lgkmcnt(5)\n"
    "ds_read_b128 a[252:255], v200 offset:11776\n"
    "v_mfma_f32_16x16x32_bf16 v[76:79], a[224:227], a[128:131], 0\n"
    "ds_read_b128 a[224:227], v201 offset:0\n"
    "v_mfma_f32_16x16x32_bf16 v[76:79], a[228:231], a[132:135], v[76:79]\n"
    "s_waitcnt lgkmcnt(5)\n"
    "ds_read_b128 a[228:231], v201 offset:512\n"
    "v_mfma_f32_16x16x32_bf16 v[76:79], a[232:235], a[136:139], v[76:79]\n"
    "s_add_u32 m0, s68, 2048\n"
    "ds_read_b128 a[232:235], v201 offset:1024\n"
    "v_max3_f32 v243, v64, v65, v66\n"
    "v_max_f32 v243, v243, v67\n"
    "v_mfma_f32_16x16x32_bf16 v[76:79], a[236:239], a[140:143], v[76:79]\n"
    "s_waitcnt lgkmcnt(5)\n"
    "buffer_load_dwordx4 v210, s[44:47], s26 offen offset:0 nt lds\n"
    "ds_read_b128 a[236:239], v201 offset:1536\n"
    "v_mov_b32_e64 v249, v243\n"
    "v_mfma_f32_16x16x32_bf16 v[76:79], a[240:243], a[144:147], v[76:79]\n"
    "ds_read_b128 a[240:243], v201 offset:2048\n"
    "v_permlane32_swap_b32 v243, v249\n"
    "v_max_f32 v243, v243, v249\n"
    "v_mfma_f32_16x16x32_bf16 v[76:79], a[244:247], a[148:151], v[76:79]\n"
    "s_waitcnt lgkmcnt(5)\n"
    "ds_read_b128 a[244:247], v201 offset:2560\n"
    "v_mov_b32 v249, v243\n"
    "v_mfma_f32_16x16x32_bf16 v[76:79], a[248:251], a[152:155], v[76:79]\n"
    "s_add_u32 m0, s68, 3072\n"
    "ds_read_b128 a[248:251], v201 offset:3072\n"
    "v_permlane16_swap_b32 v243, v249\n"
    "v_mfma_f32_16x16x32_bf16 v[76:79], a[252:255], a[156:159], v[76:79]\n"
    "s_waitcnt lgkmcnt(5)\n"
    "buffer_load_dwordx4 v210, s[44:47], s27 offen offset:0 nt lds\n"
    "ds_read_b128 a[252:255], v201 offset:3584\n"
    "v_max_f32_e64 v243, v243, v249\n"
    "v_mfma_f32_16x16x32_bf16 v[88:91], a[224:227], a[128:131], 0\n"
    "ds_read_b128 a[224:227], v201 offset:8192\n"
    "v_mul_f32 v232, s64, v243\n"
    "v_fma_f32 v184, v64, s58, v232\n"
    "v_fma_f32 v185, v65, s58, v232\n"
    "v_mfma_f32_16x16x32_bf16 v[88:91], a[228:231], a[132:135], v[88:91]\n"
    "s_waitcnt lgkmcnt(5)\n"
    "ds_read_b128 a[228:231], v201 offset:8704\n"
    "v_fma_f32 v186, v66, s58, v232\n"
    "v_fma_f32 v187, v67, s58, v232\n"
    "v_mfma_f32_16x16x32_bf16 v[88:91], a[232:235], a[136:139], v[88:91]\n"
    "s_add_u32 m0, s54, 32768\n"
    "ds_read_b128 a[232:235], v201 offset:9216\n"
    "v_exp_f32 v184, v184\n"
    "v_fma_f32 v188, v76, s58, v232\n"
    "v_mfma_f32_16x16x32_bf16 v[88:91], a[236:239], a[140:143], v[88:91]\n"
    "s_waitcnt lgkmcnt(5)\n"
    "buffer_load_dwordx4 v210, s[36:39], s52 offen offset:0 lds\n"
    "ds_read_b128 a[236:239], v201 offset:9728\n"
    "v_fma_f32 v189, v77, s58, v232\n"
    "v_mfma_f32_16x16x32_bf16 v[88:91], a[240:243], a[144:147], v[88:91]\n"
    "s_add_u32 m0, s54, 33664\n"
    "ds_read_b128 a[240:243], v201 offset:10240\n"
    "v_exp_f32_e64 v185, v185\n"
    "v_fma_f32 v190, v78, s58, v232\n"
    "v_mfma_f32_16x16x32_bf16 v[88:91], a[244:247], a[148:151], v[88:91]\n"
    "s_waitcnt lgkmcnt(5)\n"
    "ds_read_b128 a[244:247], v201 offset:10752\n"
    "v_exp_f32 v186, v186\n"
    "v_mfma_f32_16x16x32_bf16 v[88:91], a[248:251], a[152:155], v[88:91]\n"
    "buffer_load_dwordx4 v210, s[36:39], s52 offen offset:128 lds\n"
    "ds_read_b128 a[248:251], v201 offset:11264\n"
    "v_fma_f32 v191, v79, s58, v232\n"
    "v_mfma_f32_16x16x32_bf16 v[88:91], a[252:255], a[156:159], v[88:91]\n"
    "s_waitcnt lgkmcnt(5)\n"
    "ds_read_b128 a[252:255], v201 offset:11776\n"
    "v_exp_f32 v187, v187\n"
    "v_add_f32_e64 v238, v184, v185\n"
    "v_mfma_f32_16x16x32_bf16 v[96:99], a[224:227], a[128:131], 0\n"
    "s_add_u32 m0, s54, 34560\n"
    "ds_read_b128 a[224:227], v200 offset:0\n"
    "v_exp_f32 v188, v188\n"
    "v_mfma_f32_16x16x32_bf16 v[96:99], a[228:231], a[132:135], v[96:99]\n"
    "s_waitcnt lgkmcnt(5)\n"
    "buffer_load_dwordx4 v210, s[36:39], s52 offen offset:256 lds\n"
    "ds_read_b128 a[228:231], v200 offset:512\n"
    "v_cvt_pk_bf16_f32 v144, v184, v185\n"
    "v_mfma_f32_16x16x32_bf16 v[96:99], a[232:235], a[136:139], v[96:99]\n"
    "ds_read_b128 a[232:235], v200 offset:1024\n"
    "v_fma_f32 v192, v88, s58, v232\n"
    "v_exp_f32 v189, v189\n"
    "v_mfma_f32_16x16x32_bf16 v[96:99], a[236:239], a[140:143], v[96:99]\n"
    "s_waitcnt lgkmcnt(5)\n"
    "ds_read_b128 a[236:239], v200 offset:1536\n"
    "v_add_f32_e64 v238, v238, v186\n"
    "v_fma_f32 v193, v89, s58, v232\n"
    "v_add_f32_e64 v238, v238, v187\n"
    "v_mfma_f32_16x16x32_bf16 v[96:99], a[240:243], a[144:147], v[96:99]\n"
    "s_add_u32 m0, s54, 35456\n"
    "ds_read_b128 a[240:243], v200 offset:2048\n"
    "v_exp_f32 v190, v190\n"
    "v_mfma_f32_16x16x32_bf16 v[96:99], a[244:247], a[148:151], v[96:99]\n"
    "s_waitcnt lgkmcnt(5)\n"
    "buffer_load_dwordx4 v210, s[36:39], s52 offen offset:384 lds\n"
    "ds_read_b128 a[244:247], v200 offset:2560\n"
    "v_cvt_pk_bf16_f32 v145, v186, v187\n"
    "v_mfma_f32_16x16x32_bf16 v[96:99], a[248:251], a[152:155], v[96:99]\n"
    "s_add_u32 s52, s52, 16384\n"
    "v_fma_f32 v194, v90, s58, v232\n"
    "v_exp_f32 v191, v191\n"
    "v_add_f32 v238, v238, v188\n"
    "v_mfma_f32_16x16x32_bf16 v[96:99], a[252:255], a[156:159], v[96:99]\n"
    "s_add_u32 m0, s54, 49152\n"
    "v_fma_f32 v195, v91, s58, v232\n"
    "v_exp_f32 v192, v192\n"
    "v_add_f32 v238, v238, v189\n"
    "buffer_load_dwordx4 v210, s[36:39], s52 offen offset:0 lds\n"
    "v_cvt_pk_bf16_f32 v146, v188, v189\n"
    "v_exp_f32 v193, v193\n"
    "v_add_f32 v238, v238, v190\n"
    "v_fma_f32 v196, v96, s58, v232\n"
    "v_fma_f32 v197, v97, s58, v232\n"
    "s_add_u32 m0, s54, 50048\n"
    "v_exp_f32 v194, v194\n"
    "v_add_f32 v238, v238, v191\n"
    "v_cvt_pk_bf16_f32 v147, v190, v191\n"
    "v_fma_f32 v198, v98, s58, v232\n"
    "v_exp_f32 v195, v195\n"
    "v_add_f32 v238, v238, v192\n"
    "buffer_load_dwordx4 v210, s[36:39], s52 offen offset:128 lds\n"
    "v_fma_f32 v199, v99, s58, v232\n"
    "v_exp_f32 v196, v196\n"
    "v_add_f32 v238, v238, v193\n"
    "v_cvt_pk_bf16_f32 v156, v192, v193\n"
    "v_add_f32_e64 v238, v238, v194\n"
    "s_add_u32 m0, s54, 50944\n"
    "v_exp_f32 v197, v197\n"
    "v_exp_f32 v198, v198\n"
    "v_add_f32_e64 v238, v238, v195\n"
    "v_cvt_pk_bf16_f32 v157, v194, v195\n"
    "v_exp_f32_e64 v199, v199\n"
    "buffer_load_dwordx4 v210, s[36:39], s52 offen offset:256 lds\n"
    "v_add_f32 v238, v238, v196\n"
    "v_add_f32 v238, v238, v197\n"
    "v_cvt_pk_bf16_f32 v158, v196, v197\n"
    "v_add_f32_e64 v238, v238, v198\n"
    "v_add_f32 v238, v238, v199\n"
    "v_cvt_pk_bf16_f32 v159, v198, v199\n"
    "s_add_u32 m0, s54, 51840\n"
    "s_nop 0\n"
    "buffer_load_dwordx4 v210, s[36:39], s52 offen offset:384 lds\n"
    "s_add_u32 s52, s52, 16384\n"
    "s_add_u32 s44, s44, 319488\n"
    "s_addc_u32 s45, s45, 0\n"
    "s_mov_b64 exec, 0\n"
    "s_cmp_eq_u32 %[wid], 0\n"
    "s_cbranch_scc1 Lpad8_%=\n"
    "s_cmp_eq_u32 %[wid], 1\n"
    "s_cbranch_scc1 Lpad9_%=\n"
    "s_cmp_eq_u32 %[wid], 2\n"
    "s_nop 0\n"
    "s_cmp_eq_u32 %[wid], 3\n"
    "s_nop 0\n"
    "Lhopd2_%=:\n"
    "s_mov_b64 exec, -1\n"
    "s_waitcnt vmcnt(12)\n"
    "ds_read_b128 a[160:163], v212 offset:32768\n"
    "ds_read_b128 a[164:167], v212 offset:33280\n"
    "ds_read_b128 a[168:171], v212 offset:33792\n"
    "ds_read_b128 a[172:175], v212 offset:34304\n"
    "ds_read_b128 a[176:179], v212 offset:34816\n"
    "ds_read_b128 a[180:183], v212 offset:35328\n"
    "ds_read_b128 a[184:187], v212 offset:35840\n"
    "ds_read_b128 a[188:191], v212 offset:36352\n"
    "v_subrev_u32 v212, 0x10000, v212\n"
    "s_waitcnt lgkmcnt(0)\n"
    "v_mfma_f32_16x16x32_bf16 v[68:71], a[224:227], a[160:163], 0\n"
    "s_add_u32 m0, s54, 65536\n"
    "ds_read_b128 a[248:251], v200 offset:3072\n"
    "ds_read_b128 a[252:255], v200 offset:3584\n"
    "v_mfma_f32_16x16x32_bf16 v[68:71], a[228:231], a[164:167], v[68:71]\n"
    "s_waitcnt lgkmcnt(12)\n"
    "ds_read_b128 a[224:227], v200 offset:8192\n"
    "ds_read_b128 a[228:231], v200 offset:8704\n"
    "v_mfma_f32_16x16x32_bf16 v[68:71], a[232:235], a[168:171], v[68:71]\n"
    "s_nop 0\n"
    "buffer_load_dwordx4 v211, s[40:43], s53 offen offset:0 lds\n"
    "v_mfma_f32_16x16x32_bf16 v[68:71], a[236:239], a[172:175], v[68:71]\n"
    "s_waitcnt lgkmcnt(12)\n"
    "ds_read_b128 a[232:235], v200 offset:9216\n"
    "ds_read_b128 a[236:239], v200 offset:9728\n"
    "v_mfma_f32_16x16x32_bf16 v[68:71], a[240:243], a[176:179], v[68:71]\n"
    "s_nop 0\n"
    "ds_read_b128 a[240:243], v200 offset:10240\n"
    "v_mfma_f32_16x16x32_bf16 v[68:71], a[244:247], a[180:183], v[68:71]\n"
    "s_waitcnt lgkmcnt(5)\n"
    "ds_read_b128 a[244:247], v200 offset:10752\n"
    "v_mfma_f32_16x16x32_bf16 v[68:71], a[248:251], a[184:187], v[68:71]\n"
    "s_add_u32 m0, s54, 66432\n"
    "ds_read_b128 a[248:251], v200 offset:11264\n"
    "v_mfma_f32_16x16x32_bf16 v[68:71], a[252:255], a[188:191], v[68:71]\n"
    "s_waitcnt lgkmcnt(5)\n"
    "buffer_load_dwordx4 v211, s[40:43], s53 offen offset:128 lds\n"
    "v_mfma_f32_16x16x32_bf16 v[80:83], a[224:227], a[160:163], 0\n"
    "ds_read_b128 a[252:255], v200 offset:11776\n"
    "ds_read_b128 a[224:227], v201 offset:0\n"
    "v_mfma_f32_16x16x32_bf16 v[80:83], a[228:231], a[164:167], v[80:83]\n"
    "s_waitcnt lgkmcnt(5)\n"
    "ds_read_b128 a[228:231], v201 offset:512\n"
    "v_max3_f32 v245, v68, v69, v70\n"
    "v_mfma_f32_16x16x32_bf16 v[80:83], a[232:235], a[168:171], v[80:83]\n"
    "ds_read_b128 a[232:235], v201 offset:1024\n"
    "v_max_f32 v245, v245, v71\n"
    "v_mfma_f32_16x16x32_bf16 v[80:83], a[236:239], a[172:175], v[80:83]\n"
    "s_waitcnt lgkmcnt(5)\n"
    "ds_read_b128 a[236:239], v201 offset:1536\n"
    "v_mov_b32_e64 v252, v245\n"
    "v_mfma_f32_16x16x32_bf16 v[80:83], a[240:243], a[176:179], v[80:83]\n"
    "s_add_u32 m0, s54, 67328\n"
    "ds_read_b128 a[240:243], v201 offset:2048\n"
    "v_mfma_f32_16x16x32_bf16 v[80:83], a[244:247], a[180:183], v[80:83]\n"
    "s_waitcnt lgkmcnt(5)\n"
    "buffer_load_dwordx4 v211, s[40:43], s53 offen offset:256 lds\n"
    "v_mfma_f32_16x16x32_bf16 v[80:83], a[248:251], a[184:187], v[80:83]\n"
    "s_nop 0\n"
    "ds_read_b128 a[244:247], v201 offset:2560\n"
    "ds_read_b128 a[248:251], v201 offset:3072\n"
    "v_mfma_f32_16x16x32_bf16 v[80:83], a[252:255], a[188:191], v[80:83]\n"
    "s_waitcnt lgkmcnt(5)\n"
    "ds_read_b128 a[252:255], v201 offset:3584\n"
    "v_mfma_f32_16x16x32_bf16 v[92:95], a[224:227], a[160:163], 0\n"
    "s_nop 0\n"
    "ds_read_b128 a[224:227], v201 offset:8192\n"
    "v_mfma_f32_16x16x32_bf16 v[92:95], a[228:231], a[164:167], v[92:95]\n"
    "s_waitcnt lgkmcnt(5)\n"
    "ds_read_b128 a[228:231], v201 offset:8704\n"
    "v_mfma_f32_16x16x32_bf16 v[92:95], a[232:235], a[168:171], v[92:95]\n"
    "s_add_u32 m0, s54, 68224\n"
    "ds_read_b128 a[232:235], v201 offset:9216\n"
    "v_mfma_f32_16x16x32_bf16 v[92:95], a[236:239], a[172:175], v[92:95]\n"
    "s_waitcnt lgkmcnt(5)\n"
    "buffer_load_dwordx4 v211, s[40:43], s53 offen offset:384 lds\n"
    "v_mfma_f32_16x16x32_bf16 v[92:95], a[240:243], a[176:179], v[92:95]\n"
    "s_add_u32 s53, s53, 16384\n"
    "ds_read_b128 a[236:239], v201 offset:9728\n"
    "ds_read_b128 a[240:243], v201 offset:10240\n"
    "v_mfma_f32_16x16x32_bf16 v[92:95], a[244:247], a[180:183], v[92:95]\n"
    "s_waitcnt lgkmcnt(5)\n"
    "ds_read_b128 a[244:247], v201 offset:10752\n"
    "v_mfma_f32_16x16x32_bf16 v[92:95], a[248:251], a[184:187], v[92:95]\n"
    "ds_read_b128 a[248:251], v201 offset:11264\n"
    "v_mfma_f32_16x16x32_bf16 v[92:95], a[252:255], a[188:191], v[92:95]\n"
    "s_waitcnt lgkmcnt(5)\n"
    "ds_read_b128 a[252:255], v201 offset:11776\n"
    "v_mfma_f32_16x16x32_bf16 v[100:103], a[224:227], a[160:163], 0\n"
    "s_add_u32 m0, s54, 81920\n"
    "ds_read_b128 a[224:227], v200 offset:0\n"
    "v_mfma_f32_16x16x32_bf16 v[100:103], a[228:231], a[164:167], v[100:103]\n"
    "s_waitcnt lgkmcnt(5)\n"
    "buffer_load_dwordx4 v211, s[40:43], s53 offen offset:0 lds\n"
    "v_mfma_f32_16x16x32_bf16 v[100:103], a[232:235], a[168:171], v[100:103]\n"
    "s_nop 0\n"
    "ds_read_b128 a[228:231], v200 offset:512\n"
    "ds_read_b128 a[232:235], v200 offset:1024\n"
    "v_mfma_f32_16x16x32_bf16 v[100:103], a[236:239], a[172:175], v[100:103]\n"
    "s_waitcnt lgkmcnt(5)\n"
    "ds_read_b128 a[236:239], v200 offset:1536\n"
    "v_mfma_f32_16x16x32_bf16 v[100:103], a[240:243], a[176:179], v[100:103]\n"
    "s_nop 0\n"
    "ds_read_b128 a[240:243], v200 offset:2048\n"
    "v_mfma_f32_16x16x32_bf16 v[100:103], a[244:247], a[180:183], v[100:103]\n"
    "s_waitcnt lgkmcnt(5)\n"
    "ds_read_b128 a[244:247], v200 offset:2560\n"
    "v_mfma_f32_16x16x32_bf16 v[100:103], a[248:251], a[184:187], v[100:103]\n"
    "s_add_u32 m0, s54, 82816\n"
    "v_permlane32_swap_b32 v245, v252\n"
    "v_mfma_f32_16x16x32_bf16 v[100:103], a[252:255], a[188:191], v[100:103]\n"
    "buffer_load_dwordx4 v211, s[40:43], s53 offen offset:128 lds\n"
    "v_max_f32 v245, v245, v252\n"
    "v_mov_b32 v252, v245\n"
    "s_nop 1\n"
    "v_permlane16_swap_b32 v245, v252\n"
    "v_max_f32 v245, v245, v252\n"
    "v_mul_f32 v233, s64, v245\n"
    "s_add_u32 m0, s54, 83712\n"
    "s_nop 0\n"
    "buffer_load_dwordx4 v211, s[40:43], s53 offen offset:256 lds\n"
    "s_add_u32 m0, s54, 84608\n"
    "s_nop 0\n"
    "buffer_load_dwordx4 v211, s[40:43], s53 offen offset:384 lds\n"
    "s_add_u32 s53, s53, 16384\n"
    "v_xor_b32 v253, 0x2020, v213\n"
    "s_waitcnt vmcnt(16)\n"
    "s_barrier\n"
    "ds_read_b128 a[192:195], v213 offset:0\n"
    "ds_read_b128 a[196:199], v213 offset:512\n"
    "ds_read_b128 a[200:203], v213 offset:1024\n"
    "ds_read_b128 a[204:207], v213 offset:1536\n"
    "ds_read_b128 a[208:211], v253 offset:0\n"
    "ds_read_b128 a[212:215], v253 offset:512\n"
    "ds_read_b128 a[216:219], v253 offset:1024\n"
    "ds_read_b128 a[220:223], v253 offset:1536\n"
    "s_waitcnt lgkmcnt(0)\n"
    "s_waitcnt lgkmcnt(12)\n"
    "v_mfma_f32_16x16x32_bf16 v[72:75], a[224:227], a[192:195], 0\n"
    "ds_read_b128 a[248:251], v200 offset:3072\n"
    "ds_read_b128 a[252:255], v200 offset:3584\n"
    "v_mfma_f32_16x16x32_bf16 v[72:75], a[228:231], a[196:199], v[72:75]\n"
    "s_waitcnt lgkmcnt(12)\n"
    "ds_read_b128 a[224:227], v200 offset:8192\n"
    "ds_read_b128 a[228:231], v200 offset:8704\n"
    "v_mfma_f32_16x16x32_bf16 v[72:75], a[232:235], a[200:203], v[72:75]\n"
    "ds_read_b128 a[232:235], v200 offset:9216\n"
    "v_fma_f32 v184, v68, s58, v233\n"
    "v_mfma_f32_16x16x32_bf16 v[72:75], a[236:239], a[204:207], v[72:75]\n"
    "s_waitcnt lgkmcnt(13)\n"
    "ds_read_b128 a[236:239], v200 offset:9728\n"
    "v_fma_f32 v185, v69, s58, v233\n"
    "v_mfma_f32_16x16x32_bf16 v[72:75], a[240:243], a[208:211], v[72:75]\n"
    "ds_read_b128 a[240:243], v200 offset:10240\n"
    "v_fma_f32 v186, v70, s58, v233\n"
    "v_mfma_f32_16x16x32_bf16 v[72:75], a[244:247], a[212:215], v[72:75]\n"
    "s_waitcnt lgkmcnt(5)\n"
    "ds_read_b128 a[244:247], v200 offset:10752\n"
    "v_fma_f32 v187, v71, s58, v233\n"
    "v_mfma_f32_16x16x32_bf16 v[72:75], a[248:251], a[216:219], v[72:75]\n"
    "ds_read_b128 a[248:251], v200 offset:11264\n"
    "v_fma_f32 v188, v80, s58, v233\n"
    "v_mfma_f32_16x16x32_bf16 v[72:75], a[252:255], a[220:223], v[72:75]\n"
    "s_waitcnt lgkmcnt(5)\n"
    "ds_read_b128 a[252:255], v200 offset:11776\n"
    "v_fma_f32 v189, v81, s58, v233\n"
    "v_mfma_f32_16x16x32_bf16 v[84:87], a[224:227], a[192:195], 0\n"
    "v_exp_f32_e64 v184, v184\n"
    "v_mfma_f32_16x16x32_bf16 v[84:87], a[228:231], a[196:199], v[84:87]\n"
    "s_waitcnt lgkmcnt(4)\n"
    "v_exp_f32 v185, v185\n"
    "v_mfma_f32_16x16x32_bf16 v[84:87], a[232:235], a[200:203], v[84:87]\n"
    "v_exp_f32 v186, v186\n"
    "v_mfma_f32_16x16x32_bf16 v[84:87], a[236:239], a[204:207], v[84:87]\n"
    "s_waitcnt lgkmcnt(2)\n"
    "v_fma_f32 v190, v82, s58, v233\n"
    "v_fma_f32 v191, v83, s58, v233\n"
    "v_mfma_f32_16x16x32_bf16 v[84:87], a[240:243], a[208:211], v[84:87]\n"
    "s_waitcnt vmcnt(8) lgkmcnt(0)\n"
    "v_exp_f32_e64 v187, v187\n"
    "v_mfma_f32_16x16x32_bf16 v[84:87], a[244:247], a[212:215], v[84:87]\n"
    "s_barrier\n"
    "ds_read_b128 a[224:227], v200 offset:32768\n"
    "ds_read_b128 a[228:231], v200 offset:33280\n"
    "v_mfma_f32_16x16x32_bf16 v[84:87], a[248:251], a[216:219], v[84:87]\n"
    "s_add_u32 m0, s54, 0\n"
    "ds_read_b128 a[232:235], v200 offset:33792\n"
    "ds_read_b128 a[236:239], v200 offset:34304\n"
    "v_mfma_f32_16x16x32_bf16 v[84:87], a[252:255], a[220:223], v[84:87]\n"
    "s_waitcnt lgkmcnt(2)\n"
    "buffer_load_dwordx4 v210, s[36:39], s52 offen offset:0 lds\n"
    "v_mfma_f32_16x16x32_bf16 v[104:107], a[224:227], a[128:131], 0\n"
    "s_add_u32 m0, s54, 896\n"
    "v_exp_f32_e64 v188, v188\n"
    "v_mfma_f32_16x16x32_bf16 v[104:107], a[228:231], a[132:135], v[104:107]\n"
    "ds_read_b128 a[240:243], v200 offset:34816\n"
    "ds_read_b128 a[244:247], v200 offset:35328\n"
    "v_mfma_f32_16x16x32_bf16 v[108:111], a[228:231], a[164:167], 0\n"
    "ds_read_b128 a[248:251], v200 offset:35840\n"
    "v_add_f32_e64 v239, v184, v185\n"
    "v_mfma_f32_16x16x32_bf16 v[108:111], a[224:227], a[160:163], v[108:111]\n"
    "buffer_load_dwordx4 v210, s[36:39], s52 offen offset:128 lds\n"
    "v_mfma_f32_16x16x32_bf16 v[112:115], a[224:227], a[192:195], 0\n"
    "s_add_u32 m0, s54, 1792\n"
    "ds_read_b128 a[252:255], v200 offset:36352\n"
    "v_cvt_pk_bf16_f32 v148, v184, v185\n"
    "v_mfma_f32_16x16x32_bf16 v[112:115], a[228:231], a[196:199], v[112:115]\n"
    "s_waitcnt lgkmcnt(4)\n"
    "ds_read_b128 a[224:227], v200 offset:40960\n"
    "v_fma_f32 v192, v92, s58, v233\n"
    "v_mfma_f32_16x16x32_bf16 v[112:115], a[232:235], a[200:203], v[112:115]\n"
    "v_exp_f32 v189, v189\n"
    "v_mfma_f32_16x16x32_bf16 v[112:115], a[236:239], a[204:207], v[112:115]\n"
    "ds_read_b128 a[228:231], v200 offset:41472\n"
    "v_add_f32_e64 v239, v239, v186\n"
    "v_mfma_f32_16x16x32_bf16 v[108:111], a[236:239], a[172:175], v[108:111]\n"
    "buffer_load_dwordx4 v210, s[36:39], s52 offen offset:256 lds\n"
    "v_mfma_f32_16x16x32_bf16 v[108:111], a[232:235], a[168:171], v[108:111]\n"
    "s_add_u32 m0, s54, 2688\n"
    "v_fma_f32 v193, v93, s58, v233\n"
    "v_add_f32_e64 v239, v239, v187\n"
    "v_mfma_f32_16x16x32_bf16 v[104:107], a[232:235], a[136:139], v[104:107]\n"
    "v_exp_f32 v190, v190\n"
    "v_mfma_f32_16x16x32_bf16 v[104:107], a[236:239], a[140:143], v[104:107]\n"
    "s_waitcnt lgkmcnt(4)\n"
    "ds_read_b128 a[232:235], v200 offset:41984\n"
    "v_cvt_pk_bf16_f32 v149, v186, v187\n"
    "v_mfma_f32_16x16x32_bf16 v[104:107], a[240:243], a[144:147], v[104:107]\n"
    "buffer_load_dwordx4 v210, s[36:39], s52 offen offset:384 lds\n"
    "v_mfma_f32_16x16x32_bf16 v[104:107], a[244:247], a[148:151], v[104:107]\n"
    "s_add_u32 s52, s52, 16384\n"
    "ds_read_b128 a[236:239], v200 offset:42496\n"
    "v_fma_f32 v194, v94, s58, v233\n"
    "v_mfma_f32_16x16x32_bf16 v[108:111], a[244:247], a[180:183], v[108:111]\n"
    "s_add_u32 m0, s54, 16384\n"
    "v_exp_f32_e64 v191, v191\n"
    "v_mfma_f32_16x16x32_bf16 v[108:111], a[240:243], a[176:179], v[108:111]\n"
    "v_add_f32_e64 v239, v239, v188\n"
    "v_fma_f32 v195, v95, s58, v233\n"
    "v_mfma_f32_16x16x32_bf16 v[112:115], a[240:243], a[208:211], v[112:115]\n"
    "v_exp_f32 v192, v192\n"
    "v_mfma_f32_16x16x32_bf16 v[112:115], a[244:247], a[212:215], v[112:115]\n"
    "s_waitcnt lgkmcnt(4)\n"
    "buffer_load_dwordx4 v210, s[36:39], s52 offen offset:0 lds\n"
    "v_mfma_f32_16x16x32_bf16 v[112:115], a[248:251], a[216:219], v[112:115]\n"
    "s_add_u32 m0, s54, 17280\n"
    "ds_read_b128 a[240:243], v200 offset:43008\n"
    "v_add_f32_e64 v239, v239, v189\n"
    "v_mfma_f32_16x16x32_bf16 v[112:115], a[252:255], a[220:223], v[112:115]\n"
    "ds_read_b128 a[244:247], v200 offset:43520\n"
    "v_cvt_pk_bf16_f32 v150, v188, v189\n"
    "v_mfma_f32_16x16x32_bf16 v[108:111], a[252:255], a[188:191], v[108:111]\n"
    "v_fma_f32 v196, v100, s58, v233\n"
    "v_max3_f32 v247, v72, v73, v74\n"
    "v_mfma_f32_16x16x32_bf16 v[108:111], a[248:251], a[184:187], v[108:111]\n"
    "buffer_load_dwordx4 v210, s[36:39], s52 offen offset:128 lds\n"
    "v_mfma_f32_16x16x32_bf16 v[104:107], a[248:251], a[152:155], v[104:107]\n"
    "ds_read_b128 a[248:251], v200 offset:44032\n"
    "v_max_f32 v247, v247, v75\n"
    "v_mfma_f32_16x16x32_bf16 v[104:107], a[252:255], a[156:159], v[104:107]\n"
    "s_waitcnt lgkmcnt(5)\n"
    "ds_read_b128 a[252:255], v200 offset:44544\n"
    "v_mov_b32_e64 v253, v247\n"
    "v_mfma_f32_16x16x32_bf16 v[116:119], a[224:227], a[128:131], 0\n"
    "v_exp_f32_e64 v193, v193\n"
    "v_mfma_f32_16x16x32_bf16 v[116:119], a[228:231], a[132:135], v[116:119]\n"
    "s_add_u32 m0, s54, 18176\n"
    "v_permlane32_swap_b32 v247, v253\n"
    "v_mfma_f32_16x16x32_bf16 v[120:123], a[228:231], a[164:167], 0\n"
    "s_nop 0\n"
    "buffer_load_dwordx4 v210, s[36:39], s52 offen offset:256 lds\n"
    "v_mfma_f32_16x16x32_bf16 v[120:123], a[224:227], a[160:163], v[120:123]\n"
    "v_max_f32 v247, v247, v253\n"
    "v_mov_b32 v253, v247\n"
    "v_mfma_f32_16x16x32_bf16 v[124:127], a[224:227], a[192:195], 0\n"
    "ds_read_b128 a[224:227], v201 offset:32768\n"
    "v_add_f32_e64 v239, v239, v190\n"
    "v_mfma_f32_16x16x32_bf16 v[124:127], a[228:231], a[196:199], v[124:127]\n"
    "s_waitcnt lgkmcnt(5)\n"
    "v_permlane16_swap_b32 v247, v253\n"
    "v_mfma_f32_16x16x32_bf16 v[124:127], a[232:235], a[200:203], v[124:127]\n"
    "ds_read_b128 a[228:231], v201 offset:33280\n"
    "v_max_f32_e64 v247, v247, v253\n"
    "v_mfma_f32_16x16x32_bf16 v[124:127], a[236:239], a[204:207], v[124:127]\n"
    "v_mul_f32_e64 v234, s64, v247\n"
    "v_fma_f32 v197, v101, s58, v233\n"
    "v_mfma_f32_16x16x32_bf16 v[120:123], a[236:239], a[172:175], v[120:123]\n"
    "s_add_u32 m0, s54, 19072\n"
    "v_exp_f32_e64 v194, v194\n"
    "v_mfma_f32_16x16x32_bf16 v[120:123], a[232:235], a[168:171], v[120:123]\n"
    "buffer_load_dwordx4 v210, s[36:39], s52 offen offset:384 lds\n"
    "v_mfma_f32_16x16x32_bf16 v[116:119], a[232:235], a[136:139], v[116:119]\n"
    "s_add_u32 s52, s52, 16384\n"
    "ds_read_b128 a[232:235], v201 offset:33792\n"
    "v_add_f32 v239, v239, v191\n"
    "v_mfma_f32_16x16x32_bf16 v[116:119], a[236:239], a[140:143], v[116:119]\n"
    "s_waitcnt lgkmcnt(5)\n"
    "ds_read_b128 a[236:239], v201 offset:34304\n"
    "v_cvt_pk_bf16_f32 v151, v190, v191\n"
    "v_mfma_f32_16x16x32_bf16 v[116:119], a[240:243], a[144:147], v[116:119]\n"
    "v_fma_f32 v198, v102, s58, v233\n"
    "v_add_f32_e64 v239, v239, v192\n"
    "v_mfma_f32_16x16x32_bf16 v[116:119], a[244:247], a[148:151], v[116:119]\n"
    "v_exp_f32_e64 v195, v195\n"
    "v_mfma_f32_16x16x32_bf16 v[120:123], a[244:247], a[180:183], v[120:123]\n"
    "v_fma_f32 v199, v103, s58, v233\n"
    "v_add_f32_e64 v239, v239, v193\n"
    "v_mfma_f32_16x16x32_bf16 v[120:123], a[240:243], a[176:179], v[120:123]\n"
    "Lpad1_%=:\n"
    "s_cbranch_execz Lhopd0_%=\n"
    "v_exp_f32 v196, v196\n"
    "v_mfma_f32_16x16x32_bf16 v[124:127], a[240:243], a[208:211], v[124:127]\n"
    "s_nop 0\n"
    "ds_read_b128 a[240:243], v201 offset:34816\n"
    "v_cvt_pk_bf16_f32 v160, v192, v193\n"
    "v_mfma_f32_16x16x32_bf16 v[124:127], a[244:247], a[212:215], v[124:127]\n"
    "s_waitcnt lgkmcnt(5)\n"
    "ds_read_b128 a[244:247], v201 offset:35328\n"
    "v_fma_f32 v184, v72, s58, v234\n"
    "v_mfma_f32_16x16x32_bf16 v[124:127], a[248:251], a[216:219], v[124:127]\n"
    "s_add_u32 m0, s54, 98304\n"
    "v_exp_f32_e64 v197, v197\n"
    "v_mfma_f32_16x16x32_bf16 v[124:127], a[252:255], a[220:223], v[124:127]\n"
    "buffer_load_dwordx4 v211, s[40:43], s53 offen offset:0 lds\n"
    "v_mfma_f32_16x16x32_bf16 v[120:123], a[252:255], a[188:191], v[120:123]\n"
    "v_add_f32_e64 v239, v239, v194\n"
    "v_fma_f32 v185, v73, s58, v234\n"
    "v_mfma_f32_16x16x32_bf16 v[120:123], a[248:251], a[184:187], v[120:123]\n"
    "v_exp_f32_e64 v198, v198\n"
    "v_mfma_f32_16x16x32_bf16 v[116:119], a[248:251], a[152:155], v[116:119]\n"
    "ds_read_b128 a[248:251], v201 offset:35840\n"
    "v_add_f32 v239, v239, v195\n"
    "v_mfma_f32_16x16x32_bf16 v[116:119], a[252:255], a[156:159], v[116:119]\n"
    "s_waitcnt lgkmcnt(5)\n"
    "ds_read_b128 a[252:255], v201 offset:36352\n"
    "v_cvt_pk_bf16_f32 v161, v194, v195\n"
    "v_mfma_f32_16x16x32_bf16 v[128:131], a[224:227], a[128:131], 0\n"
    "s_add_u32 m0, s54, 99200\n"
    "v_fma_f32 v186, v74, s58, v234\n"
    "v_add_f32_e64 v239, v239, v196\n"
    "v_mfma_f32_16x16x32_bf16 v[128:131], a[228:231], a[132:135], v[128:131]\n"
    "buffer_load_dwordx4 v211, s[40:43], s53 offen offset:128 lds\n"
    "v_mfma_f32_16x16x32_bf16 v[132:135], a[228:231], a[164:167], 0\n"
    "v_exp_f32 v199, v199\n"
    "v_mfma_f32_16x16x32_bf16 v[132:135], a[224:227], a[160:163], v[132:135]\n"
    "s_waitcnt lgkmcnt(4)\n"
    "ds_read_b128 a[224:227], v201 offset:40960\n"
    "ds_read_b128 a[228:231], v201 offset:41472\n"
    "v_mfma_f32_16x16x32_bf16 v[132:135], a[232:235], a[168:171], v[132:135]\n"
    "v_fma_f32 v187, v75, s58, v234\n"
    "v_add_f32_e64 v239, v239, v197\n"
    "v_mfma_f32_16x16x32_bf16 v[132:135], a[236:239], a[172:175], v[132:135]\n"
    "v_exp_f32_e64 v184, v184\n"
    "v_mfma_f32_16x16x32_bf16 v[128:131], a[236:239], a[140:143], v[128:131]\n"
    "s_add_u32 m0, s54, 100096\n"
    "v_cvt_pk_bf16_f32 v162, v196, v197\n"
    "v_fma_f32 v188, v84, s58, v234\n"
    "v_mfma_f32_16x16x32_bf16 v[128:131], a[232:235], a[136:139], v[128:131]\n"
    "s_waitcnt lgkmcnt(4)\n"
    "buffer_load_dwordx4 v211, s[40:43], s53 offen offset:256 lds\n"
    "v_mfma_f32_16x16x32_bf16 v[128:131], a[240:243], a[144:147], v[128:131]\n"
    "v_exp_f32 v185, v185\n"
    "v_mfma_f32_16x16x32_bf16 v[128:131], a[244:247], a[148:151], v[128:131]\n"
    "ds_read_b128 a[232:235], v201 offset:41984\n"
    "ds_read_b128 a[236:239], v201 offset:42496\n"
    "v_mfma_f32_16x16x32_bf16 v[132:135], a[244:247], a[180:183], v[132:135]\n"
    "v_add_f32_e64 v239, v239, v198\n"
    "v_fma_f32 v189, v85, s58, v234\n"
    "v_mfma_f32_16x16x32_bf16 v[132:135], a[240:243], a[176:179], v[132:135]\n"
    "s_waitcnt lgkmcnt(4)\n"
    "v_exp_f32 v186, v186\n"
    "v_mfma_f32_16x16x32_bf16 v[132:135], a[248:251], a[184:187], v[132:135]\n"
    "s_add_u32 m0, s54, 100992\n"
    "ds_read_b128 a[240:243], v201 offset:43008\n"
    "ds_read_b128 a[244:247], v201 offset:43520\n"
    "v_mfma_f32_16x16x32_bf16 v[132:135], a[252:255], a[188:191], v[132:135]\n"
    "buffer_load_dwordx4 v211, s[40:43], s53 offen offset:384 lds\n"
    "v_mfma_f32_16x16x32_bf16 v[128:131], a[252:255], a[156:159], v[128:131]\n"
    "s_add_u32 s53, s53, 16384\n"
    "v_add_f32 v239, v239, v199\n"
    "v_cvt_pk_bf16_f32 v163, v198, v199\n"
    "v_mfma_f32_16x16x32_bf16 v[128:131], a[248:251], a[152:155], v[128:131]\n"
    "s_waitcnt lgkmcnt(4)\n"
    "ds_read_b128 a[248:251], v201 offset:44032\n"
    "ds_read_b128 a[252:255], v201 offset:44544\n"
    "v_mfma_f32_16x16x32_bf16 v[136:139], a[224:227], a[128:131], 0\n"
    "v_fma_f32 v190, v86, s58, v234\n"
    "v_fma_f32 v191, v87, s58, v234\n"
    "v_mfma_f32_16x16x32_bf16 v[136:139], a[228:231], a[132:135], v[136:139]\n"
    "v_exp_f32_e64 v187, v187\n"
    "v_mfma_f32_16x16x32_bf16 v[140:143], a[228:231], a[164:167], 0\n"
    "v_exp_f32_e64 v188, v188\n"
    "v_mfma_f32_16x16x32_bf16 v[140:143], a[224:227], a[160:163], v[140:143]\n"
    "s_waitcnt lgkmcnt(4)\n"
    "v_add_f32 v240, v184, v185\n"
    "v_cvt_pk_bf16_f32 v152, v184, v185\n"
    "v_mfma_f32_16x16x32_bf16 v[140:143], a[232:235], a[168:171], v[140:143]\n"
    "v_exp_f32_e64 v189, v189\n"
    "v_mfma_f32_16x16x32_bf16 v[140:143], a[236:239], a[172:175], v[140:143]\n"
    "s_waitcnt vmcnt(12) lgkmcnt(0)\n"
    "v_add_f32 v240, v240, v186\n"
    "v_add_f32 v240, v240, v187\n"
    "v_mfma_f32_16x16x32_bf16 v[136:139], a[236:239], a[140:143], v[136:139]\n"
    "s_barrier\n"
    "ds_read_b64_tr_b16 a[224:225], v202 offset:0\n"
    "ds_read_b64_tr_b16 a[226:227], v202 offset:8192\n"
    "v_mfma_f32_16x16x32_bf16 v[136:139], a[232:235], a[136:139], v[136:139]\n"
    "ds_read_b64_tr_b16 a[228:229], v206 offset:0\n"
    "ds_read_b64_tr_b16 a[230:231], v206 offset:8192\n"
    "v_mfma_f32_16x16x32_bf16 v[136:139], a[240:243], a[144:147], v[136:139]\n"
    "v_exp_f32_e64 v190, v190\n"
    "v_mfma_f32_16x16x32_bf16 v[136:139], a[244:247], a[148:151], v[136:139]\n"
    "ds_read_b64_tr_b16 a[232:233], v204 offset:0\n"
    "ds_read_b64_tr_b16 a[234:235], v204 offset:8192\n"
    "v_mfma_f32_16x16x32_bf16 v[140:143], a[244:247], a[180:183], v[140:143]\n"
    "ds_read_b64_tr_b16 a[236:237], v208 offset:0\n"
    "ds_read_b64_tr_b16 a[238:239], v208 offset:8192\n"
    "v_mfma_f32_16x16x32_bf16 v[140:143], a[240:243], a[176:179], v[140:143]\n"
    "ds_read_b64_tr_b16 a[240:241], v202 offset:512\n"
    "v_cvt_pk_bf16_f32 v153, v186, v187\n"
    "v_mfma_f32_16x16x32_bf16 v[140:143], a[248:251], a[184:187], v[140:143]\n"
    "v_exp_f32_e64 v191, v191\n"
    "v_mfma_f32_16x16x32_bf16 v[140:143], a[252:255], a[188:191], v[140:143]\n"
    "ds_read_b64_tr_b16 a[242:243], v202 offset:8704\n"
    "ds_read_b64_tr_b16 a[244:245], v206 offset:512\n"
    "v_mfma_f32_16x16x32_bf16 v[136:139], a[252:255], a[156:159], v[136:139]\n"
    "ds_read_b64_tr_b16 a[246:247], v206 offset:8704\n"
    "v_add_f32_e64 v240, v240, v188\n"
    "v_mfma_f32_16x16x32_bf16 v[136:139], a[248:251], a[152:155], v[136:139]\n"
    "v_add_f32_e64 v240, v240, v189\n"
    "v_cvt_pk_bf16_f32 v154, v188, v189\n"
    "v_add_f32_e64 v240, v240, v190\n"
    "v_add_f32 v240, v240, v191\n"
    "v_cvt_pk_bf16_f32 v155, v190, v191\n"
    "v_max3_f32 v243, v238, v239, v240\n"
    "v_cmp_lt_f32 vcc, s59, v243\n"
    "s_cbranch_vccnz LrareA_%=\n"
    "LbackA_%=:\n"
    "s_waitcnt lgkmcnt(8)\n"
    "v_mfma_f32_16x16x32_bf16 a[0:3], a[224:227], v[144:147], 0\n"
    "s_add_u32 m0, s54, 114688\n"
    "ds_read_b64_tr_b16 a[248:249], v204 offset:512\n"
    "ds_read_b128 v[228:231], v226\n"
    "v_mfma_f32_16x16x32_bf16 a[0:3], a[228:231], v[156:159], a[0:3]\n"
    "buffer_load_dwordx4 v211, s[40:43], s53 offen offset:0 lds\n"
    "v_mfma_f32_16x16x32_bf16 a[64:67], a[228:231], v[160:163], 0\n"
    "ds_read_b64_tr_b16 a[250:251], v204 offset:8704\n"
    "v_mov_b32_e64 v236, v238\n"
    "v_mfma_f32_16x16x32_bf16 a[64:67], a[224:227], v[148:151], a[64:67]\n"
    "ds_read_b64_tr_b16 a[252:253], v208 offset:512\n"
    "ds_read_b64_tr_b16 a[254:255], v208 offset:8704\n"
    "v_mfma_f32_16x16x32_bf16 v[0:3], a[224:227], v[152:155], 0\n"
    "s_waitcnt lgkmcnt(9)\n"
    "ds_read_b64_tr_b16 a[224:225], v202 offset:1024\n"
    "v_mov_b32 v237, v239\n"
    "v_mfma_f32_16x16x32_bf16 v[4:7], a[232:235], v[152:155], 0\n"
    "s_mov_b32 s60, 1\n"
    "ds_read_b64_tr_b16 a[226:227], v202 offset:9216\n"
    "v_mov_b32 v235, v240\n"
    "v_mfma_f32_16x16x32_bf16 a[68:71], a[232:235], v[148:151], 0\n"
    "ds_read_b64_tr_b16 a[228:229], v206 offset:1024\n"
    "v_add_u32 v226, 0x400, v226\n"
    "v_mfma_f32_16x16x32_bf16 a[68:71], a[236:239], v[160:163], a[68:71]\n"
    "s_add_u32 m0, s54, 115584\n"
    "ds_read_b64_tr_b16 a[230:231], v206 offset:9216\n"
    "v_fma_f32 v184, v104, s58, v232\n"
    "v_mfma_f32_16x16x32_bf16 a[4:7], a[236:239], v[156:159], 0\n"
    "s_waitcnt lgkmcnt(7)\n"
    "buffer_load_dwordx4 v211, s[40:43], s53 offen offset:128 lds\n"
    "v_mfma_f32_16x16x32_bf16 a[4:7], a[232:235], v[144:147], a[4:7]\n"
    "s_waitcnt lgkmcnt(9)\n"
    "ds_read_b64_tr_b16 a[232:233], v204 offset:1024\n"
    "v_fma_f32 v185, v105, s58, v232\n"
    "v_mfma_f32_16x16x32_bf16 a[8:11], a[240:243], v[144:147], 0\n"
    "s_mov_b32 s13, 0\n"
    "ds_read_b64_tr_b16 a[234:235], v204 offset:9216\n"
    "v_fma_f32 v186, v106, s58, v232\n"
    "v_mfma_f32_16x16x32_bf16 a[8:11], a[244:247], v[156:159], a[8:11]\n"
    "s_nop 0\n"
    "ds_read_b64_tr_b16 a[236:237], v208 offset:1024\n"
    "v_fma_f32 v187, v107, s58, v232\n"
    "v_mfma_f32_16x16x32_bf16 a[72:75], a[244:247], v[160:163], 0\n"
    "v_exp_f32_e64 v184, v184\n"
    "v_mfma_f32_16x16x32_bf16 a[72:75], a[240:243], v[148:151], a[72:75]\n"
    "ds_read_b64_tr_b16 a[238:239], v208 offset:9216\n"
    "v_fma_f32 v188, v116, s58, v232\n"
    "v_mfma_f32_16x16x32_bf16 v[8:11], a[240:243], v[152:155], 0\n"
    "s_waitcnt lgkmcnt(8)\n"
    "v_exp_f32 v185, v185\n"
    "v_mfma_f32_16x16x32_bf16 v[12:15], a[248:251], v[152:155], 0\n"
    "s_add_u32 m0, s54, 116480\n"
    "ds_read_b64_tr_b16 a[240:241], v202 offset:1536\n"
    "v_fma_f32 v189, v117, s58, v232\n"
    "v_mfma_f32_16x16x32_bf16 a[76:79], a[248:251], v[148:151], 0\n"
    "buffer_load_dwordx4 v211, s[40:43], s53 offen offset:256 lds\n"
    "v_mfma_f32_16x16x32_bf16 a[76:79], a[252:255], v[160:163], a[76:79]\n"
    "v_exp_f32_e64 v186, v186\n"
    "v_mfma_f32_16x16x32_bf16 a[12:15], a[252:255], v[156:159], 0\n"
    "ds_read_b64_tr_b16 a[242:243], v202 offset:9728\n"
    "ds_read_b64_tr_b16 a[244:245], v206 offset:1536\n"
    "v_mfma_f32_16x16x32_bf16 a[12:15], a[248:251], v[144:147], a[12:15]\n"
    "s_waitcnt lgkmcnt(7)\n"
    "ds_read_b64_tr_b16 a[246:247], v206 offset:9728\n"
    "v_fma_f32 v190, v118, s58, v232\n"
    "v_mfma_f32_16x16x32_bf16 a[16:19], a[224:227], v[144:147], 0\n"
    "v_exp_f32 v187, v187\n"
    "v_mfma_f32_16x16x32_bf16 a[16:19], a[228:231], v[156:159], a[16:19]\n"
    "s_add_u32 m0, s54, 117376\n"
    "ds_read_b64_tr_b16 a[248:249], v204 offset:1536\n"
    "ds_read_b64_tr_b16 a[250:251], v204 offset:9728\n"
    "v_mfma_f32_16x16x32_bf16 a[80:83], a[228:231], v[160:163], 0\n"
    "buffer_load_dwordx4 v211, s[40:43], s53 offen offset:384 lds\n"
    "v_mfma_f32_16x16x32_bf16 a[80:83], a[224:227], v[148:151], a[80:83]\n"
    "s_add_u32 s53, s53, 16384\n"
    "ds_read_b64_tr_b16 a[252:253], v208 offset:1536\n"
    "ds_read_b64_tr_b16 a[254:255], v208 offset:9728\n"
    "v_mfma_f32_16x16x32_bf16 v[16:19], a[224:227], v[152:155], 0\n"
    "s_waitcnt lgkmcnt(8)\n"
    "ds_read_b64_tr_b16 a[224:225], v203 offset:0\n"
    "v_fma_f32 v191, v119, s58, v232\n"
    "v_mfma_f32_16x16x32_bf16 v[20:23], a[232:235], v[152:155], 0\n"
    "v_exp_f32 v188, v188\n"
    "v_mfma_f32_16x16x32_bf16 a[84:87], a[232:235], v[148:151], 0\n"
    "ds_read_b64_tr_b16 a[226:227], v203 offset:8192\n"
    "v_add_f32_e64 v238, v184, v185\n"
    "v_mfma_f32_16x16x32_bf16 a[84:87], a[236:239], v[160:163], a[84:87]\n"
    "ds_read_b64_tr_b16 a[228:229], v207 offset:0\n"
    "ds_read_b64_tr_b16 a[230:231], v207 offset:8192\n"
    "v_mfma_f32_16x16x32_bf16 a[20:23], a[236:239], v[156:159], 0\n"
    "s_add_u32 m0, s54, 32768\n"
    "v_cvt_pk_bf16_f32 v164, v184, v185\n"
    "v_fma_f32 v192, v108, s58, v233\n"
    "v_mfma_f32_16x16x32_bf16 a[20:23], a[232:235], v[144:147], a[20:23]\n"
    "s_waitcnt lgkmcnt(8)\n"
    "buffer_load_dwordx4 v210, s[36:39], s52 offen offset:0 lds\n"
    "v_mfma_f32_16x16x32_bf16 a[24:27], a[240:243], v[144:147], 0\n"
    "v_exp_f32 v189, v189\n"
    "v_mfma_f32_16x16x32_bf16 a[24:27], a[244:247], v[156:159], a[24:27]\n"
    "ds_read_b64_tr_b16 a[232:233], v205 offset:0\n"
    "ds_read_b64_tr_b16 a[234:235], v205 offset:8192\n"
    "v_add_f32_e64 v238, v238, v186\n"
    "v_mfma_f32_16x16x32_bf16 a[88:91], a[244:247], v[160:163], 0\n"
    "ds_read_b64_tr_b16 a[236:237], v209 offset:0\n"
    "v_fma_f32 v193, v109, s58, v233\n"
    "v_mfma_f32_16x16x32_bf16 a[88:91], a[240:243], v[148:151], a[88:91]\n"
    "v_exp_f32 v190, v190\n"
    "v_mfma_f32_16x16x32_bf16 v[24:27], a[240:243], v[152:155], 0\n"
    "s_waitcnt lgkmcnt(7)\n"
    "ds_read_b64_tr_b16 a[238:239], v209 offset:8192\n"
    "v_add_f32_e64 v238, v238, v187\n"
    "v_mfma_f32_16x16x32_bf16 v[28:31], a[248:251], v[152:155], 0\n"
    "ds_read_b64_tr_b16 a[240:241], v203 offset:512\n"
    "v_cvt_pk_bf16_f32 v165, v186, v187\n"
    "v_mfma_f32_16x16x32_bf16 a[92:95], a[248:251], v[148:151], 0\n"
    "s_add_u32 m0, s54, 33664\n"
    "ds_read_b64_tr_b16 a[242:243], v203 offset:8704\n"
    "v_fma_f32 v194, v110, s58, v233\n"
    "v_mfma_f32_16x16x32_bf16 a[92:95], a[252:255], v[160:163], a[92:95]\n"
    "buffer_load_dwordx4 v210, s[36:39], s52 offen offset:128 lds\n"
    "v_mfma_f32_16x16x32_bf16 a[28:31], a[252:255], v[156:159], 0\n"
    "v_exp_f32 v191, v191\n"
    "v_mfma_f32_16x16x32_bf16 a[28:31], a[248:251], v[144:147], a[28:31]\n"
    "s_waitcnt lgkmcnt(6)\n"
    "ds_read_b64_tr_b16 a[244:245], v207 offset:512\n"
    "ds_read_b64_tr_b16 a[246:247], v207 offset:8704\n"
    "v_mfma_f32_16x16x32_bf16 a[32:35], a[224:227], v[144:147], 0\n"
    "ds_read_b64_tr_b16 a[248:249], v205 offset:512\n"
    "v_add_f32_e64 v238, v238, v188\n"
    "v_mfma_f32_16x16x32_bf16 a[32:35], a[228:231], v[156:159], a[32:35]\n"
    "ds_read_b64_tr_b16 a[250:251], v205 offset:8704\n"
    "v_fma_f32 v195, v111, s58, v233\n"
    "v_mfma_f32_16x16x32_bf16 a[96:99], a[228:231], v[160:163], 0\n"
    "v_exp_f32_e64 v192, v192\n"
    "v_mfma_f32_16x16x32_bf16 a[96:99], a[224:227], v[148:151], a[96:99]\n"
    "s_add_u32 m0, s54, 34560\n"
    "ds_read_b64_tr_b16 a[252:253], v209 offset:512\n"
    "ds_read_b64_tr_b16 a[254:255], v209 offset:8704\n"
    "v_mfma_f32_16x16x32_bf16 v[32:35], a[224:227], v[152:155], 0\n"
    "s_waitcnt lgkmcnt(8)\n"
    "buffer_load_dwordx4 v210, s[36:39], s52 offen offset:256 lds\n"
    "v_mfma_f32_16x16x32_bf16 v[36:39], a[232:235], v[152:155], 0\n"
    "s_nop 0\n"
    "ds_read_b64_tr_b16 a[224:225], v203 offset:1024\n"
    "v_add_f32_e64 v238, v238, v189\n"
    "v_mfma_f32_16x16x32_bf16 a[100:103], a[232:235], v[148:151], 0\n"
    "ds_read_b64_tr_b16 a[226:227], v203 offset:9216\n"
    "v_cvt_pk_bf16_f32 v166, v188, v189\n"
    "v_mfma_f32_16x16x32_bf16 a[100:103], a[236:239], v[160:163], a[100:103]\n"
    "ds_read_b64_tr_b16 a[228:229], v207 offset:1024\n"
    "ds_read_b64_tr_b16 a[230:231], v207 offset:9216\n"
    "v_mfma_f32_16x16x32_bf16 a[36:39], a[236:239], v[156:159], 0\n"
    "v_fma_f32 v196, v120, s58, v233\n"
    "v_add_f32_e64 v238, v238, v190\n"
    "v_mfma_f32_16x16x32_bf16 a[36:39], a[232:235], v[144:147], a[36:39]\n"
    "s_waitcnt lgkmcnt(8)\n"
    "v_exp_f32 v193, v193\n"
    "v_mfma_f32_16x16x32_bf16 a[40:43], a[240:243], v[144:147], 0\n"
    "s_add_u32 m0, s54, 35456\n"
    "ds_read_b64_tr_b16 a[232:233], v205 offset:1024\n"
    "v_fma_f32 v197, v121, s58, v233\n"
    "v_mfma_f32_16x16x32_bf16 a[40:43], a[244:247], v[156:159], a[40:43]\n"
    "buffer_load_dwordx4 v210, s[36:39], s52 offen offset:384 lds\n"
    "v_mfma_f32_16x16x32_bf16 a[104:107], a[244:247], v[160:163], 0\n"
    "s_add_u32 s52, s52, 16384\n"
    "v_exp_f32_e64 v194, v194\n"
    "v_mfma_f32_16x16x32_bf16 a[104:107], a[240:243], v[148:151], a[104:107]\n"
    "ds_read_b64_tr_b16 a[234:235], v205 offset:9216\n"
    "ds_read_b64_tr_b16 a[236:237], v209 offset:1024\n"
    "v_mfma_f32_16x16x32_bf16 v[40:43], a[240:243], v[152:155], 0\n"
    "s_waitcnt lgkmcnt(7)\n"
    "ds_read_b64_tr_b16 a[238:239], v209 offset:9216\n"
    "v_add_f32 v238, v238, v191\n"
    "v_mfma_f32_16x16x32_bf16 v[44:47], a[248:251], v[152:155], 0\n"
    "ds_read_b64_tr_b16 a[240:241], v203 offset:1536\n"
    "v_cvt_pk_bf16_f32 v167, v190, v191\n"
    "v_mfma_f32_16x16x32_bf16 a[108:111], a[248:251], v[148:151], 0\n"
    "ds_read_b64_tr_b16 a[242:243], v203 offset:9728\n"
    "v_fma_f32 v198, v122, s58, v233\n"
    "v_mfma_f32_16x16x32_bf16 a[108:111], a[252:255], v[160:163], a[108:111]\n"
    "s_add_u32 m0, s54, 49152\n"
    "v_exp_f32_e64 v195, v195\n"
    "v_mfma_f32_16x16x32_bf16 a[44:47], a[252:255], v[156:159], 0\n"
    "s_nop 0\n"
    "buffer_load_dwordx4 v210, s[36:39], s52 offen offset:0 lds\n"
    "v_mfma_f32_16x16x32_bf16 a[44:47], a[248:251], v[144:147], a[44:47]\n"
    "s_waitcnt lgkmcnt(6)\n"
    "ds_read_b64_tr_b16 a[244:245], v207 offset:1536\n"
    "ds_read_b64_tr_b16 a[246:247], v207 offset:9728\n"
    "v_mfma_f32_16x16x32_bf16 a[48:51], a[224:227], v[144:147], 0\n"
    "ds_read_b64_tr_b16 a[248:249], v205 offset:1536\n"
    "ds_read_b64_tr_b16 a[250:251], v205 offset:9728\n"
    "v_mfma_f32_16x16x32_bf16 a[48:51], a[228:231], v[156:159], a[48:51]\n"
    "ds_read_b64_tr_b16 a[252:253], v209 offset:1536\n"
    "ds_read_b64_tr_b16 a[254:255], v209 offset:9728\n"
    "v_mfma_f32_16x16x32_bf16 a[112:115], a[228:231], v[160:163], 0\n"
    "v_fma_f32 v199, v123, s58, v233\n"
    "v_add_f32_e64 v239, v192, v193\n"
    "v_mfma_f32_16x16x32_bf16 a[112:115], a[224:227], v[148:151], a[112:115]\n"
    "v_exp_f32_e64 v196, v196\n"
    "v_cvt_pk_bf16_f32 v168, v192, v193\n"
    "v_mfma_f32_16x16x32_bf16 v[48:51], a[224:227], v[152:155], 0\n"
    "s_waitcnt lgkmcnt(8)\n"
    "v_fma_f32 v184, v112, s58, v234\n"
    "v_add_f32 v239, v239, v194\n"
    "v_mfma_f32_16x16x32_bf16 v[52:55], a[232:235], v[152:155], 0\n"
    "s_add_u32 m0, s54, 50048\n"
    "v_exp_f32_e64 v197, v197\n"
    "v_mfma_f32_16x16x32_bf16 a[116:119], a[232:235], v[148:151], 0\n"
    "s_waitcnt vmcnt(13) lgkmcnt(0)\n"
    "buffer_load_dwordx4 v210, s[36:39], s52 offen offset:128 lds\n"
    "v_mfma_f32_16x16x32_bf16 a[116:119], a[236:239], v[160:163], a[116:119]\n"
    "s_barrier\n"
    "ds_read_b128 a[224:227], v200 offset:0\n"
    "ds_read_b128 a[228:231], v200 offset:512\n"
    "v_mfma_f32_16x16x32_bf16 a[52:55], a[236:239], v[156:159], 0\n"
    "v_fma_f32 v185, v113, s58, v234\n"
    "v_add_f32_e64 v239, v239, v195\n"
    "v_mfma_f32_16x16x32_bf16 a[52:55], a[232:235], v[144:147], a[52:55]\n"
    "v_exp_f32_e64 v198, v198\n"
    "v_mfma_f32_16x16x32_bf16 a[56:59], a[240:243], v[144:147], 0\n"
    "s_add_u32 m0, s54, 50944\n"
    "ds_read_b128 a[232:235], v200 offset:1024\n"
    "v_cvt_pk_bf16_f32 v169, v194, v195\n"
    "v_mfma_f32_16x16x32_bf16 a[56:59], a[244:247], v[156:159], a[56:59]\n"
    "buffer_load_dwordx4 v210, s[36:39], s52 offen offset:256 lds\n"
    "v_mfma_f32_16x16x32_bf16 a[120:123], a[244:247], v[160:163], 0\n"
    "ds_read_b128 a[236:239], v200 offset:1536\n"
    "v_fma_f32 v186, v114, s58, v234\n"
    "v_mfma_f32_16x16x32_bf16 a[120:123], a[240:243], v[148:151], a[120:123]\n"
    "v_exp_f32_e64 v199, v199\n"
    "v_mfma_f32_16x16x32_bf16 v[56:59], a[240:243], v[152:155], 0\n"
    "ds_read_b128 a[240:243], v200 offset:2048\n"
    "v_add_f32_e64 v239, v239, v196\n"
    "v_mfma_f32_16x16x32_bf16 v[60:63], a[248:251], v[152:155], 0\n"
    "ds_read_b128 a[244:247], v200 offset:2560\n"
    "v_fma_f32 v187, v115, s58, v234\n"
    "v_mfma_f32_16x16x32_bf16 a[124:127], a[248:251], v[148:151], 0\n"
    "v_exp_f32_e64 v184, v184\n"
    "v_mfma_f32_16x16x32_bf16 a[124:127], a[252:255], v[160:163], a[124:127]\n"
    "s_add_u32 m0, s54, 51840\n"
    "v_add_f32_e64 v239, v239, v197\n"
    "v_cvt_pk_bf16_f32 v170, v196, v197\n"
    "v_mfma_f32_16x16x32_bf16 a[60:63], a[252:255], v[156:159], 0\n"
    "s_nop 0\n"
    "buffer_load_dwordx4 v210, s[36:39], s52 offen offset:384 lds\n"
    "v_mfma_f32_16x16x32_bf16 a[60:63], a[248:251], v[144:147], a[60:63]\n"
    "s_waitcnt lgkmcnt(4)\n"
    "ds_read_b128 a[248:251], v200 offset:3072\n"
    "v_fma_f32 v188, v124, s58, v234\n"
    "v_mfma_f32_16x16x32_bf16 v[64:67], a[224:227], a[128:131], 0\n"
    "s_add_u32 s52, s52, 16384\n"
    "v_exp_f32_e64 v185, v185\n"
    "v_mfma_f32_16x16x32_bf16 v[64:67], a[228:231], a[132:135], v[64:67]\n"
    "ds_read_b128 a[252:255], v200 offset:3584\n"
    "v_add_f32_e64 v239, v239, v198\n"
    "v_mfma_f32_16x16x32_bf16 v[68:71], a[228:231], a[164:167], 0\n"
    "v_fma_f32 v189, v125, s58, v234\n"
    "v_add_f32_e64 v239, v239, v199\n"
    "v_mfma_f32_16x16x32_bf16 v[68:71], a[224:227], a[160:163], v[68:71]\n"
    "v_exp_f32_e64 v186, v186\n"
    "v_mfma_f32_16x16x32_bf16 v[72:75], a[224:227], a[192:195], 0\n"
    "s_add_u32 m0, s54, 65536\n"
    "ds_read_b128 a[224:227], v200 offset:8192\n"
    "v_cvt_pk_bf16_f32 v171, v198, v199\n"
    "v_mfma_f32_16x16x32_bf16 v[72:75], a[228:231], a[196:199], v[72:75]\n"
    "s_waitcnt lgkmcnt(5)\n"
    "buffer_load_dwordx4 v211, s[40:43], s53 offen offset:0 lds\n"
    "v_mfma_f32_16x16x32_bf16 v[72:75], a[232:235], a[200:203], v[72:75]\n"
    "s_nop 0\n"
    "ds_read_b128 a[228:231], v200 offset:8704\n"
    "v_fma_f32 v190, v126, s58, v234\n"
    "v_mfma_f32_16x16x32_bf16 v[72:75], a[236:239], a[204:207], v[72:75]\n"
    "v_exp_f32_e64 v187, v187\n"
    "v_mfma_f32_16x16x32_bf16 v[68:71], a[236:239], a[172:175], v[68:71]\n"
    "v_fma_f32 v191, v127, s58, v234\n"
    "v_add_f32_e64 v240, v184, v185\n"
    "v_mfma_f32_16x16x32_bf16 v[68:71], a[232:235], a[168:171], v[68:71]\n"
    "v_exp_f32_e64 v188, v188\n"
    "v_mfma_f32_16x16x32_bf16 v[64:67], a[232:235], a[136:139], v[64:67]\n"
    "s_nop 0\n"
    "ds_read_b128 a[232:235], v200 offset:9216\n"
    "v_cvt_pk_bf16_f32 v172, v184, v185\n"
    "v_mfma_f32_16x16x32_bf16 v[64:67], a[236:239], a[140:143], v[64:67]\n"
    "s_waitcnt lgkmcnt(5)\n"
    "ds_read_b128 a[236:239], v200 offset:9728\n"
    "v_fma_f32 v192, v128, s58, v232\n"
    "v_mfma_f32_16x16x32_bf16 v[64:67], a[240:243], a[144:147], v[64:67]\n"
    "s_add_u32 m0, s54, 66432\n"
    "v_exp_f32_e64 v189, v189\n"
    "v_mfma_f32_16x16x32_bf16 v[64:67], a[244:247], a[148:151], v[64:67]\n"
    "buffer_load_dwordx4 v211, s[40:43], s53 offen offset:128 lds\n"
    "v_mfma_f32_16x16x32_bf16 v[68:71], a[244:247], a[180:183], v[68:71]\n"
    "v_add_f32_e64 v240, v240, v186\n"
    "v_fma_f32 v193, v129, s58, v232\n"
    "v_mfma_f32_16x16x32_bf16 v[68:71], a[240:243], a[176:179], v[68:71]\n"
    "v_exp_f32 v190, v190\n"
    "v_add_f32 v240, v240, v187\n"
    "v_mfma_f32_16x16x32_bf16 v[72:75], a[240:243], a[208:211], v[72:75]\n"
    "s_nop 0\n"
    "ds_read_b128 a[240:243], v200 offset:10240\n"
    "v_cvt_pk_bf16_f32 v173, v186, v187\n"
    "v_mfma_f32_16x16x32_bf16 v[72:75], a[244:247], a[212:215], v[72:75]\n"
    "s_waitcnt lgkmcnt(5)\n"
    "ds_read_b128 a[244:247], v200 offset:10752\n"
    "v_fma_f32 v194, v130, s58, v232\n"
    "v_mfma_f32_16x16x32_bf16 v[72:75], a[248:251], a[216:219], v[72:75]\n"
    "s_add_u32 m0, s54, 67328\n"
    "v_exp_f32_e64 v191, v191\n"
    "v_mfma_f32_16x16x32_bf16 v[72:75], a[252:255], a[220:223], v[72:75]\n"
    "buffer_load_dwordx4 v211, s[40:43], s53 offen offset:256 lds\n"
    "v_mfma_f32_16x16x32_bf16 v[68:71], a[252:255], a[188:191], v[68:71]\n"
    "v_add_f32_e64 v240, v240, v188\n"
    "v_fma_f32 v195, v131, s58, v232\n"
    "v_mfma_f32_16x16x32_bf16 v[68:71], a[248:251], a[184:187], v[68:71]\n"
    "v_exp_f32_e64 v192, v192\n"
    "v_mfma_f32_16x16x32_bf16 v[64:67], a[248:251], a[152:155], v[64:67]\n"
    "ds_read_b128 a[248:251], v200 offset:11264\n"
    "v_add_f32 v240, v240, v189\n"
    "v_mfma_f32_16x16x32_bf16 v[64:67], a[252:255], a[156:159], v[64:67]\n"
    "s_waitcnt lgkmcnt(5)\n"
    "ds_read_b128 a[252:255], v200 offset:11776\n"
    "v_cvt_pk_bf16_f32 v174, v188, v189\n"
    "v_mfma_f32_16x16x32_bf16 v[76:79], a[224:227], a[128:131], 0\n"
    "v_fma_f32 v196, v136, s58, v232\n"
    "v_add_f32_e64 v240, v240, v190\n"
    "v_mfma_f32_16x16x32_bf16 v[76:79], a[228:231], a[132:135], v[76:79]\n"
    "v_exp_f32_e64 v193, v193\n"
    "v_mfma_f32_16x16x32_bf16 v[80:83], a[228:231], a[164:167], 0\n"
    "s_add_u32 m0, s54, 68224\n"
    "v_fma_f32 v197, v137, s58, v232\n"
    "v_add_f32_e64 v240, v240, v191\n"
    "v_mfma_f32_16x16x32_bf16 v[80:83], a[224:227], a[160:163], v[80:83]\n"
    "buffer_load_dwordx4 v211, s[40:43], s53 offen offset:384 lds\n"
    "v_mfma_f32_16x16x32_bf16 v[84:87], a[224:227], a[192:195], 0\n"
    "s_add_u32 s53, s53, 16384\n"
    "v_exp_f32 v194, v194\n"
    "v_mfma_f32_16x16x32_bf16 v[84:87], a[228:231], a[196:199], v[84:87]\n"
    "s_waitcnt lgkmcnt(4)\n"
    "ds_read_b128 a[224:227], v201 offset:0\n"
    "v_cvt_pk_bf16_f32 v175, v190, v191\n"
    "v_mfma_f32_16x16x32_bf16 v[84:87], a[232:235], a[200:203], v[84:87]\n"
    "ds_read_b128 a[228:231], v201 offset:512\n"
    "v_fma_f32 v198, v138, s58, v232\n"
    "v_mfma_f32_16x16x32_bf16 v[84:87], a[236:239], a[204:207], v[84:87]\n"
    "v_exp_f32_e64 v195, v195\n"
    "v_mfma_f32_16x16x32_bf16 v[80:83], a[236:239], a[172:175], v[80:83]\n"
    "v_add_f32_e64 v238, v238, v192\n"
    "v_fma_f32 v199, v139, s58, v232\n"
    "v_mfma_f32_16x16x32_bf16 v[80:83], a[232:235], a[168:171], v[80:83]\n"
    "s_add_u32 m0, s54, 81920\n"
    "v_exp_f32_e64 v196, v196\n"
    "v_mfma_f32_16x16x32_bf16 v[76:79], a[232:235], a[136:139], v[76:79]\n"
    "buffer_load_dwordx4 v211, s[40:43], s53 offen offset:0 lds\n"
    "v_mfma_f32_16x16x32_bf16 v[76:79], a[236:239], a[140:143], v[76:79]\n"
    "s_waitcnt lgkmcnt(4)\n"
    "ds_read_b128 a[232:235], v201 offset:1024\n"
    "v_add_f32 v238, v238, v193\n"
    "v_mfma_f32_16x16x32_bf16 v[76:79], a[240:243], a[144:147], v[76:79]\n"
    "ds_read_b128 a[236:239], v201 offset:1536\n"
    "v_cvt_pk_bf16_f32 v176, v192, v193\n"
    "v_mfma_f32_16x16x32_bf16 v[76:79], a[244:247], a[148:151], v[76:79]\n"
    "v_fma_f32 v184, v132, s58, v233\n"
    "v_add_f32_e64 v238, v238, v194\n"
    "v_mfma_f32_16x16x32_bf16 v[80:83], a[244:247], a[180:183], v[80:83]\n"
    "v_exp_f32_e64 v197, v197\n"
    "v_mfma_f32_16x16x32_bf16 v[80:83], a[240:243], a[176:179], v[80:83]\n"
    "v_fma_f32 v185, v133, s58, v233\n"
    "v_add_f32_e64 v238, v238, v195\n"
    "v_mfma_f32_16x16x32_bf16 v[84:87], a[240:243], a[208:211], v[84:87]\n"
    "s_add_u32 m0, s54, 82816\n"
    "v_exp_f32 v198, v198\n"
    "v_mfma_f32_16x16x32_bf16 v[84:87], a[244:247], a[212:215], v[84:87]\n"
    "s_waitcnt lgkmcnt(4)\n"
    "buffer_load_dwordx4 v211, s[40:43], s53 offen offset:128 lds\n"
    "v_mfma_f32_16x16x32_bf16 v[84:87], a[248:251], a[216:219], v[84:87]\n"
    "ds_read_b128 a[240:243], v201 offset:2048\n"
    "v_cvt_pk_bf16_f32 v177, v194, v195\n"
    "v_mfma_f32_16x16x32_bf16 v[84:87], a[252:255], a[220:223], v[84:87]\n"
    "ds_read_b128 a[244:247], v201 offset:2560\n"
    "v_fma_f32 v186, v134, s58, v233\n"
    "v_mfma_f32_16x16x32_bf16 v[80:83], a[252:255], a[188:191], v[80:83]\n"
    "v_exp_f32_e64 v199, v199\n"
    "v_mfma_f32_16x16x32_bf16 v[80:83], a[248:251], a[184:187], v[80:83]\n"
    "v_add_f32_e64 v238, v238, v196\n"
    "v_fma_f32 v187, v135, s58, v233\n"
    "v_mfma_f32_16x16x32_bf16 v[76:79], a[248:251], a[152:155], v[76:79]\n"
    "v_exp_f32 v184, v184\n"
    "v_mfma_f32_16x16x32_bf16 v[76:79], a[252:255], a[156:159], v[76:79]\n"
    "s_waitcnt lgkmcnt(4)\n"
    "ds_read_b128 a[248:251], v201 offset:3072\n"
    "ds_read_b128 a[252:255], v201 offset:3584\n"
    "v_add_f32_e64 v238, v238, v197\n"
    "v_mfma_f32_16x16x32_bf16 v[88:91], a[224:227], a[128:131], 0\n"
    "s_add_u32 m0, s54, 83712\n"
    "v_cvt_pk_bf16_f32 v178, v196, v197\n"
    "v_fma_f32 v188, v140, s58, v233\n"
    "v_mfma_f32_16x16x32_bf16 v[88:91], a[228:231], a[132:135], v[88:91]\n"
    "buffer_load_dwordx4 v211, s[40:43], s53 offen offset:256 lds\n"
    "v_mfma_f32_16x16x32_bf16 v[92:95], a[228:231], a[164:167], 0\n"
    "v_exp_f32 v185, v185\n"
    "v_mfma_f32_16x16x32_bf16 v[92:95], a[224:227], a[160:163], v[92:95]\n"
    "s_waitcnt lgkmcnt(4)\n"
    "ds_read_b128 a[224:227], v201 offset:8192\n"
    "ds_read_b128 a[228:231], v201 offset:8704\n"
    "v_mfma_f32_16x16x32_bf16 v[92:95], a[232:235], a[168:171], v[92:95]\n"
    "v_add_f32_e64 v238, v238, v198\n"
    "v_fma_f32 v189, v141, s58, v233\n"
    "v_mfma_f32_16x16x32_bf16 v[92:95], a[236:239], a[172:175], v[92:95]\n"
    "v_exp_f32_e64 v186, v186\n"
    "v_mfma_f32_16x16x32_bf16 v[88:91], a[236:239], a[140:143], v[88:91]\n"
    "s_add_u32 m0, s54, 84608\n"
    "v_add_f32 v238, v238, v199\n"
    "v_cvt_pk_bf16_f32 v179, v198, v199\n"
    "v_mfma_f32_16x16x32_bf16 v[88:91], a[232:235], a[136:139], v[88:91]\n"
    "s_waitcnt lgkmcnt(4)\n"
    "buffer_load_dwordx4 v211, s[40:43], s53 offen offset:384 lds\n"
    "v_mfma_f32_16x16x32_bf16 v[88:91], a[240:243], a[144:147], v[88:91]\n"
    "s_add_u32 s53, s53, 16384\n"
    "ds_read_b128 a[232:235], v201 offset:9216\n"
    "ds_read_b128 a[236:239], v201 offset:9728\n"
    "v_mfma_f32_16x16x32_bf16 v[88:91], a[244:247], a[148:151], v[88:91]\n"
    "Lpad2_%=:\n"
    "s_cbranch_execz Lhopd0_%=\n"
    "v_fma_f32 v190, v142, s58, v233\n"
    "v_add_f32 v239, v239, v184\n"
    "v_mfma_f32_16x16x32_bf16 v[92:95], a[244:247], a[180:183], v[92:95]\n"
    "v_exp_f32 v187, v187\n"
    "v_mfma_f32_16x16x32_bf16 v[92:95], a[240:243], a[176:179], v[92:95]\n"
    "s_waitcnt lgkmcnt(4)\n"
    "ds_read_b128 a[240:243], v201 offset:10240\n"
    "ds_read_b128 a[244:247], v201 offset:10752\n"
    "v_mfma_f32_16x16x32_bf16 v[92:95], a[248:251], a[184:187], v[92:95]\n"
    "v_fma_f32 v191, v143, s58, v233\n"
    "v_add_f32_e64 v239, v239, v185\n"
    "v_mfma_f32_16x16x32_bf16 v[92:95], a[252:255], a[188:191], v[92:95]\n"
    "v_exp_f32_e64 v188, v188\n"
    "v_mfma_f32_16x16x32_bf16 v[88:91], a[252:255], a[156:159], v[88:91]\n"
    "v_cvt_pk_bf16_f32 v180, v184, v185\n"
    "v_add_f32 v239, v239, v186\n"
    "v_mfma_f32_16x16x32_bf16 v[88:91], a[248:251], a[152:155], v[88:91]\n"
    "s_waitcnt lgkmcnt(4)\n"
    "buffer_store_dwordx4 v[228:231], v216, s[48:51], s24 offen\n"
    "v_mfma_f32_16x16x32_bf16 v[96:99], a[224:227], a[128:131], 0\n"
    "ds_read_b128 a[248:251], v201 offset:11264\n"
    "ds_read_b128 a[252:255], v201 offset:11776\n"
    "v_mfma_f32_16x16x32_bf16 v[96:99], a[228:231], a[132:135], v[96:99]\n"
    "v_exp_f32_e64 v189, v189\n"
    "v_mfma_f32_16x16x32_bf16 v[100:103], a[228:231], a[164:167], 0\n"
    "v_exp_f32_e64 v190, v190\n"
    "v_mfma_f32_16x16x32_bf16 v[100:103], a[224:227], a[160:163], v[100:103]\n"
    "s_waitcnt lgkmcnt(4)\n"
    "v_add_f32 v239, v239, v187\n"
    "v_cvt_pk_bf16_f32 v181, v186, v187\n"
    "v_mfma_f32_16x16x32_bf16 v[100:103], a[232:235], a[168:171], v[100:103]\n"
    "s_mov_b32 m0, s65\n"
    "v_exp_f32 v191, v191\n"
    "v_mfma_f32_16x16x32_bf16 v[100:103], a[236:239], a[172:175], v[100:103]\n"
    "buffer_load_dwordx4 v210, s[44:47], s24 offen offset:0 nt lds\n"
    "v_mfma_f32_16x16x32_bf16 v[96:99], a[236:239], a[140:143], v[96:99]\n"
    "s_waitcnt vmcnt(10) lgkmcnt(0)\n"
    "v_add_f32 v239, v239, v188\n"
    "v_add_f32 v239, v239, v189\n"
    "v_mfma_f32_16x16x32_bf16 v[96:99], a[232:235], a[136:139], v[96:99]\n"
    "s_barrier\n"
    "ds_read_b64_tr_b16 a[224:225], v202 offset:32768\n"
    "ds_read_b64_tr_b16 a[226:227], v202 offset:40960\n"
    "v_mfma_f32_16x16x32_bf16 v[96:99], a[240:243], a[144:147], v[96:99]\n"
    "s_add_u32 s65, s65, 1024\n"
    "ds_read_b64_tr_b16 a[228:229], v206 offset:32768\n"
    "ds_read_b64_tr_b16 a[230:231], v206 offset:40960\n"
    "v_mfma_f32_16x16x32_bf16 v[96:99], a[244:247], a[148:151], v[96:99]\n"
    "s_mov_b32 s24, s25\n"
    "ds_read_b64_tr_b16 a[232:233], v204 offset:32768\n"
    "ds_read_b64_tr_b16 a[234:235], v204 offset:40960\n"
    "v_mfma_f32_16x16x32_bf16 v[100:103], a[244:247], a[180:183], v[100:103]\n"
    "s_mov_b32 s25, s26\n"
    "ds_read_b64_tr_b16 a[236:237], v208 offset:32768\n"
    "ds_read_b64_tr_b16 a[238:239], v208 offset:40960\n"
    "v_mfma_f32_16x16x32_bf16 v[100:103], a[240:243], a[176:179], v[100:103]\n"
    "s_mov_b32 s26, s27\n"
    "ds_read_b64_tr_b16 a[240:241], v202 offset:33280\n"
    "v_cvt_pk_bf16_f32 v182, v188, v189\n"
    "v_mfma_f32_16x16x32_bf16 v[100:103], a[248:251], a[184:187], v[100:103]\n"
    "s_mov_b32 s27, s28\n"
    "ds_read_b64_tr_b16 a[242:243], v202 offset:41472\n"
    "ds_read_b64_tr_b16 a[244:245], v206 offset:33280\n"
    "v_mfma_f32_16x16x32_bf16 v[100:103], a[252:255], a[188:191], v[100:103]\n"
    "s_mov_b32 s28, s29\n"
    "ds_read_b64_tr_b16 a[246:247], v206 offset:41472\n"
    "v_add_f32 v239, v239, v190\n"
    "v_mfma_f32_16x16x32_bf16 v[96:99], a[252:255], a[156:159], v[96:99]\n"
    "s_mov_b32 s29, s30\n"
    "v_add_f32 v239, v239, v191\n"
    "v_cvt_pk_bf16_f32 v183, v190, v191\n"
    "v_mfma_f32_16x16x32_bf16 v[96:99], a[248:251], a[152:155], v[96:99]\n"
    "s_mov_b32 s30, s31\n"
    "v_max3_f32 v243, v238, v239, v240\n"
    "v_cmp_lt_f32 vcc, s59, v243\n"
    "s_cbranch_vccnz LrareU0_%=\n"
    "LbackU0_%=:\n"
    "Lloop_%=:\n"
    "s_waitcnt lgkmcnt(8)\n"
    "v_mfma_f32_16x16x32_bf16 a[0:3], a[224:227], v[164:167], a[0:3]\n"
    "s_nop 0\n"
    "ds_read_b64_tr_b16 a[248:249], v204 offset:33280\n"
    "ds_read_b128 v[228:231], v226\n"
    "v_mfma_f32_16x16x32_bf16 a[0:3], a[228:231], v[176:179], a[0:3]\n"
    "s_add_u32 m0, s54, 0\n"
    "ds_read_b64_tr_b16 a[250:251], v204 offset:41472\n"
    "v_add_u32 v226, 0x400, v226\n"
    "v_mfma_f32_16x16x32_bf16 a[64:67], a[228:231], v[180:183], a[64:67]\n"
    "buffer_load_dwordx4 v210, s[36:39], s52 offen offset:0 lds\n"
    "v_mfma_f32_16x16x32_bf16 a[64:67], a[224:227], v[168:171], a[64:67]\n"
    "ds_read_b64_tr_b16 a[252:253], v208 offset:33280\n"
    "ds_read_b64_tr_b16 a[254:255], v208 offset:41472\n"
    "v_mfma_f32_16x16x32_bf16 v[0:3], a[224:227], v[172:175], v[0:3]\n"
    "s_waitcnt lgkmcnt(9)\n"
    "ds_read_b64_tr_b16 a[224:225], v202 offset:33792\n"
    "v_add_f32 v236, v236, v238\n"
    "v_mfma_f32_16x16x32_bf16 v[4:7], a[232:235], v[172:175], v[4:7]\n"
    "s_lshl_b32 s60, s60, 1\n"
    "ds_read_b64_tr_b16 a[226:227], v202 offset:41984\n"
    "v_add_f32 v237, v237, v239\n"
    "v_mfma_f32_16x16x32_bf16 a[68:71], a[232:235], v[168:171], a[68:71]\n"
    "ds_read_b64_tr_b16 a[228:229], v206 offset:33792\n"
    "v_add_f32_e64 v235, v235, v240\n"
    "v_mfma_f32_16x16x32_bf16 a[68:71], a[236:239], v[180:183], a[68:71]\n"
    "ds_read_b64_tr_b16 a[230:231], v206 offset:41984\n"
    "v_fma_f32 v184, v64, s58, v232\n"
    "v_mfma_f32_16x16x32_bf16 a[4:7], a[236:239], v[176:179], a[4:7]\n"
    "s_waitcnt lgkmcnt(7)\n"
    "v_fma_f32 v185, v65, s58, v232\n"
    "v_fma_f32 v186, v66, s58, v232\n"
    "v_mfma_f32_16x16x32_bf16 a[4:7], a[232:235], v[164:167], a[4:7]\n"
    "s_waitcnt lgkmcnt(9)\n"
    "ds_read_b64_tr_b16 a[232:233], v204 offset:33792\n"
    "v_fma_f32 v187, v67, s58, v232\n"
    "v_mfma_f32_16x16x32_bf16 a[8:11], a[240:243], v[164:167], a[8:11]\n"
    "s_and_b32 s12, s60, 0x80\n"
    "buffer_store_dwordx4 v[228:231], v216, s[48:51], s24 offen\n"
    "v_mfma_f32_16x16x32_bf16 a[8:11], a[244:247], v[176:179], a[8:11]\n"
    "v_exp_f32_e64 v184, v184\n"
    "v_mfma_f32_16x16x32_bf16 a[72:75], a[244:247], v[180:183], a[72:75]\n"
    "ds_read_b64_tr_b16 a[234:235], v204 offset:41984\n"
    "v_fma_f32 v188, v76, s58, v232\n"
    "v_mfma_f32_16x16x32_bf16 a[72:75], a[240:243], v[168:171], a[72:75]\n"
    "v_exp_f32 v185, v185\n"
    "v_mfma_f32_16x16x32_bf16 v[8:11], a[240:243], v[172:175], v[8:11]\n"
    "s_waitcnt lgkmcnt(6)\n"
    "ds_read_b64_tr_b16 a[236:237], v208 offset:33792\n"
    "ds_read_b64_tr_b16 a[238:239], v208 offset:41984\n"
    "v_mfma_f32_16x16x32_bf16 v[12:15], a[248:251], v[172:175], v[12:15]\n"
    "ds_read_b64_tr_b16 a[240:241], v202 offset:34304\n"
    "v_fma_f32 v189, v77, s58, v232\n"
    "v_mfma_f32_16x16x32_bf16 a[76:79], a[248:251], v[168:171], a[76:79]\n"
    "v_exp_f32_e64 v186, v186\n"
    "v_mfma_f32_16x16x32_bf16 a[76:79], a[252:255], v[180:183], a[76:79]\n"
    "ds_read_b64_tr_b16 a[242:243], v202 offset:42496\n"
    "ds_read_b64_tr_b16 a[244:245], v206 offset:34304\n"
    "v_mfma_f32_16x16x32_bf16 a[12:15], a[252:255], v[176:179], a[12:15]\n"
    "s_mov_b32 m0, s65\n"
    "ds_read_b64_tr_b16 a[246:247], v206 offset:42496\n"
    "v_fma_f32 v190, v78, s58, v232\n"
    "v_mfma_f32_16x16x32_bf16 a[12:15], a[248:251], v[164:167], a[12:15]\n"
    "s_waitcnt lgkmcnt(8)\n"
    "buffer_load_dwordx4 v210, s[44:47], s24 offen offset:0 nt lds\n"
    "v_mfma_f32_16x16x32_bf16 a[16:19], a[224:227], v[164:167], a[16:19]\n"
    "s_add_u32 s65, s65, 1024\n"
    "v_exp_f32_e64 v187, v187\n"
    "v_mfma_f32_16x16x32_bf16 a[16:19], a[228:231], v[176:179], a[16:19]\n"
    "s_mov_b32 s24, s25\n"
    "ds_read_b64_tr_b16 a[248:249], v204 offset:34304\n"
    "ds_read_b64_tr_b16 a[250:251], v204 offset:42496\n"
    "v_mfma_f32_16x16x32_bf16 a[80:83], a[228:231], v[180:183], a[80:83]\n"
    "s_mov_b32 s25, s26\n"
    "ds_read_b64_tr_b16 a[252:253], v208 offset:34304\n"
    "v_fma_f32 v191, v79, s58, v232\n"
    "v_mfma_f32_16x16x32_bf16 a[80:83], a[224:227], v[168:171], a[80:83]\n"
    "s_mov_b32 s26, s27\n"
    "v_exp_f32 v188, v188\n"
    "v_mfma_f32_16x16x32_bf16 v[16:19], a[224:227], v[172:175], v[16:19]\n"
    "s_waitcnt lgkmcnt(7)\n"
    "ds_read_b64_tr_b16 a[254:255], v208 offset:42496\n"
    "v_add_f32 v238, v184, v185\n"
    "v_mfma_f32_16x16x32_bf16 v[20:23], a[232:235], v[172:175], v[20:23]\n"
    "s_mov_b32 s27, s28\n"
    "ds_read_b64_tr_b16 a[224:225], v203 offset:32768\n"
    "v_cvt_pk_bf16_f32 v144, v184, v185\n"
    "v_mfma_f32_16x16x32_bf16 a[84:87], a[232:235], v[168:171], a[84:87]\n"
    "s_mov_b32 s28, s29\n"
    "ds_read_b64_tr_b16 a[226:227], v203 offset:40960\n"
    "v_fma_f32 v192, v68, s58, v233\n"
    "v_mfma_f32_16x16x32_bf16 a[84:87], a[236:239], v[180:183], a[84:87]\n"
    "s_add_u32 m0, s54, 896\n"
    "v_exp_f32_e64 v189, v189\n"
    "v_mfma_f32_16x16x32_bf16 a[20:23], a[236:239], v[176:179], a[20:23]\n"
    "s_mov_b32 s29, s30\n"
    "buffer_load_dwordx4 v210, s[36:39], s52 offen offset:128 lds\n"
    "v_mfma_f32_16x16x32_bf16 a[20:23], a[232:235], v[164:167], a[20:23]\n"
    "s_waitcnt lgkmcnt(6)\n"
    "ds_read_b64_tr_b16 a[228:229], v207 offset:32768\n"
    "ds_read_b64_tr_b16 a[230:231], v207 offset:40960\n"
    "v_mfma_f32_16x16x32_bf16 a[24:27], a[240:243], v[164:167], a[24:27]\n"
    "s_mov_b32 s30, s31\n"
    "ds_read_b64_tr_b16 a[232:233], v205 offset:32768\n"
    "v_add_f32 v238, v238, v186\n"
    "v_mfma_f32_16x16x32_bf16 a[24:27], a[244:247], v[176:179], a[24:27]\n"
    "ds_read_b64_tr_b16 a[234:235], v205 offset:40960\n"
    "v_fma_f32 v193, v69, s58, v233\n"
    "v_mfma_f32_16x16x32_bf16 a[88:91], a[244:247], v[180:183], a[88:91]\n"
    "v_exp_f32_e64 v190, v190\n"
    "v_mfma_f32_16x16x32_bf16 a[88:91], a[240:243], v[168:171], a[88:91]\n"
    "ds_read_b64_tr_b16 a[236:237], v209 offset:32768\n"
    "ds_read_b64_tr_b16 a[238:239], v209 offset:40960\n"
    "v_mfma_f32_16x16x32_bf16 v[24:27], a[240:243], v[172:175], v[24:27]\n"
    "s_waitcnt lgkmcnt(8)\n"
    "ds_read_b64_tr_b16 a[240:241], v203 offset:33280\n"
    "v_add_f32 v238, v238, v187\n"
    "v_mfma_f32_16x16x32_bf16 v[28:31], a[248:251], v[172:175], v[28:31]\n"
    "ds_read_b64_tr_b16 a[242:243], v203 offset:41472\n"
    "v_cvt_pk_bf16_f32 v145, v186, v187\n"
    "v_mfma_f32_16x16x32_bf16 a[92:95], a[248:251], v[168:171], a[92:95]\n"
    "s_add_u32 m0, s54, 1792\n"
    "ds_read_b64_tr_b16 a[244:245], v207 offset:33280\n"
    "v_fma_f32 v194, v70, s58, v233\n"
    "v_mfma_f32_16x16x32_bf16 a[92:95], a[252:255], v[180:183], a[92:95]\n"
    "buffer_load_dwordx4 v210, s[36:39], s52 offen offset:256 lds\n"
    "v_mfma_f32_16x16x32_bf16 a[28:31], a[252:255], v[176:179], a[28:31]\n"
    "v_exp_f32 v191, v191\n"
    "v_mfma_f32_16x16x32_bf16 a[28:31], a[248:251], v[164:167], a[28:31]\n"
    "s_waitcnt lgkmcnt(7)\n"
    "ds_read_b64_tr_b16 a[246:247], v207 offset:41472\n"
    "v_add_f32_e64 v238, v238, v188\n"
    "v_mfma_f32_16x16x32_bf16 a[32:35], a[224:227], v[164:167], a[32:35]\n"
    "ds_read_b64_tr_b16 a[248:249], v205 offset:33280\n"
    "v_fma_f32 v195, v71, s58, v233\n"
    "v_mfma_f32_16x16x32_bf16 a[32:35], a[228:231], v[176:179], a[32:35]\n"
    "v_exp_f32_e64 v192, v192\n"
    "v_mfma_f32_16x16x32_bf16 a[96:99], a[228:231], v[180:183], a[96:99]\n"
    "ds_read_b64_tr_b16 a[250:251], v205 offset:41472\n"
    "v_add_f32_e64 v238, v238, v189\n"
    "v_mfma_f32_16x16x32_bf16 a[96:99], a[224:227], v[168:171], a[96:99]\n"
    "s_nop 0\n"
    "ds_read_b64_tr_b16 a[252:253], v209 offset:33280\n"
    "ds_read_b64_tr_b16 a[254:255], v209 offset:41472\n"
    "v_mfma_f32_16x16x32_bf16 v[32:35], a[224:227], v[172:175], v[32:35]\n"
    "s_waitcnt lgkmcnt(8)\n"
    "ds_read_b64_tr_b16 a[224:225], v203 offset:33792\n"
    "v_cvt_pk_bf16_f32 v146, v188, v189\n"
    "v_mfma_f32_16x16x32_bf16 v[36:39], a[232:235], v[172:175], v[36:39]\n"
    "s_add_u32 m0, s54, 2688\n"
    "ds_read_b64_tr_b16 a[226:227], v203 offset:41984\n"
    "v_fma_f32 v196, v80, s58, v233\n"
    "v_mfma_f32_16x16x32_bf16 a[100:103], a[232:235], v[168:171], a[100:103]\n"
    "buffer_load_dwordx4 v210, s[36:39], s52 offen offset:384 lds\n"
    "v_mfma_f32_16x16x32_bf16 a[100:103], a[236:239], v[180:183], a[100:103]\n"
    "s_add_u32 s52, s52, 16384\n"
    "v_exp_f32_e64 v193, v193\n"
    "v_mfma_f32_16x16x32_bf16 a[36:39], a[236:239], v[176:179], a[36:39]\n"
    "ds_read_b64_tr_b16 a[228:229], v207 offset:33792\n"
    "ds_read_b64_tr_b16 a[230:231], v207 offset:41984\n"
    "v_mfma_f32_16x16x32_bf16 a[36:39], a[232:235], v[164:167], a[36:39]\n"
    "s_waitcnt lgkmcnt(8)\n"
    "ds_read_b64_tr_b16 a[232:233], v205 offset:33792\n"
    "v_add_f32 v238, v238, v190\n"
    "v_mfma_f32_16x16x32_bf16 a[40:43], a[240:243], v[164:167], a[40:43]\n"
    "ds_read_b64_tr_b16 a[234:235], v205 offset:41984\n"
    "v_fma_f32 v197, v81, s58, v233\n"
    "v_mfma_f32_16x16x32_bf16 a[40:43], a[244:247], v[176:179], a[40:43]\n"
    "v_exp_f32_e64 v194, v194\n"
    "v_mfma_f32_16x16x32_bf16 a[104:107], a[244:247], v[180:183], a[104:107]\n"
    "ds_read_b64_tr_b16 a[236:237], v209 offset:33792\n"
    "v_add_f32_e64 v238, v238, v191\n"
    "v_mfma_f32_16x16x32_bf16 a[104:107], a[240:243], v[168:171], a[104:107]\n"
    "s_add_u32 m0, s54, 16384\n"
    "ds_read_b64_tr_b16 a[238:239], v209 offset:41984\n"
    "v_cvt_pk_bf16_f32 v147, v190, v191\n"
    "v_mfma_f32_16x16x32_bf16 v[40:43], a[240:243], v[172:175], v[40:43]\n"
    "s_waitcnt lgkmcnt(8)\n"
    "buffer_load_dwordx4 v210, s[36:39], s52 offen offset:0 lds\n"
    "v_mfma_f32_16x16x32_bf16 v[44:47], a[248:251], v[172:175], v[44:47]\n"
    "s_nop 0\n"
    "ds_read_b64_tr_b16 a[240:241], v203 offset:34304\n"
    "v_fma_f32 v198, v82, s58, v233\n"
    "v_mfma_f32_16x16x32_bf16 a[108:111], a[248:251], v[168:171], a[108:111]\n"
    "v_exp_f32_e64 v195, v195\n"
    "v_mfma_f32_16x16x32_bf16 a[108:111], a[252:255], v[180:183], a[108:111]\n"
    "ds_read_b64_tr_b16 a[242:243], v203 offset:42496\n"
    "ds_read_b64_tr_b16 a[244:245], v207 offset:34304\n"
    "v_mfma_f32_16x16x32_bf16 a[44:47], a[252:255], v[176:179], a[44:47]\n"
    "s_nop 0\n"
    "ds_read_b64_tr_b16 a[246:247], v207 offset:42496\n"
    "v_fma_f32 v199, v83, s58, v233\n"
    "v_mfma_f32_16x16x32_bf16 a[44:47], a[248:251], v[164:167], a[44:47]\n"
    "s_waitcnt lgkmcnt(8)\n"
    "ds_read_b64_tr_b16 a[248:249], v205 offset:34304\n"
    "ds_read_b64_tr_b16 a[250:251], v205 offset:42496\n"
    "v_mfma_f32_16x16x32_bf16 a[48:51], a[224:227], v[164:167], a[48:51]\n"
    "ds_read_b64_tr_b16 a[252:253], v209 offset:34304\n"
    "ds_read_b64_tr_b16 a[254:255], v209 offset:42496\n"
    "v_mfma_f32_16x16x32_bf16 a[48:51], a[228:231], v[176:179], a[48:51]\n"
    "v_exp_f32_e64 v196, v196\n"
    "v_mfma_f32_16x16x32_bf16 a[112:115], a[228:231], v[180:183], a[112:115]\n"
    "s_add_u32 m0, s54, 17280\n"
    "v_add_f32_e64 v239, v192, v193\n"
    "v_cvt_pk_bf16_f32 v148, v192, v193\n"
    "v_mfma_f32_16x16x32_bf16 a[112:115], a[224:227], v[168:171], a[112:115]\n"
    "s_nop 0\n"
    "buffer_load_dwordx4 v210, s[36:39], s52 offen offset:128 lds\n"
    "v_mfma_f32_16x16x32_bf16 v[48:51], a[224:227], v[172:175], v[48:51]\n"
    "s_waitcnt lgkmcnt(8)\n"
    "ds_read_b128 a[224:227], v200 offset:32768\n"
    "v_fma_f32 v184, v72, s58, v234\n"
    "v_mfma_f32_16x16x32_bf16 v[52:55], a[232:235], v[172:175], v[52:55]\n"
    "v_exp_f32_e64 v197, v197\n"
    "v_mfma_f32_16x16x32_bf16 a[116:119], a[232:235], v[168:171], a[116:119]\n"
    "ds_read_b128 a[228:231], v200 offset:33280\n"
    "v_add_f32_e64 v239, v239, v194\n"
    "v_mfma_f32_16x16x32_bf16 a[116:119], a[236:239], v[180:183], a[116:119]\n"
    "v_fma_f32 v185, v73, s58, v234\n"
    "v_add_f32_e64 v239, v239, v195\n"
    "v_mfma_f32_16x16x32_bf16 a[52:55], a[236:239], v[176:179], a[52:55]\n"
    "v_exp_f32 v198, v198\n"
    "v_mfma_f32_16x16x32_bf16 a[52:55], a[232:235], v[164:167], a[52:55]\n"
    "s_waitcnt lgkmcnt(6)\n"
    "ds_read_b128 a[232:235], v200 offset:33792\n"
    "v_cvt_pk_bf16_f32 v149, v194, v195\n"
    "v_mfma_f32_16x16x32_bf16 a[56:59], a[240:243], v[164:167], a[56:59]\n"
    "ds_read_b128 a[236:239], v200 offset:34304\n"
    "v_fma_f32 v186, v74, s58, v234\n"
    "v_mfma_f32_16x16x32_bf16 a[56:59], a[244:247], v[176:179], a[56:59]\n"
    "s_add_u32 m0, s54, 18176\n"
    "v_exp_f32_e64 v199, v199\n"
    "v_mfma_f32_16x16x32_bf16 a[120:123], a[244:247], v[180:183], a[120:123]\n"
    "buffer_load_dwordx4 v210, s[36:39], s52 offen offset:256 lds\n"
    "v_mfma_f32_16x16x32_bf16 a[120:123], a[240:243], v[168:171], a[120:123]\n"
    "v_add_f32_e64 v239, v239, v196\n"
    "v_fma_f32 v187, v75, s58, v234\n"
    "v_mfma_f32_16x16x32_bf16 v[56:59], a[240:243], v[172:175], v[56:59]\n"
    "s_waitcnt vmcnt(9) lgkmcnt(4)\n"
    "v_exp_f32 v184, v184\n"
    "v_mfma_f32_16x16x32_bf16 v[60:63], a[248:251], v[172:175], v[60:63]\n"
    "s_barrier\n"
    "ds_read_b128 a[240:243], v200 offset:34816\n"
    "v_add_f32 v239, v239, v197\n"
    "v_mfma_f32_16x16x32_bf16 a[124:127], a[248:251], v[168:171], a[124:127]\n"
    "ds_read_b128 a[244:247], v200 offset:35328\n"
    "v_cvt_pk_bf16_f32 v150, v196, v197\n"
    "v_mfma_f32_16x16x32_bf16 a[124:127], a[252:255], v[180:183], a[124:127]\n"
    "v_fma_f32 v188, v84, s58, v234\n"
    "v_add_f32_e64 v239, v239, v198\n"
    "v_mfma_f32_16x16x32_bf16 a[60:63], a[252:255], v[176:179], a[60:63]\n"
    "v_exp_f32 v185, v185\n"
    "v_mfma_f32_16x16x32_bf16 a[60:63], a[248:251], v[164:167], a[60:63]\n"
    "s_waitcnt lgkmcnt(4)\n"
    "ds_read_b128 a[248:251], v200 offset:35840\n"
    "v_fma_f32 v189, v85, s58, v234\n"
    "v_mfma_f32_16x16x32_bf16 v[104:107], a[224:227], a[128:131], 0\n"
    "s_add_u32 m0, s54, 19072\n"
    "v_exp_f32_e64 v186, v186\n"
    "v_mfma_f32_16x16x32_bf16 v[104:107], a[228:231], a[132:135], v[104:107]\n"
    "buffer_load_dwordx4 v210, s[36:39], s52 offen offset:384 lds\n"
    "v_mfma_f32_16x16x32_bf16 v[108:111], a[228:231], a[164:167], 0\n"
    "s_add_u32 s52, s52, 16384\n"
    "ds_read_b128 a[252:255], v200 offset:36352\n"
    "v_add_f32_e64 v239, v239, v199\n"
    "v_mfma_f32_16x16x32_bf16 v[108:111], a[224:227], a[160:163], v[108:111]\n"
    "v_cvt_pk_bf16_f32 v151, v198, v199\n"
    "v_fma_f32 v190, v86, s58, v234\n"
    "v_mfma_f32_16x16x32_bf16 v[112:115], a[224:227], a[192:195], 0\n"
    "v_exp_f32 v187, v187\n"
    "v_mfma_f32_16x16x32_bf16 v[112:115], a[228:231], a[196:199], v[112:115]\n"
    "s_waitcnt lgkmcnt(4)\n"
    "ds_read_b128 a[224:227], v200 offset:40960\n"
    "v_fma_f32 v191, v87, s58, v234\n"
    "v_mfma_f32_16x16x32_bf16 v[112:115], a[232:235], a[200:203], v[112:115]\n"
    "v_exp_f32_e64 v188, v188\n"
    "v_mfma_f32_16x16x32_bf16 v[112:115], a[236:239], a[204:207], v[112:115]\n"
    "ds_read_b128 a[228:231], v200 offset:41472\n"
    "v_add_f32_e64 v240, v184, v185\n"
    "v_mfma_f32_16x16x32_bf16 v[108:111], a[236:239], a[172:175], v[108:111]\n"
    "s_add_u32 m0, s54, 98304\n"
    "v_cvt_pk_bf16_f32 v152, v184, v185\n"
    "v_fma_f32 v192, v88, s58, v232\n"
    "v_mfma_f32_16x16x32_bf16 v[108:111], a[232:235], a[168:171], v[108:111]\n"
    "buffer_load_dwordx4 v211, s[40:43], s53 offen offset:0 lds\n"
    "v_mfma_f32_16x16x32_bf16 v[104:107], a[232:235], a[136:139], v[104:107]\n"
    "v_exp_f32 v189, v189\n"
    "v_mfma_f32_16x16x32_bf16 v[104:107], a[236:239], a[140:143], v[104:107]\n"
    "s_waitcnt lgkmcnt(4)\n"
    "ds_read_b128 a[232:235], v200 offset:41984\n"
    "v_add_f32_e64 v240, v240, v186\n"
    "v_mfma_f32_16x16x32_bf16 v[104:107], a[240:243], a[144:147], v[104:107]\n"
    "ds_read_b128 a[236:239], v200 offset:42496\n"
    "v_fma_f32 v193, v89, s58, v232\n"
    "v_mfma_f32_16x16x32_bf16 v[104:107], a[244:247], a[148:151], v[104:107]\n"
    "v_exp_f32_e64 v190, v190\n"
    "v_mfma_f32_16x16x32_bf16 v[108:111], a[244:247], a[180:183], v[108:111]\n"
    "v_add_f32_e64 v240, v240, v187\n"
    "v_cvt_pk_bf16_f32 v153, v186, v187\n"
    "v_mfma_f32_16x16x32_bf16 v[108:111], a[240:243], a[176:179], v[108:111]\n"
    "v_fma_f32 v194, v90, s58, v232\n"
    "v_add_f32_e64 v240, v240, v188\n"
    "v_mfma_f32_16x16x32_bf16 v[112:115], a[240:243], a[208:211], v[112:115]\n"
    "s_add_u32 m0, s54, 99200\n"
    "v_exp_f32 v191, v191\n"
    "v_mfma_f32_16x16x32_bf16 v[112:115], a[244:247], a[212:215], v[112:115]\n"
    "s_waitcnt lgkmcnt(4)\n"
    "buffer_load_dwordx4 v211, s[40:43], s53 offen offset:128 lds\n"
    "v_mfma_f32_16x16x32_bf16 v[112:115], a[248:251], a[216:219], v[112:115]\n"
    "ds_read_b128 a[240:243], v200 offset:43008\n"
    "v_fma_f32 v195, v91, s58, v232\n"
    "v_mfma_f32_16x16x32_bf16 v[112:115], a[252:255], a[220:223], v[112:115]\n"
    "v_exp_f32_e64 v192, v192\n"
    "v_mfma_f32_16x16x32_bf16 v[108:111], a[252:255], a[188:191], v[108:111]\n"
    "ds_read_b128 a[244:247], v200 offset:43520\n"
    "v_add_f32_e64 v240, v240, v189\n"
    "v_mfma_f32_16x16x32_bf16 v[108:111], a[248:251], a[184:187], v[108:111]\n"
    "v_cvt_pk_bf16_f32 v154, v188, v189\n"
    "v_fma_f32 v196, v96, s58, v232\n"
    "v_mfma_f32_16x16x32_bf16 v[104:107], a[248:251], a[152:155], v[104:107]\n"
    "v_exp_f32 v193, v193\n"
    "v_mfma_f32_16x16x32_bf16 v[104:107], a[252:255], a[156:159], v[104:107]\n"
    "s_waitcnt lgkmcnt(4)\n"
    "ds_read_b128 a[248:251], v200 offset:44032\n"
    "v_add_f32_e64 v240, v240, v190\n"
    "v_mfma_f32_16x16x32_bf16 v[116:119], a[224:227], a[128:131], 0\n"
    "ds_read_b128 a[252:255], v200 offset:44544\n"
    "v_fma_f32 v197, v97, s58, v232\n"
    "v_mfma_f32_16x16x32_bf16 v[116:119], a[228:231], a[132:135], v[116:119]\n"
    "s_add_u32 m0, s54, 100096\n"
    "v_exp_f32_e64 v194, v194\n"
    "v_mfma_f32_16x16x32_bf16 v[120:123], a[228:231], a[164:167], 0\n"
    "buffer_load_dwordx4 v211, s[40:43], s53 offen offset:256 lds\n"
    "v_mfma_f32_16x16x32_bf16 v[120:123], a[224:227], a[160:163], v[120:123]\n"
    "v_add_f32_e64 v240, v240, v191\n"
    "v_cvt_pk_bf16_f32 v155, v190, v191\n"
    "v_mfma_f32_16x16x32_bf16 v[124:127], a[224:227], a[192:195], 0\n"
    "ds_read_b128 a[224:227], v201 offset:32768\n"
    "v_fma_f32 v198, v98, s58, v232\n"
    "v_mfma_f32_16x16x32_bf16 v[124:127], a[228:231], a[196:199], v[124:127]\n"
    "s_waitcnt lgkmcnt(5)\n"
    "v_exp_f32 v195, v195\n"
    "v_mfma_f32_16x16x32_bf16 v[124:127], a[232:235], a[200:203], v[124:127]\n"
    "ds_read_b128 a[228:231], v201 offset:33280\n"
    "v_add_f32_e64 v238, v238, v192\n"
    "v_mfma_f32_16x16x32_bf16 v[124:127], a[236:239], a[204:207], v[124:127]\n"
    "v_fma_f32 v199, v99, s58, v232\n"
    "v_add_f32_e64 v238, v238, v193\n"
    "v_mfma_f32_16x16x32_bf16 v[120:123], a[236:239], a[172:175], v[120:123]\n"
    "v_exp_f32_e64 v196, v196\n"
    "v_mfma_f32_16x16x32_bf16 v[120:123], a[232:235], a[168:171], v[120:123]\n"
    "v_cvt_pk_bf16_f32 v156, v192, v193\n"
    "v_fma_f32 v184, v92, s58, v233\n"
    "v_mfma_f32_16x16x32_bf16 v[116:119], a[232:235], a[136:139], v[116:119]\n"
    "s_add_u32 m0, s54, 100992\n"
    "v_exp_f32 v197, v197\n"
    "v_mfma_f32_16x16x32_bf16 v[116:119], a[236:239], a[140:143], v[116:119]\n"
    "s_waitcnt lgkmcnt(4)\n"
    "buffer_load_dwordx4 v211, s[40:43], s53 offen offset:384 lds\n"
    "v_mfma_f32_16x16x32_bf16 v[116:119], a[240:243], a[144:147], v[116:119]\n"
    "s_add_u32 s53, s53, 16384\n"
    "ds_read_b128 a[232:235], v201 offset:33792\n"
    "v_add_f32_e64 v238, v238, v194\n"
    "v_mfma_f32_16x16x32_bf16 v[116:119], a[244:247], a[148:151], v[116:119]\n"
    "ds_read_b128 a[236:239], v201 offset:34304\n"
    "v_fma_f32 v185, v93, s58, v233\n"
    "v_mfma_f32_16x16x32_bf16 v[120:123], a[244:247], a[180:183], v[120:123]\n"
    "v_exp_f32_e64 v198, v198\n"
    "v_mfma_f32_16x16x32_bf16 v[120:123], a[240:243], a[176:179], v[120:123]\n"
    "v_add_f32_e64 v238, v238, v195\n"
    "v_cvt_pk_bf16_f32 v157, v194, v195\n"
    "v_mfma_f32_16x16x32_bf16 v[124:127], a[240:243], a[208:211], v[124:127]\n"
    "ds_read_b128 a[240:243], v201 offset:34816\n"
    "v_fma_f32 v186, v94, s58, v233\n"
    "v_mfma_f32_16x16x32_bf16 v[124:127], a[244:247], a[212:215], v[124:127]\n"
    "s_waitcnt lgkmcnt(5)\n"
    "v_exp_f32 v199, v199\n"
    "v_mfma_f32_16x16x32_bf16 v[124:127], a[248:251], a[216:219], v[124:127]\n"
    "ds_read_b128 a[244:247], v201 offset:35328\n"
    "v_add_f32_e64 v238, v238, v196\n"
    "v_mfma_f32_16x16x32_bf16 v[124:127], a[252:255], a[220:223], v[124:127]\n"
    "s_add_u32 m0, s54, 114688\n"
    "v_fma_f32 v187, v95, s58, v233\n"
    "v_add_f32_e64 v238, v238, v197\n"
    "v_mfma_f32_16x16x32_bf16 v[120:123], a[252:255], a[188:191], v[120:123]\n"
    "buffer_load_dwordx4 v211, s[40:43], s53 offen offset:0 lds\n"
    "v_mfma_f32_16x16x32_bf16 v[120:123], a[248:251], a[184:187], v[120:123]\n"
    "v_exp_f32_e64 v184, v184\n"
    "v_mfma_f32_16x16x32_bf16 v[116:119], a[248:251], a[152:155], v[116:119]\n"
    "ds_read_b128 a[248:251], v201 offset:35840\n"
    "v_cvt_pk_bf16_f32 v158, v196, v197\n"
    "v_mfma_f32_16x16x32_bf16 v[116:119], a[252:255], a[156:159], v[116:119]\n"
    "s_waitcnt lgkmcnt(5)\n"
    "ds_read_b128 a[252:255], v201 offset:36352\n"
    "v_fma_f32 v188, v100, s58, v233\n"
    "v_mfma_f32_16x16x32_bf16 v[128:131], a[224:227], a[128:131], 0\n"
    "v_exp_f32 v185, v185\n"
    "v_mfma_f32_16x16x32_bf16 v[128:131], a[228:231], a[132:135], v[128:131]\n"
    "v_add_f32_e64 v238, v238, v198\n"
    "v_fma_f32 v189, v101, s58, v233\n"
    "v_mfma_f32_16x16x32_bf16 v[132:135], a[228:231], a[164:167], 0\n"
    "v_exp_f32 v186, v186\n"
    "v_mfma_f32_16x16x32_bf16 v[132:135], a[224:227], a[160:163], v[132:135]\n"
    "s_waitcnt lgkmcnt(4)\n"
    "ds_read_b128 a[224:227], v201 offset:40960\n"
    "ds_read_b128 a[228:231], v201 offset:41472\n"
    "v_mfma_f32_16x16x32_bf16 v[132:135], a[232:235], a[168:171], v[132:135]\n"
    "s_add_u32 m0, s54, 115584\n"
    "v_add_f32_e64 v238, v238, v199\n"
    "v_cvt_pk_bf16_f32 v159, v198, v199\n"
    "v_mfma_f32_16x16x32_bf16 v[132:135], a[236:239], a[172:175], v[132:135]\n"
    "buffer_load_dwordx4 v211, s[40:43], s53 offen offset:128 lds\n"
    "v_mfma_f32_16x16x32_bf16 v[128:131], a[236:239], a[140:143], v[128:131]\n"
    "v_fma_f32 v190, v102, s58, v233\n"
    "v_add_f32_e64 v239, v239, v184\n"
    "v_mfma_f32_16x16x32_bf16 v[128:131], a[232:235], a[136:139], v[128:131]\n"
    "s_waitcnt lgkmcnt(4)\n"
    "v_exp_f32 v187, v187\n"
    "v_mfma_f32_16x16x32_bf16 v[128:131], a[240:243], a[144:147], v[128:131]\n"
    "ds_read_b128 a[232:235], v201 offset:41984\n"
    "ds_read_b128 a[236:239], v201 offset:42496\n"
    "v_mfma_f32_16x16x32_bf16 v[128:131], a[244:247], a[148:151], v[128:131]\n"
    "v_fma_f32 v191, v103, s58, v233\n"
    "v_add_f32_e64 v239, v239, v185\n"
    "v_mfma_f32_16x16x32_bf16 v[132:135], a[244:247], a[180:183], v[132:135]\n"
    "v_exp_f32 v188, v188\n"
    "v_mfma_f32_16x16x32_bf16 v[132:135], a[240:243], a[176:179], v[132:135]\n"
    "s_waitcnt lgkmcnt(4)\n"
    "ds_read_b128 a[240:243], v201 offset:43008\n"
    "ds_read_b128 a[244:247], v201 offset:43520\n"
    "v_mfma_f32_16x16x32_bf16 v[132:135], a[248:251], a[184:187], v[132:135]\n"
    "s_add_u32 m0, s54, 116480\n"
    "v_cvt_pk_bf16_f32 v160, v184, v185\n"
    "v_add_f32_e64 v239, v239, v186\n"
    "v_mfma_f32_16x16x32_bf16 v[132:135], a[252:255], a[188:191], v[132:135]\n"
    "buffer_load_dwordx4 v211, s[40:43], s53 offen offset:256 lds\n"
    "v_mfma_f32_16x16x32_bf16 v[128:131], a[252:255], a[156:159], v[128:131]\n"
    "v_exp_f32 v189, v189\n"
    "v_mfma_f32_16x16x32_bf16 v[128:131], a[248:251], a[152:155], v[128:131]\n"
    "s_waitcnt lgkmcnt(4)\n"
    "ds_read_b128 a[248:251], v201 offset:44032\n"
    "ds_read_b128 a[252:255], v201 offset:44544\n"
    "v_mfma_f32_16x16x32_bf16 v[136:139], a[224:227], a[128:131], 0\n"
    "v_exp_f32_e64 v190, v190\n"
    "v_mfma_f32_16x16x32_bf16 v[136:139], a[228:231], a[132:135], v[136:139]\n"
    "v_add_f32_e64 v239, v239, v187\n"
    "v_cvt_pk_bf16_f32 v161, v186, v187\n"
    "v_mfma_f32_16x16x32_bf16 v[140:143], a[228:231], a[164:167], 0\n"
    "v_exp_f32 v191, v191\n"
    "v_mfma_f32_16x16x32_bf16 v[140:143], a[224:227], a[160:163], v[140:143]\n"
    "s_waitcnt lgkmcnt(4)\n"
    "ds_read_b64_tr_b16 a[224:225], v202 offset:0\n"
    "ds_read_b64_tr_b16 a[226:227], v202 offset:8192\n"
    "v_mfma_f32_16x16x32_bf16 v[140:143], a[232:235], a[168:171], v[140:143]\n"
    "ds_read_b64_tr_b16 a[228:229], v206 offset:0\n"
    "ds_read_b64_tr_b16 a[230:231], v206 offset:8192\n"
    "v_mfma_f32_16x16x32_bf16 v[140:143], a[236:239], a[172:175], v[140:143]\n"
    "s_add_u32 m0, s54, 117376\n"
    "v_add_f32 v239, v239, v188\n"
    "v_add_f32 v239, v239, v189\n"
    "v_mfma_f32_16x16x32_bf16 v[136:139], a[236:239], a[140:143], v[136:139]\n"
    "s_nop 0\n"
    "buffer_load_dwordx4 v211, s[40:43], s53 offen offset:384 lds\n"
    "v_mfma_f32_16x16x32_bf16 v[136:139], a[232:235], a[136:139], v[136:139]\n"
    "s_waitcnt lgkmcnt(6)\n"
    "ds_read_b64_tr_b16 a[232:233], v204 offset:0\n"
    "ds_read_b64_tr_b16 a[234:235], v204 offset:8192\n"
    "v_mfma_f32_16x16x32_bf16 v[136:139], a[240:243], a[144:147], v[136:139]\n"
    "s_add_u32 s53, s53, 16384\n"
    "ds_read_b64_tr_b16 a[236:237], v208 offset:0\n"
    "ds_read_b64_tr_b16 a[238:239], v208 offset:8192\n"
    "v_mfma_f32_16x16x32_bf16 v[136:139], a[244:247], a[148:151], v[136:139]\n"
    "v_cvt_pk_bf16_f32 v162, v188, v189\n"
    "v_add_f32_e64 v239, v239, v190\n"
    "v_mfma_f32_16x16x32_bf16 v[140:143], a[244:247], a[180:183], v[140:143]\n"
    "v_add_f32_e64 v239, v239, v191\n"
    "v_cvt_pk_bf16_f32 v163, v190, v191\n"
    "v_mfma_f32_16x16x32_bf16 v[140:143], a[240:243], a[176:179], v[140:143]\n"
    "s_waitcnt vmcnt(8) lgkmcnt(8)\n"
    "ds_read_b64_tr_b16 a[240:241], v202 offset:512\n"
    "v_max3_f32 v243, v238, v239, v240\n"
    "v_mfma_f32_16x16x32_bf16 v[140:143], a[248:251], a[184:187], v[140:143]\n"
    "s_barrier\n"
    "ds_read_b64_tr_b16 a[242:243], v202 offset:8704\n"
    "ds_read_b64_tr_b16 a[244:245], v206 offset:512\n"
    "v_mfma_f32_16x16x32_bf16 v[140:143], a[252:255], a[188:191], v[140:143]\n"
    "ds_read_b64_tr_b16 a[246:247], v206 offset:8704\n"
    "v_cmp_lt_f32 vcc, s59, v243\n"
    "v_mfma_f32_16x16x32_bf16 v[136:139], a[252:255], a[156:159], v[136:139]\n"
    "v_mfma_f32_16x16x32_bf16 v[136:139], a[248:251], a[152:155], v[136:139]\n"
    "s_or_b64 s[14:15], vcc, s[12:13]\n"
    "Lmid_%=:\n"
    "Lpad3_%=:\n"
    "s_cbranch_scc1 Lspec_%=\n"
    "Lue_%=:\n"
    "s_waitcnt lgkmcnt(8)\n"
    "v_mfma_f32_16x16x32_bf16 a[0:3], a[224:227], v[144:147], a[0:3]\n"
    "ds_read_b64_tr_b16 a[248:249], v204 offset:512\n"
    "v_add_f32_e64 v236, v236, v238\n"
    "v_mfma_f32_16x16x32_bf16 a[0:3], a[228:231], v[156:159], a[0:3]\n"
    "s_add_u32 m0, s54, 32768\n"
    "ds_read_b64_tr_b16 a[250:251], v204 offset:8704\n"
    "v_add_f32_e64 v237, v237, v239\n"
    "v_mfma_f32_16x16x32_bf16 a[64:67], a[228:231], v[160:163], a[64:67]\n"
    "buffer_load_dwordx4 v210, s[36:39], s52 offen offset:0 lds\n"
    "v_mfma_f32_16x16x32_bf16 a[64:67], a[224:227], v[148:151], a[64:67]\n"
    "ds_read_b64_tr_b16 a[252:253], v208 offset:512\n"
    "ds_read_b64_tr_b16 a[254:255], v208 offset:8704\n"
    "v_mfma_f32_16x16x32_bf16 v[0:3], a[224:227], v[152:155], v[0:3]\n"
    "s_waitcnt lgkmcnt(8)\n"
    "ds_read_b64_tr_b16 a[224:225], v202 offset:1024\n"
    "v_add_f32 v235, v235, v240\n"
    "v_mfma_f32_16x16x32_bf16 v[4:7], a[232:235], v[152:155], v[4:7]\n"
    "ds_read_b64_tr_b16 a[226:227], v202 offset:9216\n"
    "v_fma_f32 v184, v104, s58, v232\n"
    "v_mfma_f32_16x16x32_bf16 a[68:71], a[232:235], v[148:151], a[68:71]\n"
    "ds_read_b64_tr_b16 a[228:229], v206 offset:1024\n"
    "v_fma_f32 v185, v105, s58, v232\n"
    "v_mfma_f32_16x16x32_bf16 a[68:71], a[236:239], v[160:163], a[68:71]\n"
    "ds_read_b64_tr_b16 a[230:231], v206 offset:9216\n"
    "v_fma_f32 v186, v106, s58, v232\n"
    "v_mfma_f32_16x16x32_bf16 a[4:7], a[236:239], v[156:159], a[4:7]\n"
    "v_fma_f32 v187, v107, s58, v232\n"
    "v_fma_f32 v188, v116, s58, v232\n"
    "v_mfma_f32_16x16x32_bf16 a[4:7], a[232:235], v[144:147], a[4:7]\n"
    "s_waitcnt lgkmcnt(8)\n"
    "v_exp_f32 v184, v184\n"
    "v_mfma_f32_16x16x32_bf16 a[8:11], a[240:243], v[144:147], a[8:11]\n"
    "v_exp_f32_e64 v185, v185\n"
    "v_mfma_f32_16x16x32_bf16 a[8:11], a[244:247], v[156:159], a[8:11]\n"
    "s_add_u32 m0, s54, 33664\n"
    "ds_read_b64_tr_b16 a[232:233], v204 offset:1024\n"
    "ds_read_b64_tr_b16 a[234:235], v204 offset:9216\n"
    "v_mfma_f32_16x16x32_bf16 a[72:75], a[244:247], v[160:163], a[72:75]\n"
    "buffer_load_dwordx4 v210, s[36:39], s52 offen offset:128 lds\n"
    "v_mfma_f32_16x16x32_bf16 a[72:75], a[240:243], v[148:151], a[72:75]\n"
    "ds_read_b64_tr_b16 a[236:237], v208 offset:1024\n"
    "ds_read_b64_tr_b16 a[238:239], v208 offset:9216\n"
    "v_mfma_f32_16x16x32_bf16 v[8:11], a[240:243], v[152:155], v[8:11]\n"
    "s_waitcnt lgkmcnt(8)\n"
    "ds_read_b64_tr_b16 a[240:241], v202 offset:1536\n"
    "v_fma_f32 v189, v117, s58, v232\n"
    "v_mfma_f32_16x16x32_bf16 v[12:15], a[248:251], v[152:155], v[12:15]\n"
    "v_exp_f32 v186, v186\n"
    "v_mfma_f32_16x16x32_bf16 a[76:79], a[248:251], v[148:151], a[76:79]\n"
    "ds_read_b64_tr_b16 a[242:243], v202 offset:9728\n"
    "v_fma_f32 v190, v118, s58, v232\n"
    "v_mfma_f32_16x16x32_bf16 a[76:79], a[252:255], v[160:163], a[76:79]\n"
    "v_exp_f32_e64 v187, v187\n"
    "v_mfma_f32_16x16x32_bf16 a[12:15], a[252:255], v[156:159], a[12:15]\n"
    "ds_read_b64_tr_b16 a[244:245], v206 offset:1536\n"
    "ds_read_b64_tr_b16 a[246:247], v206 offset:9728\n"
    "v_mfma_f32_16x16x32_bf16 a[12:15], a[248:251], v[144:147], a[12:15]\n"
    "s_waitcnt lgkmcnt(8)\n"
    "ds_read_b64_tr_b16 a[248:249], v204 offset:1536\n"
    "v_fma_f32 v191, v119, s58, v232\n"
    "v_mfma_f32_16x16x32_bf16 a[16:19], a[224:227], v[144:147], a[16:19]\n"
    "v_exp_f32 v188, v188\n"
    "v_mfma_f32_16x16x32_bf16 a[16:19], a[228:231], v[156:159], a[16:19]\n"
    "s_add_u32 m0, s54, 34560\n"
    "ds_read_b64_tr_b16 a[250:251], v204 offset:9728\n"
    "v_add_f32_e64 v238, v184, v185\n"
    "v_mfma_f32_16x16x32_bf16 a[80:83], a[228:231], v[160:163], a[80:83]\n"
    "buffer_load_dwordx4 v210, s[36:39], s52 offen offset:256 lds\n"
    "v_mfma_f32_16x16x32_bf16 a[80:83], a[224:227], v[148:151], a[80:83]\n"
    "s_nop 0\n"
    "ds_read_b64_tr_b16 a[252:253], v208 offset:1536\n"
    "ds_read_b64_tr_b16 a[254:255], v208 offset:9728\n"
    "v_mfma_f32_16x16x32_bf16 v[16:19], a[224:227], v[152:155], v[16:19]\n"
    "s_waitcnt lgkmcnt(8)\n"
    "ds_read_b64_tr_b16 a[224:225], v203 offset:0\n"
    "v_cvt_pk_bf16_f32 v164, v184, v185\n"
    "v_mfma_f32_16x16x32_bf16 v[20:23], a[232:235], v[152:155], v[20:23]\n"
    "ds_read_b64_tr_b16 a[226:227], v203 offset:8192\n"
    "v_fma_f32 v192, v108, s58, v233\n"
    "v_mfma_f32_16x16x32_bf16 a[84:87], a[232:235], v[148:151], a[84:87]\n"
    "v_exp_f32_e64 v189, v189\n"
    "v_mfma_f32_16x16x32_bf16 a[84:87], a[236:239], v[160:163], a[84:87]\n"
    "ds_read_b64_tr_b16 a[228:229], v207 offset:0\n"
    "ds_read_b64_tr_b16 a[230:231], v207 offset:8192\n"
    "v_mfma_f32_16x16x32_bf16 a[20:23], a[236:239], v[156:159], a[20:23]\n"
    "v_add_f32_e64 v238, v238, v186\n"
    "v_fma_f32 v193, v109, s58, v233\n"
    "v_mfma_f32_16x16x32_bf16 a[20:23], a[232:235], v[144:147], a[20:23]\n"
    "s_waitcnt lgkmcnt(8)\n"
    "v_exp_f32 v190, v190\n"
    "v_mfma_f32_16x16x32_bf16 a[24:27], a[240:243], v[144:147], a[24:27]\n"
    "ds_read_b64_tr_b16 a[232:233], v205 offset:0\n"
    "v_add_f32_e64 v238, v238, v187\n"
    "v_mfma_f32_16x16x32_bf16 a[24:27], a[244:247], v[156:159], a[24:27]\n"
    "s_add_u32 m0, s54, 35456\n"
    "ds_read_b64_tr_b16 a[234:235], v205 offset:8192\n"
    "v_cvt_pk_bf16_f32 v165, v186, v187\n"
    "v_mfma_f32_16x16x32_bf16 a[88:91], a[244:247], v[160:163], a[88:91]\n"
    "buffer_load_dwordx4 v210, s[36:39], s52 offen offset:384 lds\n"
    "v_mfma_f32_16x16x32_bf16 a[88:91], a[240:243], v[148:151], a[88:91]\n"
    "s_add_u32 s52, s52, 16384\n"
    "ds_read_b64_tr_b16 a[236:237], v209 offset:0\n"
    "ds_read_b64_tr_b16 a[238:239], v209 offset:8192\n"
    "v_mfma_f32_16x16x32_bf16 v[24:27], a[240:243], v[152:155], v[24:27]\n"
    "s_waitcnt lgkmcnt(8)\n"
    "ds_read_b64_tr_b16 a[240:241], v203 offset:512\n"
    "v_fma_f32 v194, v110, s58, v233\n"
    "v_mfma_f32_16x16x32_bf16 v[28:31], a[248:251], v[152:155], v[28:31]\n"
    "v_exp_f32 v191, v191\n"
    "v_mfma_f32_16x16x32_bf16 a[92:95], a[248:251], v[148:151], a[92:95]\n"
    "ds_read_b64_tr_b16 a[242:243], v203 offset:8704\n"
    "v_add_f32_e64 v238, v238, v188\n"
    "v_mfma_f32_16x16x32_bf16 a[92:95], a[252:255], v[160:163], a[92:95]\n"
    "ds_read_b64_tr_b16 a[244:245], v207 offset:512\n"
    "ds_read_b64_tr_b16 a[246:247], v207 offset:8704\n"
    "v_mfma_f32_16x16x32_bf16 a[28:31], a[252:255], v[156:159], a[28:31]\n"
    "v_fma_f32 v195, v111, s58, v233\n"
    "v_add_f32_e64 v238, v238, v189\n"
    "v_mfma_f32_16x16x32_bf16 a[28:31], a[248:251], v[144:147], a[28:31]\n"
    "s_waitcnt lgkmcnt(8)\n"
    "v_exp_f32 v192, v192\n"
    "v_mfma_f32_16x16x32_bf16 a[32:35], a[224:227], v[144:147], a[32:35]\n"
    "ds_read_b64_tr_b16 a[248:249], v205 offset:512\n"
    "v_cvt_pk_bf16_f32 v166, v188, v189\n"
    "v_mfma_f32_16x16x32_bf16 a[32:35], a[228:231], v[156:159], a[32:35]\n"
    "s_add_u32 m0, s54, 49152\n"
    "ds_read_b64_tr_b16 a[250:251], v205 offset:8704\n"
    "v_fma_f32 v196, v120, s58, v233\n"
    "v_mfma_f32_16x16x32_bf16 a[96:99], a[228:231], v[160:163], a[96:99]\n"
    "buffer_load_dwordx4 v210, s[36:39], s52 offen offset:0 lds\n"
    "v_mfma_f32_16x16x32_bf16 a[96:99], a[224:227], v[148:151], a[96:99]\n"
    "v_exp_f32 v193, v193\n"
    "v_mfma_f32_16x16x32_bf16 v[32:35], a[224:227], v[152:155], v[32:35]\n"
    "s_waitcnt lgkmcnt(6)\n"
    "ds_read_b64_tr_b16 a[252:253], v209 offset:512\n"
    "ds_read_b64_tr_b16 a[254:255], v209 offset:8704\n"
    "v_mfma_f32_16x16x32_bf16 v[36:39], a[232:235], v[152:155], v[36:39]\n"
    "ds_read_b64_tr_b16 a[224:225], v203 offset:1024\n"
    "v_add_f32_e64 v238, v238, v190\n"
    "v_mfma_f32_16x16x32_bf16 a[100:103], a[232:235], v[148:151], a[100:103]\n"
    "ds_read_b64_tr_b16 a[226:227], v203 offset:9216\n"
    "v_fma_f32 v197, v121, s58, v233\n"
    "v_mfma_f32_16x16x32_bf16 a[100:103], a[236:239], v[160:163], a[100:103]\n"
    "v_exp_f32_e64 v194, v194\n"
    "v_mfma_f32_16x16x32_bf16 a[36:39], a[236:239], v[156:159], a[36:39]\n"
    "ds_read_b64_tr_b16 a[228:229], v207 offset:1024\n"
    "ds_read_b64_tr_b16 a[230:231], v207 offset:9216\n"
    "v_mfma_f32_16x16x32_bf16 a[36:39], a[232:235], v[144:147], a[36:39]\n"
    "s_waitcnt lgkmcnt(8)\n"
    "ds_read_b64_tr_b16 a[232:233], v205 offset:1024\n"
    "v_add_f32 v238, v238, v191\n"
    "v_mfma_f32_16x16x32_bf16 a[40:43], a[240:243], v[144:147], a[40:43]\n"
    "ds_read_b64_tr_b16 a[234:235], v205 offset:9216\n"
    "v_cvt_pk_bf16_f32 v167, v190, v191\n"
    "v_mfma_f32_16x16x32_bf16 a[40:43], a[244:247], v[156:159], a[40:43]\n"
    "s_add_u32 m0, s54, 50048\n"
    "ds_read_b64_tr_b16 a[236:237], v209 offset:1024\n"
    "v_fma_f32 v198, v122, s58, v233\n"
    "v_mfma_f32_16x16x32_bf16 a[104:107], a[244:247], v[160:163], a[104:107]\n"
    "buffer_load_dwordx4 v210, s[36:39], s52 offen offset:128 lds\n"
    "v_mfma_f32_16x16x32_bf16 a[104:107], a[240:243], v[148:151], a[104:107]\n"
    "v_exp_f32 v195, v195\n"
    "v_mfma_f32_16x16x32_bf16 v[40:43], a[240:243], v[152:155], v[40:43]\n"
    "s_waitcnt lgkmcnt(7)\n"
    "ds_read_b64_tr_b16 a[238:239], v209 offset:9216\n"
    "v_fma_f32 v199, v123, s58, v233\n"
    "v_mfma_f32_16x16x32_bf16 v[44:47], a[248:251], v[152:155], v[44:47]\n"
    "v_exp_f32_e64 v196, v196\n"
    "v_mfma_f32_16x16x32_bf16 a[108:111], a[248:251], v[148:151], a[108:111]\n"
    "ds_read_b64_tr_b16 a[240:241], v203 offset:1536\n"
    "ds_read_b64_tr_b16 a[242:243], v203 offset:9728\n"
    "v_mfma_f32_16x16x32_bf16 a[108:111], a[252:255], v[160:163], a[108:111]\n"
    "ds_read_b64_tr_b16 a[244:245], v207 offset:1536\n"
    "ds_read_b64_tr_b16 a[246:247], v207 offset:9728\n"
    "v_mfma_f32_16x16x32_bf16 a[44:47], a[252:255], v[156:159], a[44:47]\n"
    "v_add_f32 v239, v192, v193\n"
    "v_cvt_pk_bf16_f32 v168, v192, v193\n"
    "v_mfma_f32_16x16x32_bf16 a[44:47], a[248:251], v[144:147], a[44:47]\n"
    "s_waitcnt lgkmcnt(8)\n"
    "ds_read_b64_tr_b16 a[248:249], v205 offset:1536\n"
    "ds_read_b64_tr_b16 a[250:251], v205 offset:9728\n"
    "v_mfma_f32_16x16x32_bf16 a[48:51], a[224:227], v[144:147], a[48:51]\n"
    "ds_read_b64_tr_b16 a[252:253], v209 offset:1536\n"
    "ds_read_b64_tr_b16 a[254:255], v209 offset:9728\n"
    "v_mfma_f32_16x16x32_bf16 a[48:51], a[228:231], v[156:159], a[48:51]\n"
    "s_add_u32 m0, s54, 50944\n"
    "v_fma_f32 v184, v112, s58, v234\n"
    "v_add_f32_e64 v239, v239, v194\n"
    "v_mfma_f32_16x16x32_bf16 a[112:115], a[228:231], v[160:163], a[112:115]\n"
    "buffer_load_dwordx4 v210, s[36:39], s52 offen offset:256 lds\n"
    "v_mfma_f32_16x16x32_bf16 a[112:115], a[224:227], v[148:151], a[112:115]\n"
    "v_exp_f32 v197, v197\n"
    "v_mfma_f32_16x16x32_bf16 v[48:51], a[224:227], v[152:155], v[48:51]\n"
    "s_waitcnt lgkmcnt(8)\n"
    "ds_read_b128 a[224:227], v200 offset:0\n"
    "v_fma_f32 v185, v113, s58, v234\n"
    "v_mfma_f32_16x16x32_bf16 v[52:55], a[232:235], v[152:155], v[52:55]\n"
    "v_exp_f32_e64 v198, v198\n"
    "v_mfma_f32_16x16x32_bf16 a[116:119], a[232:235], v[148:151], a[116:119]\n"
    "ds_read_b128 a[228:231], v200 offset:512\n"
    "v_add_f32_e64 v239, v239, v195\n"
    "v_mfma_f32_16x16x32_bf16 a[116:119], a[236:239], v[160:163], a[116:119]\n"
    "v_cvt_pk_bf16_f32 v169, v194, v195\n"
    "v_fma_f32 v186, v114, s58, v234\n"
    "v_mfma_f32_16x16x32_bf16 a[52:55], a[236:239], v[156:159], a[52:55]\n"
    "v_exp_f32 v199, v199\n"
    "v_mfma_f32_16x16x32_bf16 a[52:55], a[232:235], v[144:147], a[52:55]\n"
    "s_waitcnt lgkmcnt(6)\n"
    "ds_read_b128 a[232:235], v200 offset:1024\n"
    "v_add_f32_e64 v239, v239, v196\n"
    "v_mfma_f32_16x16x32_bf16 a[56:59], a[240:243], v[144:147], a[56:59]\n"
    "ds_read_b128 a[236:239], v200 offset:1536\n"
    "v_fma_f32 v187, v115, s58, v234\n"
    "v_mfma_f32_16x16x32_bf16 a[56:59], a[244:247], v[156:159], a[56:59]\n"
    "s_add_u32 m0, s54, 51840\n"
    "v_exp_f32_e64 v184, v184\n"
    "v_mfma_f32_16x16x32_bf16 a[120:123], a[244:247], v[160:163], a[120:123]\n"
    "buffer_load_dwordx4 v210, s[36:39], s52 offen offset:384 lds\n"
    "v_mfma_f32_16x16x32_bf16 a[120:123], a[240:243], v[148:151], a[120:123]\n"
    "s_add_u32 s52, s52, 16384\n"
    "v_add_f32 v239, v239, v197\n"
    "v_cvt_pk_bf16_f32 v170, v196, v197\n"
    "v_mfma_f32_16x16x32_bf16 v[56:59], a[240:243], v[152:155], v[56:59]\n"
    "s_waitcnt vmcnt(8) lgkmcnt(4)\n"
    "ds_read_b128 a[240:243], v200 offset:2048\n"
    "v_fma_f32 v188, v124, s58, v234\n"
    "v_mfma_f32_16x16x32_bf16 v[60:63], a[248:251], v[152:155], v[60:63]\n"
    "s_barrier\n"
    "v_exp_f32 v185, v185\n"
    "v_mfma_f32_16x16x32_bf16 a[124:127], a[248:251], v[148:151], a[124:127]\n"
    "ds_read_b128 a[244:247], v200 offset:2560\n"
    "v_add_f32_e64 v239, v239, v198\n"
    "v_mfma_f32_16x16x32_bf16 a[124:127], a[252:255], v[160:163], a[124:127]\n"
    "v_fma_f32 v189, v125, s58, v234\n"
    "v_add_f32_e64 v239, v239, v199\n"
    "v_mfma_f32_16x16x32_bf16 a[60:63], a[252:255], v[156:159], a[60:63]\n"
    "v_exp_f32 v186, v186\n"
    "v_mfma_f32_16x16x32_bf16 a[60:63], a[248:251], v[144:147], a[60:63]\n"
    "s_waitcnt lgkmcnt(4)\n"
    "ds_read_b128 a[248:251], v200 offset:3072\n"
    "v_cvt_pk_bf16_f32 v171, v198, v199\n"
    "v_mfma_f32_16x16x32_bf16 v[64:67], a[224:227], a[128:131], 0\n"
    "s_add_u32 m0, s54, 65536\n"
    "ds_read_b128 a[252:255], v200 offset:3584\n"
    "v_fma_f32 v190, v126, s58, v234\n"
    "v_mfma_f32_16x16x32_bf16 v[64:67], a[228:231], a[132:135], v[64:67]\n"
    "buffer_load_dwordx4 v211, s[40:43], s53 offen offset:0 lds\n"
    "v_mfma_f32_16x16x32_bf16 v[68:71], a[228:231], a[164:167], 0\n"
    "v_exp_f32_e64 v187, v187\n"
    "v_mfma_f32_16x16x32_bf16 v[68:71], a[224:227], a[160:163], v[68:71]\n"
    "v_fma_f32 v191, v127, s58, v234\n"
    "v_add_f32_e64 v240, v184, v185\n"
    "v_mfma_f32_16x16x32_bf16 v[72:75], a[224:227], a[192:195], 0\n"
    "v_exp_f32 v188, v188\n"
    "v_mfma_f32_16x16x32_bf16 v[72:75], a[228:231], a[196:199], v[72:75]\n"
    "s_waitcnt lgkmcnt(4)\n"
    "ds_read_b128 a[224:227], v200 offset:8192\n"
    "v_cvt_pk_bf16_f32 v172, v184, v185\n"
    "v_mfma_f32_16x16x32_bf16 v[72:75], a[232:235], a[200:203], v[72:75]\n"
    "ds_read_b128 a[228:231], v200 offset:8704\n"
    "v_fma_f32 v192, v128, s58, v232\n"
    "v_mfma_f32_16x16x32_bf16 v[72:75], a[236:239], a[204:207], v[72:75]\n"
    "v_exp_f32_e64 v189, v189\n"
    "v_mfma_f32_16x16x32_bf16 v[68:71], a[236:239], a[172:175], v[68:71]\n"
    "v_add_f32_e64 v240, v240, v186\n"
    "v_fma_f32 v193, v129, s58, v232\n"
    "v_mfma_f32_16x16x32_bf16 v[68:71], a[232:235], a[168:171], v[68:71]\n"
    "v_exp_f32_e64 v190, v190\n"
    "v_mfma_f32_16x16x32_bf16 v[64:67], a[232:235], a[136:139], v[64:67]\n"
    "s_add_u32 m0, s54, 66432\n"
    "ds_read_b128 a[232:235], v200 offset:9216\n"
    "v_add_f32 v240, v240, v187\n"
    "v_mfma_f32_16x16x32_bf16 v[64:67], a[236:239], a[140:143], v[64:67]\n"
    "s_waitcnt lgkmcnt(5)\n"
    "buffer_load_dwordx4 v211, s[40:43], s53 offen offset:128 lds\n"
    "v_mfma_f32_16x16x32_bf16 v[64:67], a[240:243], a[144:147], v[64:67]\n"
    "ds_read_b128 a[236:239], v200 offset:9728\n"
    "v_cvt_pk_bf16_f32 v173, v186, v187\n"
    "v_mfma_f32_16x16x32_bf16 v[64:67], a[244:247], a[148:151], v[64:67]\n"
    "v_fma_f32 v194, v130, s58, v232\n"
    "v_add_f32_e64 v240, v240, v188\n"
    "v_mfma_f32_16x16x32_bf16 v[68:71], a[244:247], a[180:183], v[68:71]\n"
    "v_exp_f32_e64 v191, v191\n"
    "v_mfma_f32_16x16x32_bf16 v[68:71], a[240:243], a[176:179], v[68:71]\n"
    "v_fma_f32 v195, v131, s58, v232\n"
    "v_add_f32_e64 v240, v240, v189\n"
    "v_mfma_f32_16x16x32_bf16 v[72:75], a[240:243], a[208:211], v[72:75]\n"
    "v_exp_f32 v192, v192\n"
    "v_mfma_f32_16x16x32_bf16 v[72:75], a[244:247], a[212:215], v[72:75]\n"
    "s_waitcnt lgkmcnt(4)\n"
    "ds_read_b128 a[240:243], v200 offset:10240\n"
    "v_cvt_pk_bf16_f32 v174, v188, v189\n"
    "v_mfma_f32_16x16x32_bf16 v[72:75], a[248:251], a[216:219], v[72:75]\n"
    "ds_read_b128 a[244:247], v200 offset:10752\n"
    "v_fma_f32 v196, v136, s58, v232\n"
    "v_mfma_f32_16x16x32_bf16 v[72:75], a[252:255], a[220:223], v[72:75]\n"
    "v_exp_f32_e64 v193, v193\n"
    "v_mfma_f32_16x16x32_bf16 v[68:71], a[252:255], a[188:191], v[68:71]\n"
    "s_add_u32 m0, s54, 67328\n"
    "v_add_f32_e64 v240, v240, v190\n"
    "v_fma_f32 v197, v137, s58, v232\n"
    "v_mfma_f32_16x16x32_bf16 v[68:71], a[248:251], a[184:187], v[68:71]\n"
    "buffer_load_dwordx4 v211, s[40:43], s53 offen offset:256 lds\n"
    "v_mfma_f32_16x16x32_bf16 v[64:67], a[248:251], a[152:155], v[64:67]\n"
    "v_exp_f32 v194, v194\n"
    "v_mfma_f32_16x16x32_bf16 v[64:67], a[252:255], a[156:159], v[64:67]\n"
    "s_waitcnt lgkmcnt(4)\n"
    "ds_read_b128 a[248:251], v200 offset:11264\n"
    "v_add_f32_e64 v240, v240, v191\n"
    "v_mfma_f32_16x16x32_bf16 v[76:79], a[224:227], a[128:131], 0\n"
    "ds_read_b128 a[252:255], v200 offset:11776\n"
    "v_cvt_pk_bf16_f32 v175, v190, v191\n"
    "v_mfma_f32_16x16x32_bf16 v[76:79], a[228:231], a[132:135], v[76:79]\n"
    "v_fma_f32 v198, v138, s58, v232\n"
    "v_add_f32_e64 v238, v238, v192\n"
    "v_mfma_f32_16x16x32_bf16 v[80:83], a[228:231], a[164:167], 0\n"
    "v_exp_f32_e64 v195, v195\n"
    "v_mfma_f32_16x16x32_bf16 v[80:83], a[224:227], a[160:163], v[80:83]\n"
    "v_fma_f32 v199, v139, s58, v232\n"
    "v_add_f32_e64 v238, v238, v193\n"
    "v_mfma_f32_16x16x32_bf16 v[84:87], a[224:227], a[192:195], 0\n"
    "v_exp_f32 v196, v196\n"
    "v_mfma_f32_16x16x32_bf16 v[84:87], a[228:231], a[196:199], v[84:87]\n"
    "s_waitcnt lgkmcnt(4)\n"
    "ds_read_b128 a[224:227], v201 offset:0\n"
    "v_cvt_pk_bf16_f32 v176, v192, v193\n"
    "v_mfma_f32_16x16x32_bf16 v[84:87], a[232:235], a[200:203], v[84:87]\n"
    "s_add_u32 m0, s54, 68224\n"
    "ds_read_b128 a[228:231], v201 offset:512\n"
    "v_fma_f32 v184, v132, s58, v233\n"
    "v_mfma_f32_16x16x32_bf16 v[84:87], a[236:239], a[204:207], v[84:87]\n"
    "buffer_load_dwordx4 v211, s[40:43], s53 offen offset:384 lds\n"
    "v_mfma_f32_16x16x32_bf16 v[80:83], a[236:239], a[172:175], v[80:83]\n"
    "s_add_u32 s53, s53, 16384\n"
    "v_exp_f32_e64 v197, v197\n"
    "v_mfma_f32_16x16x32_bf16 v[80:83], a[232:235], a[168:171], v[80:83]\n"
    "v_add_f32_e64 v238, v238, v194\n"
    "v_fma_f32 v185, v133, s58, v233\n"
    "v_mfma_f32_16x16x32_bf16 v[76:79], a[232:235], a[136:139], v[76:79]\n"
    "v_exp_f32 v198, v198\n"
    "v_mfma_f32_16x16x32_bf16 v[76:79], a[236:239], a[140:143], v[76:79]\n"
    "s_waitcnt lgkmcnt(4)\n"
    "ds_read_b128 a[232:235], v201 offset:1024\n"
    "v_add_f32_e64 v238, v238, v195\n"
    "v_mfma_f32_16x16x32_bf16 v[76:79], a[240:243], a[144:147], v[76:79]\n"
    "ds_read_b128 a[236:239], v201 offset:1536\n"
    "v_cvt_pk_bf16_f32 v177, v194, v195\n"
    "v_mfma_f32_16x16x32_bf16 v[76:79], a[244:247], a[148:151], v[76:79]\n"
    "v_fma_f32 v186, v134, s58, v233\n"
    "v_add_f32_e64 v238, v238, v196\n"
    "v_mfma_f32_16x16x32_bf16 v[80:83], a[244:247], a[180:183], v[80:83]\n"
    "v_exp_f32_e64 v199, v199\n"
    "v_mfma_f32_16x16x32_bf16 v[80:83], a[240:243], a[176:179], v[80:83]\n"
    "v_fma_f32 v187, v135, s58, v233\n"
    "v_add_f32_e64 v238, v238, v197\n"
    "v_mfma_f32_16x16x32_bf16 v[84:87], a[240:243], a[208:211], v[84:87]\n"
    "s_add_u32 m0, s54, 81920\n"
    "v_exp_f32 v184, v184\n"
    "v_mfma_f32_16x16x32_bf16 v[84:87], a[244:247], a[212:215], v[84:87]\n"
    "s_waitcnt lgkmcnt(4)\n"
    "buffer_load_dwordx4 v211, s[40:43], s53 offen offset:0 lds\n"
    "v_mfma_f32_16x16x32_bf16 v[84:87], a[248:251], a[216:219], v[84:87]\n"
    "ds_read_b128 a[240:243], v201 offset:2048\n"
    "v_cvt_pk_bf16_f32 v178, v196, v197\n"
    "v_mfma_f32_16x16x32_bf16 v[84:87], a[252:255], a[220:223], v[84:87]\n"
    "ds_read_b128 a[244:247], v201 offset:2560\n"
    "v_fma_f32 v188, v140, s58, v233\n"
    "v_mfma_f32_16x16x32_bf16 v[80:83], a[252:255], a[188:191], v[80:83]\n"
    "v_exp_f32_e64 v185, v185\n"
    "v_mfma_f32_16x16x32_bf16 v[80:83], a[248:251], a[184:187], v[80:83]\n"
    "v_add_f32_e64 v238, v238, v198\n"
    "v_fma_f32 v189, v141, s58, v233\n"
    "v_mfma_f32_16x16x32_bf16 v[76:79], a[248:251], a[152:155], v[76:79]\n"
    "v_exp_f32 v186, v186\n"
    "v_mfma_f32_16x16x32_bf16 v[76:79], a[252:255], a[156:159], v[76:79]\n"
    "s_waitcnt lgkmcnt(4)\n"
    "ds_read_b128 a[248:251], v201 offset:3072\n"
    "ds_read_b128 a[252:255], v201 offset:3584\n"
    "v_mfma_f32_16x16x32_bf16 v[88:91], a[224:227], a[128:131], 0\n"
    "v_add_f32_e64 v238, v238, v199\n"
    "v_cvt_pk_bf16_f32 v179, v198, v199\n"
    "v_mfma_f32_16x16x32_bf16 v[88:91], a[228:231], a[132:135], v[88:91]\n"
    "v_fma_f32 v190, v142, s58, v233\n"
    "v_add_f32_e64 v239, v239, v184\n"
    "v_mfma_f32_16x16x32_bf16 v[92:95], a[228:231], a[164:167], 0\n"
    "s_add_u32 m0, s54, 82816\n"
    "v_exp_f32 v187, v187\n"
    "v_mfma_f32_16x16x32_bf16 v[92:95], a[224:227], a[160:163], v[92:95]\n"
    "s_waitcnt lgkmcnt(4)\n"
    "buffer_load_dwordx4 v211, s[40:43], s53 offen offset:128 lds\n"
    "v_mfma_f32_16x16x32_bf16 v[92:95], a[232:235], a[168:171], v[92:95]\n"
    "ds_read_b128 a[224:227], v201 offset:8192\n"
    "ds_read_b128 a[228:231], v201 offset:8704\n"
    "v_mfma_f32_16x16x32_bf16 v[92:95], a[236:239], a[172:175], v[92:95]\n"
    "v_fma_f32 v191, v143, s58, v233\n"
    "v_add_f32_e64 v239, v239, v185\n"
    "v_mfma_f32_16x16x32_bf16 v[88:91], a[236:239], a[140:143], v[88:91]\n"
    "v_exp_f32 v188, v188\n"
    "v_mfma_f32_16x16x32_bf16 v[88:91], a[232:235], a[136:139], v[88:91]\n"
    "s_waitcnt lgkmcnt(4)\n"
    "ds_read_b128 a[232:235], v201 offset:9216\n"
    "ds_read_b128 a[236:239], v201 offset:9728\n"
    "v_mfma_f32_16x16x32_bf16 v[88:91], a[240:243], a[144:147], v[88:91]\n"
    "v_cvt_pk_bf16_f32 v180, v184, v185\n"
    "v_add_f32_e64 v239, v239, v186\n"
    "v_mfma_f32_16x16x32_bf16 v[88:91], a[244:247], a[148:151], v[88:91]\n"
    "v_exp_f32_e64 v189, v189\n"
    "v_mfma_f32_16x16x32_bf16 v[92:95], a[244:247], a[180:183], v[92:95]\n"
    "v_exp_f32 v190, v190\n"
    "v_mfma_f32_16x16x32_bf16 v[92:95], a[240:243], a[176:179], v[92:95]\n"
    "s_waitcnt lgkmcnt(4)\n"
    "ds_read_b128 a[240:243], v201 offset:10240\n"
    "ds_read_b128 a[244:247], v201 offset:10752\n"
    "v_mfma_f32_16x16x32_bf16 v[92:95], a[248:251], a[184:187], v[92:95]\n"
    "s_add_u32 m0, s54, 83712\n"
    "v_add_f32_e64 v239, v239, v187\n"
    "v_cvt_pk_bf16_f32 v181, v186, v187\n"
    "v_mfma_f32_16x16x32_bf16 v[92:95], a[252:255], a[188:191], v[92:95]\n"
    "buffer_load_dwordx4 v211, s[40:43], s53 offen offset:256 lds\n"
    "v_mfma_f32_16x16x32_bf16 v[88:91], a[252:255], a[156:159], v[88:91]\n"
    "v_exp_f32 v191, v191\n"
    "v_mfma_f32_16x16x32_bf16 v[88:91], a[248:251], a[152:155], v[88:91]\n"
    "s_waitcnt lgkmcnt(4)\n"
    "ds_read_b128 a[248:251], v201 offset:11264\n"
    "ds_read_b128 a[252:255], v201 offset:11776\n"
    "v_mfma_f32_16x16x32_bf16 v[96:99], a[224:227], a[128:131], 0\n"
    "v_add_f32 v239, v239, v188\n"
    "v_add_f32 v239, v239, v189\n"
    "v_mfma_f32_16x16x32_bf16 v[96:99], a[228:231], a[132:135], v[96:99]\n"
    "v_cvt_pk_bf16_f32 v182, v188, v189\n"
    "v_add_f32_e64 v239, v239, v190\n"
    "v_mfma_f32_16x16x32_bf16 v[100:103], a[228:231], a[164:167], 0\n"
    "v_add_f32 v239, v239, v191\n"
    "v_cvt_pk_bf16_f32 v183, v190, v191\n"
    "v_mfma_f32_16x16x32_bf16 v[100:103], a[224:227], a[160:163], v[100:103]\n"
    "s_waitcnt lgkmcnt(4)\n"
    "ds_read_b64_tr_b16 a[224:225], v202 offset:32768\n"
    "ds_read_b64_tr_b16 a[226:227], v202 offset:40960\n"
    "v_mfma_f32_16x16x32_bf16 v[100:103], a[232:235], a[168:171], v[100:103]\n"
    "ds_read_b64_tr_b16 a[228:229], v206 offset:32768\n"
    "ds_read_b64_tr_b16 a[230:231], v206 offset:40960\n"
    "v_mfma_f32_16x16x32_bf16 v[100:103], a[236:239], a[172:175], v[100:103]\n"
    "v_max3_f32 v243, v238, v239, v240\n"
    "v_cmp_lt_f32 vcc, s59, v243\n"
    "v_mfma_f32_16x16x32_bf16 v[96:99], a[236:239], a[140:143], v[96:99]\n"
    "s_add_u32 m0, s54, 84608\n"
    "v_mfma_f32_16x16x32_bf16 v[96:99], a[232:235], a[136:139], v[96:99]\n"
    "s_waitcnt lgkmcnt(6)\n"
    "buffer_load_dwordx4 v211, s[40:43], s53 offen offset:384 lds\n"
    "v_mfma_f32_16x16x32_bf16 v[96:99], a[240:243], a[144:147], v[96:99]\n"
    "s_add_u32 s53, s53, 16384\n"
    "ds_read_b64_tr_b16 a[232:233], v204 offset:32768\n"
    "ds_read_b64_tr_b16 a[234:235], v204 offset:40960\n"
    "v_mfma_f32_16x16x32_bf16 v[96:99], a[244:247], a[148:151], v[96:99]\n"
    "ds_read_b64_tr_b16 a[236:237], v208 offset:32768\n"
    "ds_read_b64_tr_b16 a[238:239], v208 offset:40960\n"
    "v_mfma_f32_16x16x32_bf16 v[100:103], a[244:247], a[180:183], v[100:103]\n"
    "v_mfma_f32_16x16x32_bf16 v[100:103], a[240:243], a[176:179], v[100:103]\n"
    "s_waitcnt vmcnt(8) lgkmcnt(8)\n"
    "ds_read_b64_tr_b16 a[240:241], v202 offset:33280\n"
    "ds_read_b64_tr_b16 a[242:243], v202 offset:41472\n"
    "v_mfma_f32_16x16x32_bf16 v[100:103], a[248:251], a[184:187], v[100:103]\n"
    "s_barrier\n"
    "ds_read_b64_tr_b16 a[244:245], v206 offset:33280\n"
    "ds_read_b64_tr_b16 a[246:247], v206 offset:41472\n"
    "v_mfma_f32_16x16x32_bf16 v[100:103], a[252:255], a[188:191], v[100:103]\n"
    "v_mfma_f32_16x16x32_bf16 v[96:99], a[252:255], a[156:159], v[96:99]\n"
    "v_mfma_f32_16x16x32_bf16 v[96:99], a[248:251], a[152:155], v[96:99]\n"
    "s_cbranch_vccz Lloop_%=\n"
    "s_branch LrareUe_%=\n"
    "Lspec_%=:\n"
    "s_cbranch_execz Lhopd0_%=\n"
    "s_cbranch_vccnz LrareUo_%=\n"
    "LbackUo_%=:\n"
    "s_cmp_eq_u32 s12, 0\n"
    "s_cbranch_scc1 Lue_%=\n"
    "Lexit_%=:\n"
    "s_cmp_eq_u32 s61, 1\n"
    "s_cbranch_scc1 Lfinal_%=\n"
    "s_mov_b32 s50, 319488\n"
    "s_waitcnt lgkmcnt(8)\n"
    "v_mfma_f32_16x16x32_bf16 a[0:3], a[224:227], v[144:147], a[0:3]\n"
    "s_waitcnt vmcnt(15)\n"
    "ds_read_b64_tr_b16 a[248:249], v204 offset:512\n"
    "ds_read_b128 a[192:195], v213 offset:0\n"
    "v_mfma_f32_16x16x32_bf16 a[0:3], a[228:231], v[156:159], a[0:3]\n"
    "ds_read_b64_tr_b16 a[250:251], v204 offset:8704\n"
    "ds_read_b128 a[196:199], v213 offset:512\n"
    "ds_read_b128 a[200:203], v213 offset:1024\n"
    "v_mfma_f32_16x16x32_bf16 a[64:67], a[228:231], v[160:163], a[64:67]\n"
    "ds_read_b64_tr_b16 a[252:253], v208 offset:512\n"
    "ds_read_b128 a[204:207], v213 offset:1536\n"
    "ds_read_b128 a[128:131], v214 offset:0\n"
    "v_mfma_f32_16x16x32_bf16 a[64:67], a[224:227], v[148:151], a[64:67]\n"
    "s_add_u32 m0, s54, 32768\n"
    "ds_read_b64_tr_b16 a[254:255], v208 offset:8704\n"
    "ds_read_b128 a[132:135], v214 offset:512\n"
    "v_mfma_f32_16x16x32_bf16 v[0:3], a[224:227], v[152:155], v[0:3]\n"
    "s_waitcnt lgkmcnt(14)\n"
    "buffer_load_dwordx4 v210, s[44:47], s56 offen offset:0 nt lds\n"
    "ds_read_b64_tr_b16 a[224:225], v202 offset:1024\n"
    "v_mfma_f32_16x16x32_bf16 v[4:7], a[232:235], v[152:155], v[4:7]\n"
    "s_nop 0\n"
    "ds_read_b64_tr_b16 a[226:227], v202 offset:9216\n"
    "ds_read_b128 a[136:139], v214 offset:1024\n"
    "v_xor_b32 v253, 0x2020, v213\n"
    "v_mfma_f32_16x16x32_bf16 a[68:71], a[232:235], v[148:151], a[68:71]\n"
    "ds_read_b64_tr_b16 a[228:229], v206 offset:1024\n"
    "ds_read_b128 a[208:211], v253 offset:0\n"
    "v_mfma_f32_16x16x32_bf16 a[68:71], a[236:239], v[160:163], a[68:71]\n"
    "ds_read_b64_tr_b16 a[230:231], v206 offset:9216\n"
    "v_add_f32 v236, v236, v238\n"
    "v_add_f32 v237, v237, v239\n"
    "v_mfma_f32_16x16x32_bf16 a[4:7], a[236:239], v[156:159], a[4:7]\n"
    "ds_read_b128 a[212:215], v253 offset:512\n"
    "v_add_f32 v235, v235, v240\n"
    "v_fma_f32 v184, v104, s58, v232\n"
    "v_mfma_f32_16x16x32_bf16 a[4:7], a[232:235], v[144:147], a[4:7]\n"
    "s_waitcnt lgkmcnt(15)\n"
    "ds_read_b64_tr_b16 a[232:233], v204 offset:1024\n"
    "ds_read_b128 a[216:219], v253 offset:1024\n"
    "v_mfma_f32_16x16x32_bf16 a[8:11], a[240:243], v[144:147], a[8:11]\n"
    "s_add_u32 m0, s54, 33664\n"
    "ds_read_b64_tr_b16 a[234:235], v204 offset:9216\n"
    "ds_read_b128 a[220:223], v253 offset:1536\n"
    "v_fma_f32 v185, v105, s58, v232\n"
    "v_mfma_f32_16x16x32_bf16 a[8:11], a[244:247], v[156:159], a[8:11]\n"
    "buffer_load_dwordx4 v210, s[44:47], s56 offen offset:128 nt lds\n"
    "ds_read_b64_tr_b16 a[236:237], v208 offset:1024\n"
    "v_mfma_f32_16x16x32_bf16 a[72:75], a[244:247], v[160:163], a[72:75]\n"
    "ds_read_b64_tr_b16 a[238:239], v208 offset:9216\n"
    "ds_read_b128 a[140:143], v214 offset:1536\n"
    "v_mfma_f32_16x16x32_bf16 a[72:75], a[240:243], v[148:151], a[72:75]\n"
    "v_fma_f32 v186, v106, s58, v232\n"
    "v_fma_f32 v187, v107, s58, v232\n"
    "v_fma_f32 v188, v116, s58, v232\n"
    "v_mfma_f32_16x16x32_bf16 v[8:11], a[240:243], v[152:155], v[8:11]\n"
    "s_waitcnt lgkmcnt(15)\n"
    "ds_read_b64_tr_b16 a[240:241], v202 offset:1536\n"
    "v_exp_f32 v184, v184\n"
    "v_mfma_f32_16x16x32_bf16 v[12:15], a[248:251], v[152:155], v[12:15]\n"
    "ds_read_b64_tr_b16 a[242:243], v202 offset:9728\n"
    "v_exp_f32_e64 v185, v185\n"
    "v_mfma_f32_16x16x32_bf16 a[76:79], a[248:251], v[148:151], a[76:79]\n"
    "s_add_u32 m0, s54, 34560\n"
    "ds_read_b64_tr_b16 a[244:245], v206 offset:1536\n"
    "v_fma_f32 v189, v117, s58, v232\n"
    "v_mfma_f32_16x16x32_bf16 a[76:79], a[252:255], v[160:163], a[76:79]\n"
    "buffer_load_dwordx4 v210, s[44:47], s56 offen offset:256 nt lds\n"
    "v_fma_f32 v190, v118, s58, v232\n"
    "v_mfma_f32_16x16x32_bf16 a[12:15], a[252:255], v[156:159], a[12:15]\n"
    "ds_read_b64_tr_b16 a[246:247], v206 offset:9728\n"
    "v_exp_f32 v186, v186\n"
    "v_mfma_f32_16x16x32_bf16 a[12:15], a[248:251], v[144:147], a[12:15]\n"
    "s_waitcnt lgkmcnt(12)\n"
    "v_exp_f32 v187, v187\n"
    "v_mfma_f32_16x16x32_bf16 a[16:19], a[224:227], v[144:147], a[16:19]\n"
    "s_waitcnt lgkmcnt(4)\n"
    "ds_read_b64_tr_b16 a[248:249], v204 offset:1536\n"
    "v_fma_f32 v191, v119, s58, v232\n"
    "v_add_f32_e64 v238, v184, v185\n"
    "v_mfma_f32_16x16x32_bf16 a[16:19], a[228:231], v[156:159], a[16:19]\n"
    "Lpad4_%=:\n"
    "s_cbranch_execz Lhopd1_%=\n"
    "ds_read_b64_tr_b16 a[250:251], v204 offset:9728\n"
    "v_exp_f32 v188, v188\n"
    "v_mfma_f32_16x16x32_bf16 a[80:83], a[228:231], v[160:163], a[80:83]\n"
    "ds_read_b64_tr_b16 a[252:253], v208 offset:1536\n"
    "v_cvt_pk_bf16_f32 v164, v184, v185\n"
    "v_mfma_f32_16x16x32_bf16 a[80:83], a[224:227], v[148:151], a[80:83]\n"
    "s_add_u32 m0, s54, 35456\n"
    "ds_read_b64_tr_b16 a[254:255], v208 offset:9728\n"
    "v_fma_f32 v192, v108, s58, v233\n"
    "v_add_f32 v238, v238, v186\n"
    "v_mfma_f32_16x16x32_bf16 v[16:19], a[224:227], v[152:155], v[16:19]\n"
    "s_waitcnt lgkmcnt(9)\n"
    "buffer_load_dwordx4 v210, s[44:47], s56 offen offset:384 nt lds\n"
    "v_fma_f32 v193, v109, s58, v233\n"
    "v_mfma_f32_16x16x32_bf16 v[20:23], a[232:235], v[152:155], v[20:23]\n"
    "v_exp_f32_e64 v189, v189\n"
    "v_mfma_f32_16x16x32_bf16 a[84:87], a[232:235], v[148:151], a[84:87]\n"
    "ds_read_b64_tr_b16 a[224:225], v203 offset:0\n"
    "v_exp_f32_e64 v190, v190\n"
    "v_mfma_f32_16x16x32_bf16 a[84:87], a[236:239], v[160:163], a[84:87]\n"
    "ds_read_b64_tr_b16 a[226:227], v203 offset:8192\n"
    "ds_read_b64_tr_b16 a[228:229], v207 offset:0\n"
    "ds_read_b64_tr_b16 a[230:231], v207 offset:8192\n"
    "v_mfma_f32_16x16x32_bf16 a[20:23], a[236:239], v[156:159], a[20:23]\n"
    "v_add_f32 v238, v238, v187\n"
    "v_cvt_pk_bf16_f32 v165, v186, v187\n"
    "v_mfma_f32_16x16x32_bf16 a[20:23], a[232:235], v[144:147], a[20:23]\n"
    "s_waitcnt lgkmcnt(8)\n"
    "ds_read_b64_tr_b16 a[232:233], v205 offset:0\n"
    "v_fma_f32 v194, v110, s58, v233\n"
    "v_add_f32_e64 v238, v238, v188\n"
    "v_mfma_f32_16x16x32_bf16 a[24:27], a[240:243], v[144:147], a[24:27]\n"
    "s_add_u32 m0, s54, 49152\n"
    "ds_read_b64_tr_b16 a[234:235], v205 offset:8192\n"
    "v_exp_f32_e64 v191, v191\n"
    "v_mfma_f32_16x16x32_bf16 a[24:27], a[244:247], v[156:159], a[24:27]\n"
    "buffer_load_dwordx4 v210, s[44:47], s70 offen offset:0 nt lds\n"
    "ds_read_b64_tr_b16 a[236:237], v209 offset:0\n"
    "v_mfma_f32_16x16x32_bf16 a[88:91], a[244:247], v[160:163], a[88:91]\n"
    "ds_read_b64_tr_b16 a[238:239], v209 offset:8192\n"
    "v_fma_f32 v195, v111, s58, v233\n"
    "v_mfma_f32_16x16x32_bf16 a[88:91], a[240:243], v[148:151], a[88:91]\n"
    "v_exp_f32_e64 v192, v192\n"
    "v_add_f32 v238, v238, v189\n"
    "v_mfma_f32_16x16x32_bf16 v[24:27], a[240:243], v[152:155], v[24:27]\n"
    "s_waitcnt lgkmcnt(8)\n"
    "ds_read_b64_tr_b16 a[240:241], v203 offset:512\n"
    "v_cvt_pk_bf16_f32 v166, v188, v189\n"
    "v_fma_f32 v196, v120, s58, v233\n"
    "v_mfma_f32_16x16x32_bf16 v[28:31], a[248:251], v[152:155], v[28:31]\n"
    "v_exp_f32_e64 v193, v193\n"
    "v_mfma_f32_16x16x32_bf16 a[92:95], a[248:251], v[148:151], a[92:95]\n"
    "ds_read_b64_tr_b16 a[242:243], v203 offset:8704\n"
    "v_add_f32_e64 v238, v238, v190\n"
    "v_fma_f32 v197, v121, s58, v233\n"
    "v_mfma_f32_16x16x32_bf16 a[92:95], a[252:255], v[160:163], a[92:95]\n"
    "s_add_u32 m0, s54, 50048\n"
    "ds_read_b64_tr_b16 a[244:245], v207 offset:512\n"
    "v_exp_f32_e64 v194, v194\n"
    "v_mfma_f32_16x16x32_bf16 a[28:31], a[252:255], v[156:159], a[28:31]\n"
    "buffer_load_dwordx4 v210, s[44:47], s70 offen offset:128 nt lds\n"
    "v_mfma_f32_16x16x32_bf16 a[28:31], a[248:251], v[144:147], a[28:31]\n"
    "s_waitcnt lgkmcnt(7)\n"
    "ds_read_b64_tr_b16 a[246:247], v207 offset:8704\n"
    "v_add_f32 v238, v238, v191\n"
    "v_cvt_pk_bf16_f32 v167, v190, v191\n"
    "v_mfma_f32_16x16x32_bf16 a[32:35], a[224:227], v[144:147], a[32:35]\n"
    "ds_read_b64_tr_b16 a[248:249], v205 offset:512\n"
    "v_fma_f32 v198, v122, s58, v233\n"
    "v_fma_f32 v199, v123, s58, v233\n"
    "v_mfma_f32_16x16x32_bf16 a[32:35], a[228:231], v[156:159], a[32:35]\n"
    "v_exp_f32_e64 v195, v195\n"
    "v_mfma_f32_16x16x32_bf16 a[96:99], a[228:231], v[160:163], a[96:99]\n"
    "ds_read_b64_tr_b16 a[250:251], v205 offset:8704\n"
    "v_exp_f32_e64 v196, v196\n"
    "v_mfma_f32_16x16x32_bf16 a[96:99], a[224:227], v[148:151], a[96:99]\n"
    "ds_read_b64_tr_b16 a[252:253], v209 offset:512\n"
    "ds_read_b64_tr_b16 a[254:255], v209 offset:8704\n"
    "v_add_f32 v239, v192, v193\n"
    "v_mfma_f32_16x16x32_bf16 v[32:35], a[224:227], v[152:155], v[32:35]\n"
    "s_waitcnt lgkmcnt(8)\n"
    "ds_read_b64_tr_b16 a[224:225], v203 offset:1024\n"
    "v_cvt_pk_bf16_f32 v168, v192, v193\n"
    "v_mfma_f32_16x16x32_bf16 v[36:39], a[232:235], v[152:155], v[36:39]\n"
    "s_add_u32 m0, s54, 50944\n"
    "ds_read_b64_tr_b16 a[226:227], v203 offset:9216\n"
    "v_fma_f32 v184, v112, s58, v234\n"
    "v_add_f32_e64 v239, v239, v194\n"
    "v_mfma_f32_16x16x32_bf16 a[100:103], a[232:235], v[148:151], a[100:103]\n"
    "buffer_load_dwordx4 v210, s[44:47], s70 offen offset:256 nt lds\n"
    "v_fma_f32 v185, v113, s58, v234\n"
    "v_mfma_f32_16x16x32_bf16 a[100:103], a[236:239], v[160:163], a[100:103]\n"
    "ds_read_b64_tr_b16 a[228:229], v207 offset:1024\n"
    "v_exp_f32_e64 v197, v197\n"
    "v_mfma_f32_16x16x32_bf16 a[36:39], a[236:239], v[156:159], a[36:39]\n"
    "v_exp_f32 v198, v198\n"
    "v_mfma_f32_16x16x32_bf16 a[36:39], a[232:235], v[144:147], a[36:39]\n"
    "s_waitcnt lgkmcnt(7)\n"
    "ds_read_b64_tr_b16 a[230:231], v207 offset:9216\n"
    "v_add_f32_e64 v239, v239, v195\n"
    "v_cvt_pk_bf16_f32 v169, v194, v195\n"
    "v_mfma_f32_16x16x32_bf16 a[40:43], a[240:243], v[144:147], a[40:43]\n"
    "ds_read_b64_tr_b16 a[232:233], v205 offset:1024\n"
    "v_fma_f32 v186, v114, s58, v234\n"
    "v_add_f32_e64 v239, v239, v196\n"
    "v_mfma_f32_16x16x32_bf16 a[40:43], a[244:247], v[156:159], a[40:43]\n"
    "s_add_u32 m0, s54, 51840\n"
    "v_exp_f32_e64 v199, v199\n"
    "v_mfma_f32_16x16x32_bf16 a[104:107], a[244:247], v[160:163], a[104:107]\n"
    "buffer_load_dwordx4 v210, s[44:47], s70 offen offset:384 nt lds\n"
    "ds_read_b64_tr_b16 a[234:235], v205 offset:9216\n"
    "v_mfma_f32_16x16x32_bf16 a[104:107], a[240:243], v[148:151], a[104:107]\n"
    "ds_read_b64_tr_b16 a[236:237], v209 offset:1024\n"
    "ds_read_b64_tr_b16 a[238:239], v209 offset:9216\n"
    "v_fma_f32 v187, v115, s58, v234\n"
    "v_mfma_f32_16x16x32_bf16 v[40:43], a[240:243], v[152:155], v[40:43]\n"
    "s_waitcnt lgkmcnt(8)\n"
    "v_exp_f32 v184, v184\n"
    "v_mfma_f32_16x16x32_bf16 v[44:47], a[248:251], v[152:155], v[44:47]\n"
    "ds_read_b64_tr_b16 a[240:241], v203 offset:1536\n"
    "v_add_f32_e64 v239, v239, v197\n"
    "v_cvt_pk_bf16_f32 v170, v196, v197\n"
    "v_mfma_f32_16x16x32_bf16 a[108:111], a[248:251], v[148:151], a[108:111]\n"
    "ds_read_b64_tr_b16 a[242:243], v203 offset:9728\n"
    "v_fma_f32 v188, v124, s58, v234\n"
    "v_add_f32_e64 v239, v239, v198\n"
    "v_mfma_f32_16x16x32_bf16 a[108:111], a[252:255], v[160:163], a[108:111]\n"
    "s_add_u32 m0, s68, 3840\n"
    "v_exp_f32_e64 v185, v185\n"
    "v_mfma_f32_16x16x32_bf16 a[44:47], a[252:255], v[156:159], a[44:47]\n"
    "s_nop 0\n"
    "buffer_load_dwordx4 v210, s[44:47], s55 offen offset:256 nt lds\n"
    "ds_read_b64_tr_b16 a[244:245], v207 offset:1536\n"
    "v_mfma_f32_16x16x32_bf16 a[44:47], a[248:251], v[144:147], a[44:47]\n"
    "s_waitcnt lgkmcnt(7)\n"
    "ds_read_b64_tr_b16 a[246:247], v207 offset:9728\n"
    "ds_read_b64_tr_b16 a[248:249], v205 offset:1536\n"
    "ds_read_b64_tr_b16 a[250:251], v205 offset:9728\n"
    "v_mfma_f32_16x16x32_bf16 a[48:51], a[224:227], v[144:147], a[48:51]\n"
    "ds_read_b64_tr_b16 a[252:253], v209 offset:1536\n"
    "ds_read_b64_tr_b16 a[254:255], v209 offset:9728\n"
    "v_mfma_f32_16x16x32_bf16 a[48:51], a[228:231], v[156:159], a[48:51]\n"
    "v_fma_f32 v189, v125, s58, v234\n"
    "v_exp_f32_e64 v186, v186\n"
    "v_mfma_f32_16x16x32_bf16 a[112:115], a[228:231], v[160:163], a[112:115]\n"
    "v_add_f32_e64 v239, v239, v199\n"
    "v_cvt_pk_bf16_f32 v171, v198, v199\n"
    "v_fma_f32 v190, v126, s58, v234\n"
    "v_mfma_f32_16x16x32_bf16 a[112:115], a[224:227], v[148:151], a[112:115]\n"
    "v_exp_f32_e64 v187, v187\n"
    "v_fma_f32 v191, v127, s58, v234\n"
    "v_mfma_f32_16x16x32_bf16 v[48:51], a[224:227], v[152:155], v[48:51]\n"
    "s_waitcnt lgkmcnt(8)\n"
    "v_exp_f32 v188, v188\n"
    "v_mfma_f32_16x16x32_bf16 v[52:55], a[232:235], v[152:155], v[52:55]\n"
    "s_waitcnt vmcnt(9) lgkmcnt(0)\n"
    "v_add_f32 v240, v184, v185\n"
    "v_cvt_pk_bf16_f32 v172, v184, v185\n"
    "v_fma_f32 v192, v128, s58, v232\n"
    "v_mfma_f32_16x16x32_bf16 a[116:119], a[232:235], v[148:151], a[116:119]\n"
    "s_barrier\n"
    "ds_read_b64_tr_b16 a[224:225], v202 offset:32768\n"
    "v_exp_f32 v189, v189\n"
    "v_mfma_f32_16x16x32_bf16 a[116:119], a[236:239], v[160:163], a[116:119]\n"
    "s_add_u32 m0, s68, 4736\n"
    "ds_read_b64_tr_b16 a[226:227], v202 offset:40960\n"
    "ds_read_b64_tr_b16 a[228:229], v206 offset:32768\n"
    "v_mfma_f32_16x16x32_bf16 a[52:55], a[236:239], v[156:159], a[52:55]\n"
    "buffer_load_dwordx4 v210, s[44:47], s55 offen offset:384 nt lds\n"
    "ds_read_b64_tr_b16 a[230:231], v206 offset:40960\n"
    "v_mfma_f32_16x16x32_bf16 a[52:55], a[232:235], v[144:147], a[52:55]\n"
    "ds_read_b64_tr_b16 a[232:233], v204 offset:32768\n"
    "v_add_f32_e64 v240, v240, v186\n"
    "v_fma_f32 v193, v129, s58, v232\n"
    "v_mfma_f32_16x16x32_bf16 a[56:59], a[240:243], v[144:147], a[56:59]\n"
    "s_add_u32 m0, s68, 5888\n"
    "v_exp_f32_e64 v190, v190\n"
    "v_mfma_f32_16x16x32_bf16 a[56:59], a[244:247], v[156:159], a[56:59]\n"
    "ds_read_b64_tr_b16 a[234:235], v204 offset:40960\n"
    "ds_read_b64_tr_b16 a[236:237], v208 offset:32768\n"
    "ds_read_b64_tr_b16 a[238:239], v208 offset:40960\n"
    "v_mfma_f32_16x16x32_bf16 a[120:123], a[244:247], v[160:163], a[120:123]\n"
    "buffer_load_dwordx4 v210, s[44:47], s69 offen offset:256 nt lds\n"
    "v_add_f32_e64 v240, v240, v187\n"
    "v_mfma_f32_16x16x32_bf16 a[120:123], a[240:243], v[148:151], a[120:123]\n"
    "v_cvt_pk_bf16_f32 v173, v186, v187\n"
    "v_fma_f32 v194, v130, s58, v232\n"
    "v_mfma_f32_16x16x32_bf16 v[56:59], a[240:243], v[152:155], v[56:59]\n"
    "ds_read_b64_tr_b16 a[240:241], v202 offset:33280\n"
    "v_exp_f32_e64 v191, v191\n"
    "v_mfma_f32_16x16x32_bf16 v[60:63], a[248:251], v[152:155], v[60:63]\n"
    "ds_read_b64_tr_b16 a[242:243], v202 offset:41472\n"
    "v_add_f32_e64 v240, v240, v188\n"
    "v_fma_f32 v195, v131, s58, v232\n"
    "v_mfma_f32_16x16x32_bf16 a[124:127], a[248:251], v[148:151], a[124:127]\n"
    "v_exp_f32_e64 v192, v192\n"
    "v_mfma_f32_16x16x32_bf16 a[124:127], a[252:255], v[160:163], a[124:127]\n"
    "s_add_u32 m0, s68, 6784\n"
    "ds_read_b64_tr_b16 a[244:245], v206 offset:33280\n"
    "ds_read_b64_tr_b16 a[246:247], v206 offset:41472\n"
    "v_add_f32_e64 v240, v240, v189\n"
    "v_mfma_f32_16x16x32_bf16 a[60:63], a[252:255], v[156:159], a[60:63]\n"
    "buffer_load_dwordx4 v210, s[44:47], s69 offen offset:384 nt lds\n"
    "v_cvt_pk_bf16_f32 v174, v188, v189\n"
    "v_mfma_f32_16x16x32_bf16 a[60:63], a[248:251], v[144:147], a[60:63]\n"
    "v_fma_f32 v196, v136, s58, v232\n"
    "v_exp_f32 v193, v193\n"
    "v_add_f32 v240, v240, v190\n"
    "v_fma_f32 v197, v137, s58, v232\n"
    "v_exp_f32 v194, v194\n"
    "v_add_f32 v240, v240, v191\n"
    "v_cvt_pk_bf16_f32 v175, v190, v191\n"
    "v_fma_f32 v198, v138, s58, v232\n"
    "v_add_f32 v238, v238, v192\n"
    "v_exp_f32 v195, v195\n"
    "v_fma_f32 v199, v139, s58, v232\n"
    "v_exp_f32 v196, v196\n"
    "v_add_f32 v238, v238, v193\n"
    "v_cvt_pk_bf16_f32 v176, v192, v193\n"
    "v_fma_f32 v184, v132, s58, v233\n"
    "v_exp_f32 v197, v197\n"
    "v_add_f32 v238, v238, v194\n"
    "v_fma_f32 v185, v133, s58, v233\n"
    "v_exp_f32 v198, v198\n"
    "v_add_f32 v238, v238, v195\n"
    "v_cvt_pk_bf16_f32 v177, v194, v195\n"
    "v_fma_f32 v186, v134, s58, v233\n"
    "v_exp_f32 v199, v199\n"
    "v_add_f32 v238, v238, v196\n"
    "v_fma_f32 v187, v135, s58, v233\n"
    "v_exp_f32 v184, v184\n"
    "v_add_f32 v238, v238, v197\n"
    "v_cvt_pk_bf16_f32 v178, v196, v197\n"
    "v_fma_f32 v188, v140, s58, v233\n"
    "v_exp_f32 v185, v185\n"
    "v_add_f32 v238, v238, v198\n"
    "v_fma_f32 v189, v141, s58, v233\n"
    "v_exp_f32 v186, v186\n"
    "v_add_f32 v238, v238, v199\n"
    "v_cvt_pk_bf16_f32 v179, v198, v199\n"
    "v_fma_f32 v190, v142, s58, v233\n"
    "v_exp_f32 v187, v187\n"
    "v_add_f32 v239, v239, v184\n"
    "v_fma_f32 v191, v143, s58, v233\n"
    "v_add_f32 v239, v239, v185\n"
    "v_exp_f32 v188, v188\n"
    "v_cvt_pk_bf16_f32 v180, v184, v185\n"
    "v_exp_f32 v189, v189\n"
    "v_add_f32 v239, v239, v186\n"
    "v_exp_f32 v190, v190\n"
    "v_add_f32 v239, v239, v187\n"
    "v_cvt_pk_bf16_f32 v181, v186, v187\n"
    "v_exp_f32 v191, v191\n"
    "v_add_f32 v239, v239, v188\n"
    "v_add_f32_e64 v239, v239, v189\n"
    "v_cvt_pk_bf16_f32 v182, v188, v189\n"
    "v_add_f32_e64 v239, v239, v190\n"
    "v_add_f32 v239, v239, v191\n"
    "v_cvt_pk_bf16_f32 v183, v190, v191\n"
    "v_max3_f32 v243, v238, v239, v240\n"
    "v_cmp_lt_f32 vcc, s59, v243\n"
    "s_cbranch_vccnz LrareP14t_%=\n"
    "LbackP14t_%=:\n"
    "s_waitcnt lgkmcnt(8)\n"
    "v_mfma_f32_16x16x32_bf16 a[0:3], a[224:227], v[164:167], a[0:3]\n"
    "s_add_u32 s24, s55, 128\n"
    "ds_read_b64_tr_b16 a[248:249], v204 offset:33280\n"
    "v_add_f32_e64 v236, v236, v238\n"
    "v_mfma_f32_16x16x32_bf16 a[0:3], a[228:231], v[176:179], a[0:3]\n"
    "s_add_u32 s25, s69, 128\n"
    "s_add_u32 s26, s56, 128\n"
    "ds_read_b64_tr_b16 a[250:251], v204 offset:41472\n"
    "v_add_f32 v237, v237, v239\n"
    "v_add_f32 v235, v235, v240\n"
    "v_mov_b32 v247, v235\n"
    "v_mov_b32 v243, v236\n"
    "v_mov_b32 v245, v237\n"
    "v_accvgpr_read_b32 v104, a0\n"
    "v_mfma_f32_16x16x32_bf16 a[64:67], a[228:231], v[180:183], a[64:67]\n"
    "s_add_u32 s27, s70, 128\n"
    "v_permlane32_swap_b32 v235, v247\n"
    "v_mfma_f32_16x16x32_bf16 a[64:67], a[224:227], v[168:171], a[64:67]\n"
    "s_add_u32 s28, s57, 0\n"
    "s_add_u32 s29, s71, 0\n"
    "ds_read_b64_tr_b16 a[252:253], v208 offset:33280\n"
    "ds_read_b64_tr_b16 a[254:255], v208 offset:41472\n"
    "v_permlane32_swap_b32 v236, v243\n"
    "v_permlane32_swap_b32 v237, v245\n"
    "v_add_f32 v235, v235, v247\n"
    "v_mfma_f32_16x16x32_bf16 v[0:3], a[224:227], v[172:175], v[0:3]\n"
    "s_waitcnt lgkmcnt(8)\n"
    "ds_read_b64_tr_b16 a[224:225], v202 offset:33792\n"
    "v_add_f32_e64 v236, v236, v243\n"
    "v_mfma_f32_16x16x32_bf16 v[4:7], a[232:235], v[172:175], v[4:7]\n"
    "s_add_u32 s30, s57, 128\n"
    "s_add_u32 s31, s71, 128\n"
    "ds_read_b64_tr_b16 a[226:227], v202 offset:41984\n"
    "v_add_f32 v237, v237, v245\n"
    "v_mov_b32 v247, v235\n"
    "v_mov_b32 v243, v236\n"
    "v_mov_b32 v245, v237\n"
    "v_permlane16_swap_b32 v235, v247\n"
    "v_mfma_f32_16x16x32_bf16 a[68:71], a[232:235], v[168:171], a[68:71]\n"
    "s_mov_b32 s46, 0\n"
    "ds_write_b128 v217, v[0:3] offset:0\n"
    "v_mfma_f32_16x16x32_bf16 a[68:71], a[236:239], v[180:183], a[68:71]\n"
    "s_mov_b32 s65, s68\n"
    "s_mov_b32 s61, 1\n"
    "ds_read_b64_tr_b16 a[228:229], v206 offset:33792\n"
    "ds_read_b64_tr_b16 a[230:231], v206 offset:41984\n"
    "v_permlane16_swap_b32 v236, v243\n"
    "v_add_f32 v235, v235, v247\n"
    "v_mfma_f32_16x16x32_bf16 a[4:7], a[236:239], v[176:179], a[4:7]\n"
    "v_permlane16_swap_b32 v237, v245\n"
    "v_mfma_f32_16x16x32_bf16 a[4:7], a[232:235], v[164:167], a[4:7]\n"
    "s_waitcnt lgkmcnt(9)\n"
    "ds_read_b64_tr_b16 a[232:233], v204 offset:33792\n"
    "ds_write_b64 v219, v[234:235] offset:0\n"
    "v_add_f32 v236, v236, v243\n"
    "v_add_f32 v237, v237, v245\n"
    "v_rcp_f32_e64 v242, v236\n"
    "v_mfma_f32_16x16x32_bf16 a[8:11], a[240:243], v[164:167], a[8:11]\n"
    "ds_read_b64_tr_b16 a[234:235], v204 offset:41984\n"
    "v_rcp_f32_e64 v244, v237\n"
    "v_accvgpr_read_b32 v108, a64\n"
    "v_accvgpr_read_b32 v105, a1\n"
    "v_pk_mul_f32 v[104:105], v[104:105], v[242:243] op_sel_hi:[1,0]\n"
    "v_accvgpr_read_b32 v109, a65\n"
    "v_pk_mul_f32 v[108:109], v[108:109], v[244:245] op_sel_hi:[1,0]\n"
    "v_mfma_f32_16x16x32_bf16 a[8:11], a[244:247], v[176:179], a[8:11]\n"
    "s_waitcnt lgkmcnt(1)\n"
    "ds_read_b64_tr_b16 a[236:237], v208 offset:33792\n"
    "v_accvgpr_read_b32 v106, a2\n"
    "v_mfma_f32_16x16x32_bf16 a[72:75], a[244:247], v[180:183], a[72:75]\n"
    "s_barrier\n"
    "ds_read_b64_tr_b16 a[238:239], v208 offset:41984\n"
    "ds_read_b64 v[250:251], v220 offset:0\n"
    "ds_write_b128 v217, v[4:7] offset:1024\n"
    "v_accvgpr_read_b32 v110, a66\n"
    "v_mfma_f32_16x16x32_bf16 a[72:75], a[240:243], v[168:171], a[72:75]\n"
    "s_nop 0\n"
    "v_accvgpr_read_b32 v107, a3\n"
    "v_accvgpr_read_b32 v111, a67\n"
    "v_mfma_f32_16x16x32_bf16 v[8:11], a[240:243], v[172:175], v[8:11]\n"
    "s_waitcnt lgkmcnt(12)\n"
    "ds_read_b64_tr_b16 a[240:241], v202 offset:34304\n"
    "v_cvt_pk_bf16_f32 v120, v104, v105\n"
    "v_cvt_pk_bf16_f32 v122, v108, v109\n"
    "v_accvgpr_read_b32 v112, a4\n"
    "v_accvgpr_read_b32 v116, a68\n"
    "v_pk_mul_f32 v[106:107], v[106:107], v[242:243] op_sel_hi:[1,0]\n"
    "v_pk_mul_f32 v[110:111], v[110:111], v[244:245] op_sel_hi:[1,0]\n"
    "v_cvt_pk_bf16_f32 v121, v106, v107\n"
    "v_mfma_f32_16x16x32_bf16 v[12:15], a[248:251], v[172:175], v[12:15]\n"
    "ds_read_b64_tr_b16 a[242:243], v202 offset:42496\n"
    "v_cvt_pk_bf16_f32 v123, v110, v111\n"
    "v_mfma_f32_16x16x32_bf16 a[76:79], a[248:251], v[168:171], a[76:79]\n"
    "s_waitcnt lgkmcnt(3)\n"
    "s_waitcnt vmcnt(4)\n"
    "ds_read_b64_tr_b16 a[244:245], v206 offset:34304\n"
    "ds_read_b128 a[160:163], v212 offset:32768\n"
    "ds_read_b128 a[164:167], v212 offset:33280\n"
    "ds_read_b128 a[168:171], v212 offset:33792\n"
    "ds_read_b128 a[172:175], v212 offset:34304\n"
    "ds_read_b128 a[176:179], v212 offset:34816\n"
    "v_min_f32_e64 v243, v234, v250\n"
    "v_mfma_f32_16x16x32_bf16 a[76:79], a[252:255], v[180:183], a[76:79]\n"
    "ds_read_b64_tr_b16 a[246:247], v206 offset:42496\n"
    "v_sub_f32_e64 v245, v243, v234\n"
    "v_mfma_f32_16x16x32_bf16 a[12:15], a[252:255], v[176:179], a[12:15]\n"
    "ds_read_b128 a[180:183], v212 offset:35328\n"
    "v_sub_f32 v247, v243, v250\n"
    "v_exp_f32 v245, v245\n"
    "v_exp_f32_e64 v247, v247\n"
    "v_mul_f32 v249, v235, v245\n"
    "v_fma_f32 v249, v251, v247, v249\n"
    "v_mfma_f32_16x16x32_bf16 a[12:15], a[248:251], v[164:167], a[12:15]\n"
    "s_waitcnt lgkmcnt(15)\n"
    "ds_read_b64_tr_b16 a[248:249], v204 offset:34304\n"
    "ds_read_b128 a[184:187], v212 offset:35840\n"
    "ds_read_b128 a[188:191], v212 offset:36352\n"
    "v_rcp_f32_e64 v249, v249\n"
    "v_accvgpr_read_b32 v113, a5\n"
    "v_pk_mul_f32 v[112:113], v[112:113], v[242:243] op_sel_hi:[1,0]\n"
    "v_mul_f32_e64 v246, v245, v249\n"
    "v_mfma_f32_16x16x32_bf16 a[16:19], a[224:227], v[164:167], a[16:19]\n"
    "ds_read_b64_tr_b16 a[250:251], v204 offset:42496\n"
    "v_mul_f32_e64 v248, v247, v249\n"
    "v_mfma_f32_16x16x32_bf16 a[16:19], a[228:231], v[176:179], a[16:19]\n"
    "ds_read_b64_tr_b16 a[252:253], v208 offset:34304\n"
    "v_accvgpr_read_b32 v117, a69\n"
    "v_accvgpr_read_b32 v114, a6\n"
    "v_accvgpr_read_b32 v118, a70\n"
    "v_accvgpr_read_b32 v115, a7\n"
    "v_pk_mul_f32 v[116:117], v[116:117], v[244:245] op_sel_hi:[1,0]\n"
    "v_pk_mul_f32 v[114:115], v[114:115], v[242:243] op_sel_hi:[1,0]\n"
    "v_accvgpr_read_b32 v119, a71\n"
    "v_mfma_f32_16x16x32_bf16 a[80:83], a[228:231], v[180:183], a[80:83]\n"
    "ds_write_b64 v221, v[120:121] offset:0\n"
    "v_mfma_f32_16x16x32_bf16 a[80:83], a[224:227], v[168:171], a[80:83]\n"
    "ds_read_b64_tr_b16 a[254:255], v208 offset:42496\n"
    "v_cvt_pk_bf16_f32 v124, v112, v113\n"
    "v_cvt_pk_bf16_f32 v126, v116, v117\n"
    "v_cvt_pk_bf16_f32 v125, v114, v115\n"
    "v_accvgpr_read_b32 v104, a8\n"
    "v_pk_mul_f32 v[118:119], v[118:119], v[244:245] op_sel_hi:[1,0]\n"
    "v_cvt_pk_bf16_f32 v127, v118, v119\n"
    "v_mfma_f32_16x16x32_bf16 v[16:19], a[224:227], v[172:175], v[16:19]\n"
    "s_waitcnt lgkmcnt(15)\n"
    "ds_write_b64 v221, v[122:123] offset:2048\n"
    "v_mfma_f32_16x16x32_bf16 v[20:23], a[232:235], v[172:175], v[20:23]\n"
    "s_waitcnt lgkmcnt(5)\n"
    "ds_read_b64_tr_b16 a[224:225], v203 offset:32768\n"
    "v_accvgpr_read_b32 v108, a72\n"
    "v_accvgpr_read_b32 v105, a9\n"
    "v_accvgpr_read_b32 v109, a73\n"
    "v_accvgpr_read_b32 v106, a10\n"
    "v_pk_mul_f32 v[104:105], v[104:105], v[242:243] op_sel_hi:[1,0]\n"
    "v_pk_mul_f32 v[108:109], v[108:109], v[244:245] op_sel_hi:[1,0]\n"
    "v_mfma_f32_16x16x32_bf16 a[84:87], a[232:235], v[168:171], a[84:87]\n"
    "s_add_u32 m0, s54, 32768\n"
    "ds_write_b64 v222, v[124:125] offset:0\n"
    "v_mfma_f32_16x16x32_bf16 a[84:87], a[236:239], v[180:183], a[84:87]\n"
    "buffer_load_dwordx4 v210, s[36:39], s52 offen offset:0 lds\n"
    "ds_read_b64_tr_b16 a[226:227], v203 offset:40960\n"
    "ds_read_b64_tr_b16 a[228:229], v207 offset:32768\n"
    "ds_read_b64_tr_b16 a[230:231], v207 offset:40960\n"
    "v_accvgpr_read_b32 v110, a74\n"
    "v_accvgpr_read_b32 v107, a11\n"
    "v_mfma_f32_16x16x32_bf16 a[20:23], a[236:239], v[176:179], a[20:23]\n"
    "s_add_u32 m0, s54, 33664\n"
    "ds_write_b64 v222, v[126:127] offset:2048\n"
    "v_accvgpr_read_b32 v111, a75\n"
    "v_cvt_pk_bf16_f32 v120, v104, v105\n"
    "v_pk_mul_f32 v[106:107], v[106:107], v[242:243] op_sel_hi:[1,0]\n"
    "v_pk_mul_f32 v[110:111], v[110:111], v[244:245] op_sel_hi:[1,0]\n"
    "v_cvt_pk_bf16_f32 v122, v108, v109\n"
    "v_mfma_f32_16x16x32_bf16 a[20:23], a[232:235], v[164:167], a[20:23]\n"
    "s_waitcnt lgkmcnt(15)\n"
    "ds_write_b128 v217, v[8:11] offset:2048\n"
    "v_mfma_f32_16x16x32_bf16 a[24:27], a[240:243], v[164:167], a[24:27]\n"
    "s_nop 0\n"
    "ds_read_b64_tr_b16 a[232:233], v205 offset:32768\n"
    "v_cvt_pk_bf16_f32 v121, v106, v107\n"
    "v_cvt_pk_bf16_f32 v123, v110, v111\n"
    "v_accvgpr_read_b32 v112, a12\n"
    "v_accvgpr_read_b32 v116, a76\n"
    "v_accvgpr_read_b32 v113, a13\n"
    "v_mfma_f32_16x16x32_bf16 a[24:27], a[244:247], v[176:179], a[24:27]\n"
    "buffer_load_dwordx4 v210, s[36:39], s52 offen offset:128 lds\n"
    "v_mfma_f32_16x16x32_bf16 a[88:91], a[244:247], v[180:183], a[88:91]\n"
    "ds_read_b64_tr_b16 a[234:235], v205 offset:40960\n"
    "ds_write_b64 v223, v[120:121] offset:0\n"
    "v_accvgpr_read_b32 v117, a77\n"
    "v_pk_mul_f32 v[112:113], v[112:113], v[242:243] op_sel_hi:[1,0]\n"
    "v_pk_mul_f32 v[116:117], v[116:117], v[244:245] op_sel_hi:[1,0]\n"
    "v_accvgpr_read_b32 v114, a14\n"
    "v_mfma_f32_16x16x32_bf16 a[88:91], a[240:243], v[168:171], a[88:91]\n"
    "s_nop 0\n"
    "ds_write_b64 v223, v[122:123] offset:2048\n"
    "v_mfma_f32_16x16x32_bf16 v[24:27], a[240:243], v[172:175], v[24:27]\n"
    "s_waitcnt lgkmcnt(12)\n"
    "ds_read_b64_tr_b16 a[236:237], v209 offset:32768\n"
    "ds_read_b64_tr_b16 a[238:239], v209 offset:40960\n"
    "v_accvgpr_read_b32 v118, a78\n"
    "v_accvgpr_read_b32 v115, a15\n"
    "v_accvgpr_read_b32 v119, a79\n"
    "v_pk_mul_f32 v[114:115], v[114:115], v[242:243] op_sel_hi:[1,0]\n"
    "v_pk_mul_f32 v[118:119], v[118:119], v[244:245] op_sel_hi:[1,0]\n"
    "v_mfma_f32_16x16x32_bf16 v[28:31], a[248:251], v[172:175], v[28:31]\n"
    "s_add_u32 m0, s54, 34560\n"
    "ds_write_b128 v217, v[12:15] offset:3072\n"
    "v_mfma_f32_16x16x32_bf16 a[92:95], a[248:251], v[168:171], a[92:95]\n"
    "buffer_load_dwordx4 v210, s[36:39], s52 offen offset:256 lds\n"
    "ds_read_b64_tr_b16 a[240:241], v203 offset:33280\n"
    "ds_read_b64_tr_b16 a[242:243], v203 offset:41472\n"
    "v_cvt_pk_bf16_f32 v124, v112, v113\n"
    "v_cvt_pk_bf16_f32 v126, v116, v117\n"
    "v_mfma_f32_16x16x32_bf16 a[92:95], a[252:255], v[180:183], a[92:95]\n"
    "ds_read_b64_tr_b16 a[244:245], v207 offset:33280\n"
    "ds_read_b64_tr_b16 a[246:247], v207 offset:41472\n"
    "v_cvt_pk_bf16_f32 v125, v114, v115\n"
    "v_cvt_pk_bf16_f32 v127, v118, v119\n"
    "v_accvgpr_read_b32 v104, a16\n"
    "v_accvgpr_read_b32 v108, a80\n"
    "v_accvgpr_read_b32 v105, a17\n"
    "v_pk_mul_f32 v[104:105], v[104:105], v[242:243] op_sel_hi:[1,0]\n"
    "v_mfma_f32_16x16x32_bf16 a[28:31], a[252:255], v[176:179], a[28:31]\n"
    "s_nop 0\n"
    "ds_write_b64 v224, v[124:125] offset:0\n"
    "v_mfma_f32_16x16x32_bf16 a[28:31], a[248:251], v[164:167], a[28:31]\n"
    "s_waitcnt lgkmcnt(14)\n"
    "ds_read_b64_tr_b16 a[248:249], v205 offset:33280\n"
    "ds_read_b128 v[128:131], v225 offset:0\n"
    "ds_read_b128 v[132:135], v225 offset:1024\n"
    "v_accvgpr_read_b32 v109, a81\n"
    "v_accvgpr_read_b32 v106, a18\n"
    "v_pk_mul_f32 v[108:109], v[108:109], v[244:245] op_sel_hi:[1,0]\n"
    "v_accvgpr_read_b32 v110, a82\n"
    "v_mfma_f32_16x16x32_bf16 a[32:35], a[224:227], v[164:167], a[32:35]\n"
    "s_add_u32 m0, s54, 35456\n"
    "ds_read_b64_tr_b16 a[250:251], v205 offset:41472\n"
    "v_accvgpr_read_b32 v107, a19\n"
    "v_mfma_f32_16x16x32_bf16 a[32:35], a[228:231], v[176:179], a[32:35]\n"
    "buffer_load_dwordx4 v210, s[36:39], s52 offen offset:384 lds\n"
    "ds_read_b64_tr_b16 a[252:253], v209 offset:33280\n"
    "ds_write_b64 v224, v[126:127] offset:2048\n"
    "ds_read_b128 v[136:139], v225 offset:2048\n"
    "ds_read_b128 v[140:143], v225 offset:3072\n"
    "v_mfma_f32_16x16x32_bf16 a[96:99], a[228:231], v[180:183], a[96:99]\n"
    "s_add_u32 s52, s52, 16384\n"
    "ds_read_b64_tr_b16 a[254:255], v209 offset:41472\n"
    "v_accvgpr_read_b32 v111, a83\n"
    "v_mfma_f32_16x16x32_bf16 a[96:99], a[224:227], v[168:171], a[96:99]\n"
    "v_cvt_pk_bf16_f32 v120, v104, v105\n"
    "v_cvt_pk_bf16_f32 v122, v108, v109\n"
    "v_accvgpr_read_b32 v112, a20\n"
    "v_accvgpr_read_b32 v116, a84\n"
    "v_accvgpr_read_b32 v113, a21\n"
    "v_pk_mul_f32 v[106:107], v[106:107], v[242:243] op_sel_hi:[1,0]\n"
    "v_pk_mul_f32 v[110:111], v[110:111], v[244:245] op_sel_hi:[1,0]\n"
    "v_pk_mul_f32 v[112:113], v[112:113], v[242:243] op_sel_hi:[1,0]\n"
    "v_mfma_f32_16x16x32_bf16 v[32:35], a[224:227], v[172:175], v[32:35]\n"
    "s_waitcnt lgkmcnt(15)\n"
    "ds_write_b128 v217, v[16:19] offset:16384\n"
    "v_mfma_f32_16x16x32_bf16 v[36:39], a[232:235], v[172:175], v[36:39]\n"
    "s_waitcnt lgkmcnt(7)\n"
    "ds_read_b64_tr_b16 a[224:225], v203 offset:33792\n"
    "buffer_store_dwordx4 v[128:131], v215, s[48:51], s55 offen offset:0\n"
    "v_cvt_pk_bf16_f32 v121, v106, v107\n"
    "v_cvt_pk_bf16_f32 v123, v110, v111\n"
    "v_accvgpr_read_b32 v117, a85\n"
    "v_mfma_f32_16x16x32_bf16 a[100:103], a[232:235], v[168:171], a[100:103]\n"
    "s_add_u32 m0, s54, 49152\n"
    "s_waitcnt vmcnt(5)\n"
    "ds_read_b64_tr_b16 a[226:227], v203 offset:41984\n"
    "ds_read_b128 a[144:147], v214 offset:0\n"
    "ds_read_b128 a[148:151], v214 offset:512\n"
    "ds_read_b128 a[152:155], v214 offset:1024\n"
    "ds_read_b128 a[156:159], v214 offset:1536\n"
    "v_pk_mul_f32 v[116:117], v[116:117], v[244:245] op_sel_hi:[1,0]\n"
    "v_accvgpr_read_b32 v114, a22\n"
    "v_mfma_f32_16x16x32_bf16 a[100:103], a[236:239], v[180:183], a[100:103]\n"
    "s_waitcnt lgkmcnt(8)\n"
    "ds_read_b64_tr_b16 a[228:229], v207 offset:33792\n"
    "ds_read_b64_tr_b16 a[230:231], v207 offset:41984\n"
    "v_mfma_f32_16x16x32_bf16 a[36:39], a[236:239], v[176:179], a[36:39]\n"
    "v_accvgpr_read_b32 v118, a86\n"
    "v_accvgpr_read_b32 v115, a23\n"
    "v_accvgpr_read_b32 v119, a87\n"
    "v_cvt_pk_bf16_f32 v124, v112, v113\n"
    "v_cvt_pk_bf16_f32 v126, v116, v117\n"
    "v_pk_mul_f32 v[114:115], v[114:115], v[242:243] op_sel_hi:[1,0]\n"
    "v_pk_mul_f32 v[118:119], v[118:119], v[244:245] op_sel_hi:[1,0]\n"
    "v_cvt_pk_bf16_f32 v125, v114, v115\n"
    "v_mfma_f32_16x16x32_bf16 a[36:39], a[232:235], v[164:167], a[36:39]\n"
    "s_waitcnt lgkmcnt(15)\n"
    "buffer_load_dwordx4 v210, s[36:39], s52 offen offset:0 lds\n"
    "v_mfma_f32_16x16x32_bf16 a[40:43], a[240:243], v[164:167], a[40:43]\n"
    "Lpad5_%=:\n"
    "s_cbranch_execz Lhopd1_%=\n"
    "ds_read_b64_tr_b16 a[232:233], v205 offset:33792\n"
    "ds_write_b64 v221, v[120:121] offset:0\n"
    "v_cvt_pk_bf16_f32 v127, v118, v119\n"
    "v_accvgpr_read_b32 v104, a24\n"
    "v_accvgpr_read_b32 v108, a88\n"
    "v_accvgpr_read_b32 v105, a25\n"
    "v_mfma_f32_16x16x32_bf16 a[40:43], a[244:247], v[176:179], a[40:43]\n"
    "s_waitcnt lgkmcnt(4)\n"
    "ds_write_b64 v221, v[122:123] offset:2048\n"
    "v_mfma_f32_16x16x32_bf16 a[104:107], a[244:247], v[180:183], a[104:107]\n"
    "s_nop 0\n"
    "ds_read_b64_tr_b16 a[234:235], v205 offset:41984\n"
    "v_accvgpr_read_b32 v109, a89\n"
    "v_accvgpr_read_b32 v106, a26\n"
    "v_accvgpr_read_b32 v110, a90\n"
    "v_accvgpr_read_b32 v107, a27\n"
    "v_pk_mul_f32 v[104:105], v[104:105], v[242:243] op_sel_hi:[1,0]\n"
    "v_pk_mul_f32 v[108:109], v[108:109], v[244:245] op_sel_hi:[1,0]\n"
    "v_mfma_f32_16x16x32_bf16 a[104:107], a[240:243], v[168:171], a[104:107]\n"
    "s_add_u32 m0, s54, 50048\n"
    "ds_write_b128 v217, v[20:23] offset:17408\n"
    "v_mfma_f32_16x16x32_bf16 v[40:43], a[240:243], v[172:175], v[40:43]\n"
    "s_waitcnt lgkmcnt(14)\n"
    "buffer_load_dwordx4 v210, s[36:39], s52 offen offset:128 lds\n"
    "ds_read_b64_tr_b16 a[236:237], v209 offset:33792\n"
    "ds_read_b64_tr_b16 a[238:239], v209 offset:41984\n"
    "v_accvgpr_read_b32 v111, a91\n"
    "v_pk_mul_f32 v[106:107], v[106:107], v[242:243] op_sel_hi:[1,0]\n"
    "v_mfma_f32_16x16x32_bf16 v[44:47], a[248:251], v[172:175], v[44:47]\n"
    "s_nop 0\n"
    "ds_read_b64_tr_b16 a[240:241], v203 offset:34304\n"
    "v_cvt_pk_bf16_f32 v120, v104, v105\n"
    "v_cvt_pk_bf16_f32 v122, v108, v109\n"
    "v_cvt_pk_bf16_f32 v121, v106, v107\n"
    "v_accvgpr_read_b32 v112, a28\n"
    "v_pk_mul_f32 v[110:111], v[110:111], v[244:245] op_sel_hi:[1,0]\n"
    "v_cvt_pk_bf16_f32 v123, v110, v111\n"
    "v_accvgpr_read_b32 v116, a92\n"
    "v_mfma_f32_16x16x32_bf16 a[108:111], a[248:251], v[168:171], a[108:111]\n"
    "ds_write_b64 v222, v[124:125] offset:0\n"
    "v_mfma_f32_16x16x32_bf16 a[108:111], a[252:255], v[180:183], a[108:111]\n"
    "ds_read_b64_tr_b16 a[242:243], v203 offset:42496\n"
    "ds_read_b64_tr_b16 a[244:245], v207 offset:34304\n"
    "ds_read_b64_tr_b16 a[246:247], v207 offset:42496\n"
    "v_accvgpr_read_b32 v113, a29\n"
    "v_accvgpr_read_b32 v117, a93\n"
    "v_pk_mul_f32 v[112:113], v[112:113], v[242:243] op_sel_hi:[1,0]\n"
    "v_pk_mul_f32 v[116:117], v[116:117], v[244:245] op_sel_hi:[1,0]\n"
    "v_mfma_f32_16x16x32_bf16 a[44:47], a[252:255], v[176:179], a[44:47]\n"
    "s_nop 0\n"
    "buffer_store_dwordx4 v[136:139], v215, s[48:51], s56 offen offset:0\n"
    "v_mfma_f32_16x16x32_bf16 a[44:47], a[248:251], v[164:167], a[44:47]\n"
    "s_waitcnt lgkmcnt(12)\n"
    "ds_read_b64_tr_b16 a[248:249], v205 offset:34304\n"
    "ds_read_b64_tr_b16 a[250:251], v205 offset:42496\n"
    "ds_read_b64_tr_b16 a[252:253], v209 offset:34304\n"
    "ds_read_b64_tr_b16 a[254:255], v209 offset:42496\n"
    "v_accvgpr_read_b32 v114, a30\n"
    "v_accvgpr_read_b32 v118, a94\n"
    "v_accvgpr_read_b32 v115, a31\n"
    "v_pk_mul_f32 v[114:115], v[114:115], v[242:243] op_sel_hi:[1,0]\n"
    "v_mfma_f32_16x16x32_bf16 a[48:51], a[224:227], v[164:167], a[48:51]\n"
    "s_add_u32 m0, s54, 50944\n"
    "ds_write_b64 v222, v[126:127] offset:2048\n"
    "v_mfma_f32_16x16x32_bf16 a[48:51], a[228:231], v[176:179], a[48:51]\n"
    "v_accvgpr_read_b32 v119, a95\n"
    "v_cvt_pk_bf16_f32 v124, v112, v113\n"
    "v_cvt_pk_bf16_f32 v126, v116, v117\n"
    "v_cvt_pk_bf16_f32 v125, v114, v115\n"
    "v_accvgpr_read_b32 v104, a32\n"
    "v_pk_mul_f32 v[118:119], v[118:119], v[244:245] op_sel_hi:[1,0]\n"
    "v_cvt_pk_bf16_f32 v127, v118, v119\n"
    "v_mfma_f32_16x16x32_bf16 a[112:115], a[228:231], v[180:183], a[112:115]\n"
    "ds_write_b128 v217, v[24:27] offset:18432\n"
    "v_mfma_f32_16x16x32_bf16 a[112:115], a[224:227], v[168:171], a[112:115]\n"
    "s_nop 0\n"
    "buffer_load_dwordx4 v210, s[36:39], s52 offen offset:256 lds\n"
    "v_accvgpr_read_b32 v108, a96\n"
    "v_accvgpr_read_b32 v105, a33\n"
    "v_accvgpr_read_b32 v109, a97\n"
    "v_pk_mul_f32 v[104:105], v[104:105], v[242:243] op_sel_hi:[1,0]\n"
    "v_mfma_f32_16x16x32_bf16 v[48:51], a[224:227], v[172:175], v[48:51]\n"
    "s_waitcnt lgkmcnt(11)\n"
    "ds_write_b64 v223, v[120:121] offset:0\n"
    "v_accvgpr_read_b32 v106, a34\n"
    "v_accvgpr_read_b32 v110, a98\n"
    "v_pk_mul_f32 v[108:109], v[108:109], v[244:245] op_sel_hi:[1,0]\n"
    "v_accvgpr_read_b32 v107, a35\n"
    "v_pk_mul_f32 v[106:107], v[106:107], v[242:243] op_sel_hi:[1,0]\n"
    "v_mfma_f32_16x16x32_bf16 v[52:55], a[232:235], v[172:175], v[52:55]\n"
    "ds_write_b64 v223, v[122:123] offset:2048\n"
    "v_mfma_f32_16x16x32_bf16 a[116:119], a[232:235], v[168:171], a[116:119]\n"
    "s_add_u32 m0, s54, 51840\n"
    "ds_write_b128 v217, v[28:31] offset:19456\n"
    "v_accvgpr_read_b32 v111, a99\n"
    "v_pk_mul_f32 v[110:111], v[110:111], v[244:245] op_sel_hi:[1,0]\n"
    "v_mfma_f32_16x16x32_bf16 a[116:119], a[236:239], v[180:183], a[116:119]\n"
    "s_waitcnt vmcnt(29) lgkmcnt(0)\n"
    "buffer_load_dwordx4 v210, s[36:39], s52 offen offset:384 lds\n"
    "v_mfma_f32_16x16x32_bf16 a[52:55], a[236:239], v[176:179], a[52:55]\n"
    "s_barrier\n"
    "ds_read_b128 a[224:227], v200 offset:0\n"
    "ds_read_b128 a[228:231], v200 offset:512\n"
    "ds_read_b128 v[0:3], v218 offset:0\n"
    "ds_read_b128 v[4:7], v218 offset:1024\n"
    "ds_read_b128 v[8:11], v218 offset:2048\n"
    "v_cvt_pk_bf16_f32 v120, v104, v105\n"
    "v_cvt_pk_bf16_f32 v122, v108, v109\n"
    "v_cvt_pk_bf16_f32 v121, v106, v107\n"
    "v_mfma_f32_16x16x32_bf16 a[52:55], a[232:235], v[164:167], a[52:55]\n"
    "s_add_u32 s52, s52, 16384\n"
    "ds_read_b128 a[232:235], v200 offset:1024\n"
    "ds_read_b128 v[12:15], v218 offset:3072\n"
    "v_mfma_f32_16x16x32_bf16 a[56:59], a[240:243], v[164:167], a[56:59]\n"
    "ds_read_b128 a[236:239], v200 offset:1536\n"
    "v_cvt_pk_bf16_f32 v123, v110, v111\n"
    "v_accvgpr_read_b32 v112, a36\n"
    "v_accvgpr_read_b32 v116, a100\n"
    "v_accvgpr_read_b32 v113, a37\n"
    "v_pk_mul_f32 v[112:113], v[112:113], v[242:243] op_sel_hi:[1,0]\n"
    "v_accvgpr_read_b32 v117, a101\n"
    "v_pk_mul_f32 v[116:117], v[116:117], v[244:245] op_sel_hi:[1,0]\n"
    "v_mfma_f32_16x16x32_bf16 a[56:59], a[244:247], v[176:179], a[56:59]\n"
    "buffer_store_dwordx4 v[132:135], v215, s[48:51], s69 offen offset:0\n"
    "v_mfma_f32_16x16x32_bf16 a[120:123], a[244:247], v[180:183], a[120:123]\n"
    "ds_read_b128 v[16:19], v218 offset:16384\n"
    "ds_read_b128 v[20:23], v218 offset:17408\n"
    "ds_read_b128 v[24:27], v218 offset:18432\n"
    "ds_read_b128 v[28:31], v218 offset:19456\n"
    "v_accvgpr_read_b32 v114, a38\n"
    "v_accvgpr_read_b32 v118, a102\n"
    "v_accvgpr_read_b32 v115, a39\n"
    "v_pk_mul_f32 v[114:115], v[114:115], v[242:243] op_sel_hi:[1,0]\n"
    "v_mfma_f32_16x16x32_bf16 a[120:123], a[240:243], v[168:171], a[120:123]\n"
    "v_accvgpr_read_b32 v119, a103\n"
    "v_accvgpr_read_b32 v104, a40\n"
    "v_accvgpr_read_b32 v108, a104\n"
    "v_accvgpr_read_b32 v105, a41\n"
    "v_accvgpr_read_b32 v109, a105\n"
    "v_pk_mul_f32 v[118:119], v[118:119], v[244:245] op_sel_hi:[1,0]\n"
    "v_pk_mul_f32 v[104:105], v[104:105], v[242:243] op_sel_hi:[1,0]\n"
    "v_pk_mul_f32 v[108:109], v[108:109], v[244:245] op_sel_hi:[1,0]\n"
    "v_mfma_f32_16x16x32_bf16 v[56:59], a[240:243], v[172:175], v[56:59]\n"
    "ds_write_b64 v224, v[124:125] offset:0\n"
    "v_mfma_f32_16x16x32_bf16 v[60:63], a[248:251], v[172:175], v[60:63]\n"
    "s_nop 0\n"
    "ds_read_b128 a[240:243], v200 offset:2048\n"
    "buffer_store_dwordx4 v[140:143], v215, s[48:51], s70 offen offset:0\n"
    "ds_read_b128 v[128:131], v225 offset:0\n"
    "ds_read_b128 v[132:135], v225 offset:1024\n"
    "v_cvt_pk_bf16_f32 v124, v112, v113\n"
    "v_cvt_pk_bf16_f32 v125, v114, v115\n"
    "v_mfma_f32_16x16x32_bf16 a[124:127], a[248:251], v[168:171], a[124:127]\n"
    "s_waitcnt lgkmcnt(4)\n"
    "ds_read_b128 a[244:247], v200 offset:2560\n"
    "v_accvgpr_read_b32 v106, a42\n"
    "v_mfma_f32_16x16x32_bf16 a[124:127], a[252:255], v[180:183], a[124:127]\n"
    "s_add_u32 m0, s54, 65536\n"
    "v_accvgpr_read_b32 v110, a106\n"
    "v_accvgpr_read_b32 v107, a43\n"
    "v_accvgpr_read_b32 v111, a107\n"
    "v_accvgpr_read_b32 v112, a44\n"
    "v_accvgpr_read_b32 v113, a45\n"
    "v_pk_mul_f32 v[106:107], v[106:107], v[242:243] op_sel_hi:[1,0]\n"
    "v_pk_mul_f32 v[110:111], v[110:111], v[244:245] op_sel_hi:[1,0]\n"
    "v_pk_mul_f32 v[112:113], v[112:113], v[242:243] op_sel_hi:[1,0]\n"
    "v_mfma_f32_16x16x32_bf16 a[60:63], a[252:255], v[176:179], a[60:63]\n"
    "ds_write_b64 v224, v[126:127] offset:2048\n"
    "v_mfma_f32_16x16x32_bf16 a[60:63], a[248:251], v[164:167], a[60:63]\n"
    "s_waitcnt lgkmcnt(15)\n"
    "ds_read_b128 a[248:251], v200 offset:3072\n"
    "ds_read_b128 v[136:139], v225 offset:2048\n"
    "ds_read_b128 v[140:143], v225 offset:3072\n"
    "v_cvt_pk_bf16_f32 v126, v116, v117\n"
    "v_cvt_pk_bf16_f32 v127, v118, v119\n"
    "v_accvgpr_read_b32 v116, a108\n"
    "v_accvgpr_read_b32 v117, a109\n"
    "v_mfma_f32_16x16x32_bf16 v[64:67], a[224:227], a[128:131], 0\n"
    "s_waitcnt lgkmcnt(5)\n"
    "buffer_load_dwordx4 v211, s[40:43], s53 offen offset:0 lds\n"
    "v_mfma_f32_16x16x32_bf16 v[64:67], a[228:231], a[132:135], v[64:67]\n"
    "s_add_u32 m0, s54, 66432\n"
    "ds_read_b128 a[252:255], v200 offset:3584\n"
    "v_accvgpr_read_b32 v114, a46\n"
    "v_accvgpr_read_b32 v118, a110\n"
    "v_accvgpr_read_b32 v115, a47\n"
    "v_accvgpr_read_b32 v119, a111\n"
    "v_pk_mul_f32 v[116:117], v[116:117], v[244:245] op_sel_hi:[1,0]\n"
    "v_pk_mul_f32 v[114:115], v[114:115], v[242:243] op_sel_hi:[1,0]\n"
    "v_pk_mul_f32 v[118:119], v[118:119], v[244:245] op_sel_hi:[1,0]\n"
    "v_mfma_f32_16x16x32_bf16 v[68:71], a[228:231], a[164:167], 0\n"
    "ds_write_b64 v221, v[120:121] offset:0\n"
    "v_mfma_f32_16x16x32_bf16 v[68:71], a[224:227], a[160:163], v[68:71]\n"
    "v_mfma_f32_16x16x32_bf16 v[72:75], a[224:227], a[192:195], 0\n"
    "s_waitcnt lgkmcnt(2)\n"
    "ds_read_b128 a[224:227], v200 offset:8192\n"
    "ds_write_b64 v221, v[122:123] offset:2048\n"
    "v_cvt_pk_bf16_f32 v120, v104, v105\n"
    "v_cvt_pk_bf16_f32 v121, v106, v107\n"
    "v_cvt_pk_bf16_f32 v122, v108, v109\n"
    "v_cvt_pk_bf16_f32 v123, v110, v111\n"
    "v_mfma_f32_16x16x32_bf16 v[72:75], a[228:231], a[196:199], v[72:75]\n"
    "s_waitcnt lgkmcnt(15)\n"
    "buffer_load_dwordx4 v211, s[40:43], s53 offen offset:128 lds\n"
    "v_mfma_f32_16x16x32_bf16 v[72:75], a[232:235], a[200:203], v[72:75]\n"
    "s_add_u32 m0, s54, 67328\n"
    "ds_read_b128 a[228:231], v200 offset:8704\n"
    "ds_write_b64 v222, v[124:125] offset:0\n"
    "v_accvgpr_read_b32 v104, a48\n"
    "v_accvgpr_read_b32 v108, a112\n"
    "v_cvt_pk_bf16_f32 v124, v112, v113\n"
    "v_cvt_pk_bf16_f32 v125, v114, v115\n"
    "v_mfma_f32_16x16x32_bf16 v[72:75], a[236:239], a[204:207], v[72:75]\n"
    "ds_write_b64 v222, v[126:127] offset:2048\n"
    "v_mfma_f32_16x16x32_bf16 v[68:71], a[236:239], a[172:175], v[68:71]\n"
    "v_accvgpr_read_b32 v105, a49\n"
    "v_cvt_pk_bf16_f32 v126, v116, v117\n"
    "v_cvt_pk_bf16_f32 v127, v118, v119\n"
    "v_accvgpr_read_b32 v109, a113\n"
    "v_accvgpr_read_b32 v106, a50\n"
    "v_pk_mul_f32 v[104:105], v[104:105], v[242:243] op_sel_hi:[1,0]\n"
    "v_pk_mul_f32 v[108:109], v[108:109], v[244:245] op_sel_hi:[1,0]\n"
    "v_mfma_f32_16x16x32_bf16 v[68:71], a[232:235], a[168:171], v[68:71]\n"
    "ds_write_b64 v223, v[120:121] offset:0\n"
    "v_mfma_f32_16x16x32_bf16 v[64:67], a[232:235], a[136:139], v[64:67]\n"
    "s_nop 0\n"
    "buffer_load_dwordx4 v211, s[40:43], s53 offen offset:256 lds\n"
    "ds_read_b128 a[232:235], v200 offset:9216\n"
    "v_accvgpr_read_b32 v110, a114\n"
    "v_accvgpr_read_b32 v107, a51\n"
    "v_pk_mul_f32 v[106:107], v[106:107], v[242:243] op_sel_hi:[1,0]\n"
    "v_pk_mul_f32 v[0:1], v[0:1], v[248:249] op_sel_hi:[1,0]\n"
    "v_mfma_f32_16x16x32_bf16 v[64:67], a[236:239], a[140:143], v[64:67]\n"
    "s_waitcnt lgkmcnt(13)\n"
    "ds_read_b128 a[236:239], v200 offset:9728\n"
    "ds_write_b64 v223, v[122:123] offset:2048\n"
    "v_pk_mul_f32 v[4:5], v[4:5], v[248:249] op_sel_hi:[1,0]\n"
    "v_pk_mul_f32 v[2:3], v[2:3], v[248:249] op_sel_hi:[1,0]\n"
    "v_pk_mul_f32 v[6:7], v[6:7], v[248:249] op_sel_hi:[1,0]\n"
    "v_pk_fma_f32 v[32:33], v[32:33], v[246:247], v[0:1] op_sel_hi:[1,0,1]\n"
    "v_mfma_f32_16x16x32_bf16 v[64:67], a[240:243], a[144:147], v[64:67]\n"
    "s_add_u32 m0, s54, 68224\n"
    "ds_write_b64 v224, v[124:125] offset:0\n"
    "v_mfma_f32_16x16x32_bf16 v[64:67], a[244:247], a[148:151], v[64:67]\n"
    "v_accvgpr_read_b32 v111, a115\n"
    "v_cvt_pk_bf16_f32 v120, v104, v105\n"
    "v_cvt_pk_bf16_f32 v122, v108, v109\n"
    "v_cvt_pk_bf16_f32 v121, v106, v107\n"
    "v_accvgpr_read_b32 v112, a52\n"
    "v_pk_mul_f32 v[110:111], v[110:111], v[244:245] op_sel_hi:[1,0]\n"
    "v_pk_fma_f32 v[36:37], v[36:37], v[246:247], v[4:5] op_sel_hi:[1,0,1]\n"
    "v_mfma_f32_16x16x32_bf16 v[68:71], a[244:247], a[180:183], v[68:71]\n"
    "buffer_load_dwordx4 v211, s[40:43], s53 offen offset:384 lds\n"
    "v_mfma_f32_16x16x32_bf16 v[68:71], a[240:243], a[176:179], v[68:71]\n"
    "s_add_u32 s53, s53, 16384\n"
    "ds_write_b64 v224, v[126:127] offset:2048\n"
    "v_cvt_pk_bf16_f32 v123, v110, v111\n"
    "v_accvgpr_read_b32 v116, a116\n"
    "v_pk_fma_f32 v[34:35], v[34:35], v[246:247], v[2:3] op_sel_hi:[1,0,1]\n"
    "v_pk_fma_f32 v[38:39], v[38:39], v[246:247], v[6:7] op_sel_hi:[1,0,1]\n"
    "v_pk_mul_f32 v[8:9], v[8:9], v[248:249] op_sel_hi:[1,0]\n"
    "v_mfma_f32_16x16x32_bf16 v[72:75], a[240:243], a[208:211], v[72:75]\n"
    "s_add_u32 m0, s54, 81920\n"
    "ds_read_b128 a[240:243], v200 offset:10240\n"
    "v_accvgpr_read_b32 v113, a53\n"
    "v_mfma_f32_16x16x32_bf16 v[72:75], a[244:247], a[212:215], v[72:75]\n"
    "s_waitcnt lgkmcnt(13)\n"
    "ds_read_b128 a[244:247], v200 offset:10752\n"
    "v_accvgpr_read_b32 v117, a117\n"
    "v_accvgpr_read_b32 v114, a54\n"
    "v_accvgpr_read_b32 v118, a118\n"
    "v_accvgpr_read_b32 v115, a55\n"
    "v_pk_mul_f32 v[112:113], v[112:113], v[242:243] op_sel_hi:[1,0]\n"
    "v_pk_mul_f32 v[116:117], v[116:117], v[244:245] op_sel_hi:[1,0]\n"
    "v_pk_mul_f32 v[114:115], v[114:115], v[242:243] op_sel_hi:[1,0]\n"
    "v_mfma_f32_16x16x32_bf16 v[72:75], a[248:251], a[216:219], v[72:75]\n"
    "s_nop 0\n"
    "v_accvgpr_read_b32 v119, a119\n"
    "v_cvt_pk_bf16_f32 v124, v112, v113\n"
    "v_mfma_f32_16x16x32_bf16 v[72:75], a[252:255], a[220:223], v[72:75]\n"
    "buffer_load_dwordx4 v211, s[40:43], s53 offen offset:0 lds\n"
    "v_cvt_pk_bf16_f32 v126, v116, v117\n"
    "v_cvt_pk_bf16_f32 v125, v114, v115\n"
    "v_accvgpr_read_b32 v104, a56\n"
    "v_pk_mul_f32 v[118:119], v[118:119], v[244:245] op_sel_hi:[1,0]\n"
    "v_pk_mul_f32 v[12:13], v[12:13], v[248:249] op_sel_hi:[1,0]\n"
    "v_pk_mul_f32 v[10:11], v[10:11], v[248:249] op_sel_hi:[1,0]\n"
    "v_mfma_f32_16x16x32_bf16 v[68:71], a[252:255], a[188:191], v[68:71]\n"
    "s_add_u32 m0, s54, 82816\n"
    "v_cvt_pk_bf16_f32 v127, v118, v119\n"
    "v_accvgpr_read_b32 v108, a120\n"
    "v_accvgpr_read_b32 v105, a57\n"
    "v_accvgpr_read_b32 v109, a121\n"
    "v_accvgpr_read_b32 v106, a58\n"
    "v_pk_mul_f32 v[104:105], v[104:105], v[242:243] op_sel_hi:[1,0]\n"
    "v_pk_mul_f32 v[108:109], v[108:109], v[244:245] op_sel_hi:[1,0]\n"
    "v_pk_mul_f32 v[14:15], v[14:15], v[248:249] op_sel_hi:[1,0]\n"
    "v_mfma_f32_16x16x32_bf16 v[68:71], a[248:251], a[184:187], v[68:71]\n"
    "v_accvgpr_read_b32 v110, a122\n"
    "v_accvgpr_read_b32 v107, a59\n"
    "v_mfma_f32_16x16x32_bf16 v[64:67], a[248:251], a[152:155], v[64:67]\n"
    "s_nop 0\n"
    "ds_read_b128 a[248:251], v200 offset:11264\n"
    "v_accvgpr_read_b32 v111, a123\n"
    "v_accvgpr_read_b32 v112, a60\n"
    "v_accvgpr_read_b32 v116, a124\n"
    "v_accvgpr_read_b32 v113, a61\n"
    "v_pk_mul_f32 v[106:107], v[106:107], v[242:243] op_sel_hi:[1,0]\n"
    "v_pk_mul_f32 v[110:111], v[110:111], v[244:245] op_sel_hi:[1,0]\n"
    "v_pk_mul_f32 v[112:113], v[112:113], v[242:243] op_sel_hi:[1,0]\n"
    "v_mfma_f32_16x16x32_bf16 v[64:67], a[252:255], a[156:159], v[64:67]\n"
    "s_waitcnt lgkmcnt(11)\n"
    "buffer_load_dwordx4 v211, s[40:43], s53 offen offset:128 lds\n"
    "v_mfma_f32_16x16x32_bf16 v[76:79], a[224:227], a[128:131], 0\n"
    "s_add_u32 m0, s54, 83712\n"
    "ds_read_b128 a[252:255], v200 offset:11776\n"
    "v_accvgpr_read_b32 v117, a125\n"
    "v_accvgpr_read_b32 v114, a62\n"
    "v_accvgpr_read_b32 v118, a126\n"
    "v_accvgpr_read_b32 v115, a63\n"
    "v_pk_mul_f32 v[116:117], v[116:117], v[244:245] op_sel_hi:[1,0]\n"
    "v_pk_mul_f32 v[114:115], v[114:115], v[242:243] op_sel_hi:[1,0]\n"
    "v_pk_fma_f32 v[40:41], v[40:41], v[246:247], v[8:9] op_sel_hi:[1,0,1]\n"
    "v_mfma_f32_16x16x32_bf16 v[76:79], a[228:231], a[132:135], v[76:79]\n"
    "v_accvgpr_read_b32 v119, a127\n"
    "v_mov_b32_e64 v226, v225\n"
    "v_mfma_f32_16x16x32_bf16 v[80:83], a[228:231], a[164:167], 0\n"
    "v_max3_f32 v243, v64, v65, v66\n"
    "v_max3_f32 v245, v68, v69, v70\n"
    "v_max3_f32 v247, v72, v73, v74\n"
    "v_max_f32 v243, v243, v67\n"
    "v_max_f32 v245, v245, v71\n"
    "v_pk_mul_f32 v[118:119], v[118:119], v[244:245] op_sel_hi:[1,0]\n"
    "v_pk_fma_f32 v[44:45], v[44:45], v[246:247], v[12:13] op_sel_hi:[1,0,1]\n"
    "v_pk_fma_f32 v[42:43], v[42:43], v[246:247], v[10:11] op_sel_hi:[1,0,1]\n"
    "v_mfma_f32_16x16x32_bf16 v[80:83], a[224:227], a[160:163], v[80:83]\n"
    "v_max_f32 v247, v247, v75\n"
    "v_mov_b32 v249, v243\n"
    "v_mfma_f32_16x16x32_bf16 v[84:87], a[224:227], a[192:195], 0\n"
    "buffer_load_dwordx4 v211, s[40:43], s53 offen offset:256 lds\n"
    "ds_read_b128 a[224:227], v201 offset:0\n"
    "v_mov_b32_e64 v252, v245\n"
    "v_mov_b32 v253, v247\n"
    "v_pk_fma_f32 v[46:47], v[46:47], v[246:247], v[14:15] op_sel_hi:[1,0,1]\n"
    "v_pk_mul_f32 v[16:17], v[16:17], v[248:249] op_sel_hi:[1,0]\n"
    "v_pk_mul_f32 v[20:21], v[20:21], v[248:249] op_sel_hi:[1,0]\n"
    "v_mfma_f32_16x16x32_bf16 v[84:87], a[228:231], a[196:199], v[84:87]\n"
    "s_waitcnt lgkmcnt(8)\n"
    "ds_read_b128 a[228:231], v201 offset:512\n"
    "v_permlane32_swap_b32 v243, v249\n"
    "v_permlane32_swap_b32 v245, v252\n"
    "v_pk_mul_f32 v[18:19], v[18:19], v[248:249] op_sel_hi:[1,0]\n"
    "v_pk_mul_f32 v[22:23], v[22:23], v[248:249] op_sel_hi:[1,0]\n"
    "v_pk_fma_f32 v[48:49], v[48:49], v[246:247], v[16:17] op_sel_hi:[1,0,1]\n"
    "v_mfma_f32_16x16x32_bf16 v[84:87], a[232:235], a[200:203], v[84:87]\n"
    "s_add_u32 m0, s54, 84608\n"
    "v_permlane32_swap_b32 v247, v253\n"
    "v_mfma_f32_16x16x32_bf16 v[84:87], a[236:239], a[204:207], v[84:87]\n"
    "v_max_f32 v243, v243, v249\n"
    "v_max_f32 v245, v245, v252\n"
    "v_max_f32 v247, v247, v253\n"
    "v_mov_b32 v249, v243\n"
    "v_mov_b32 v252, v245\n"
    "v_pk_fma_f32 v[52:53], v[52:53], v[246:247], v[20:21] op_sel_hi:[1,0,1]\n"
    "v_pk_fma_f32 v[50:51], v[50:51], v[246:247], v[18:19] op_sel_hi:[1,0,1]\n"
    "v_pk_fma_f32 v[54:55], v[54:55], v[246:247], v[22:23] op_sel_hi:[1,0,1]\n"
    "v_mfma_f32_16x16x32_bf16 v[80:83], a[236:239], a[172:175], v[80:83]\n"
    "buffer_load_dwordx4 v211, s[40:43], s53 offen offset:384 lds\n"
    "v_mfma_f32_16x16x32_bf16 v[80:83], a[232:235], a[168:171], v[80:83]\n"
    "s_add_u32 s53, s53, 16384\n"
    "v_mov_b32_e64 v253, v247\n"
    "v_permlane16_swap_b32 v243, v249\n"
    "v_permlane16_swap_b32 v245, v252\n"
    "v_pk_mul_f32 v[24:25], v[24:25], v[248:249] op_sel_hi:[1,0]\n"
    "v_pk_mul_f32 v[28:29], v[28:29], v[248:249] op_sel_hi:[1,0]\n"
    "v_pk_mul_f32 v[26:27], v[26:27], v[248:249] op_sel_hi:[1,0]\n"
    "v_mfma_f32_16x16x32_bf16 v[76:79], a[232:235], a[136:139], v[76:79]\n"
    "v_permlane16_swap_b32 v247, v253\n"
    "v_mfma_f32_16x16x32_bf16 v[76:79], a[236:239], a[140:143], v[76:79]\n"
    "s_waitcnt lgkmcnt(4)\n"
    "ds_read_b128 a[232:235], v201 offset:1024\n"
    "ds_read_b128 a[236:239], v201 offset:1536\n"
    "v_max_f32 v243, v243, v249\n"
    "v_max_f32 v245, v245, v252\n"
    "v_max_f32_e64 v247, v247, v253\n"
    "v_pk_mul_f32 v[30:31], v[30:31], v[248:249] op_sel_hi:[1,0]\n"
    "v_pk_fma_f32 v[56:57], v[56:57], v[246:247], v[24:25] op_sel_hi:[1,0,1]\n"
    "v_pk_fma_f32 v[60:61], v[60:61], v[246:247], v[28:29] op_sel_hi:[1,0,1]\n"
    "v_mfma_f32_16x16x32_bf16 v[76:79], a[240:243], a[144:147], v[76:79]\n"
    "v_mul_f32 v232, s64, v243\n"
    "v_mul_f32 v233, s64, v245\n"
    "v_mfma_f32_16x16x32_bf16 v[76:79], a[244:247], a[148:151], v[76:79]\n"
    "buffer_store_dwordx4 v[128:131], v215, s[48:51], s55 offen offset:128\n"
    "ds_read_b128 v[128:131], v225 offset:0\n"
    "v_mul_f32_e64 v234, s64, v247\n"
    "v_fma_f32 v184, v64, s58, v232\n"
    "v_pk_fma_f32 v[58:59], v[58:59], v[246:247], v[26:27] op_sel_hi:[1,0,1]\n"
    "v_pk_fma_f32 v[62:63], v[62:63], v[246:247], v[30:31] op_sel_hi:[1,0,1]\n"
    "v_fma_f32 v185, v65, s58, v232\n"
    "v_mfma_f32_16x16x32_bf16 v[80:83], a[244:247], a[180:183], v[80:83]\n"
    "v_fma_f32 v186, v66, s58, v232\n"
    "v_fma_f32 v187, v67, s58, v232\n"
    "v_exp_f32_e64 v184, v184\n"
    "v_fma_f32 v188, v68, s58, v233\n"
    "v_exp_f32_e64 v185, v185\n"
    "v_fma_f32 v189, v69, s58, v233\n"
    "v_mfma_f32_16x16x32_bf16 v[80:83], a[240:243], a[176:179], v[80:83]\n"
    "v_exp_f32_e64 v186, v186\n"
    "v_mfma_f32_16x16x32_bf16 v[84:87], a[240:243], a[208:211], v[84:87]\n"
    "ds_read_b128 a[240:243], v201 offset:2048\n"
    "v_fma_f32 v190, v70, s58, v233\n"
    "v_exp_f32_e64 v187, v187\n"
    "v_fma_f32 v191, v71, s58, v233\n"
    "v_exp_f32_e64 v188, v188\n"
    "v_add_f32 v238, v184, v185\n"
    "v_mfma_f32_16x16x32_bf16 v[84:87], a[244:247], a[212:215], v[84:87]\n"
    "s_waitcnt lgkmcnt(6)\n"
    "buffer_store_dwordx4 v[136:139], v215, s[48:51], s56 offen offset:128\n"
    "v_mfma_f32_16x16x32_bf16 v[84:87], a[248:251], a[216:219], v[84:87]\n"
    "ds_read_b128 a[244:247], v201 offset:2560\n"
    "ds_read_b128 v[136:139], v225 offset:2048\n"
    "v_cvt_pk_bf16_f32 v144, v184, v185\n"
    "v_fma_f32 v192, v72, s58, v234\n"
    "v_exp_f32 v189, v189\n"
    "v_add_f32 v238, v238, v186\n"
    "v_fma_f32 v193, v73, s58, v234\n"
    "v_mfma_f32_16x16x32_bf16 v[84:87], a[252:255], a[220:223], v[84:87]\n"
    "v_exp_f32_e64 v190, v190\n"
    "v_mfma_f32_16x16x32_bf16 v[80:83], a[252:255], a[188:191], v[80:83]\n"
    "v_add_f32_e64 v238, v238, v187\n"
    "v_cvt_pk_bf16_f32 v145, v186, v187\n"
    "v_fma_f32 v194, v74, s58, v234\n"
    "v_exp_f32_e64 v191, v191\n"
    "v_fma_f32 v195, v75, s58, v234\n"
    "v_exp_f32_e64 v192, v192\n"
    "v_mfma_f32_16x16x32_bf16 v[80:83], a[248:251], a[184:187], v[80:83]\n"
    "v_add_f32_e64 v239, v188, v189\n"
    "v_cvt_pk_bf16_f32 v148, v188, v189\n"
    "v_mfma_f32_16x16x32_bf16 v[76:79], a[248:251], a[152:155], v[76:79]\n"
    "ds_read_b128 a[248:251], v201 offset:3072\n"
    "buffer_store_dwordx4 v[132:135], v215, s[48:51], s69 offen offset:128\n"
    "ds_read_b128 v[132:135], v225 offset:1024\n"
    "v_exp_f32 v193, v193\n"
    "v_add_f32 v239, v239, v190\n"
    "v_add_f32 v239, v239, v191\n"
    "v_mfma_f32_16x16x32_bf16 v[76:79], a[252:255], a[156:159], v[76:79]\n"
    "s_waitcnt lgkmcnt(8)\n"
    "ds_read_b128 a[252:255], v201 offset:3584\n"
    "ds_write_b64 v221, v[120:121] offset:0\n"
    "v_exp_f32_e64 v194, v194\n"
    "v_cvt_pk_bf16_f32 v149, v190, v191\n"
    "v_cvt_pk_bf16_f32 v120, v104, v105\n"
    "v_mfma_f32_16x16x32_bf16 v[88:91], a[224:227], a[128:131], 0\n"
    "ds_write_b64 v222, v[124:125] offset:0\n"
    "v_mfma_f32_16x16x32_bf16 v[88:91], a[228:231], a[132:135], v[88:91]\n"
    "v_cvt_pk_bf16_f32 v121, v106, v107\n"
    "v_cvt_pk_bf16_f32 v124, v112, v113\n"
    "v_cvt_pk_bf16_f32 v125, v114, v115\n"
    "v_fma_f32 v196, v76, s58, v232\n"
    "v_fma_f32 v197, v77, s58, v232\n"
    "v_fma_f32 v198, v78, s58, v232\n"
    "v_fma_f32 v199, v79, s58, v232\n"
    "v_mfma_f32_16x16x32_bf16 v[92:95], a[228:231], a[164:167], 0\n"
    "s_waitcnt lgkmcnt(3)\n"
    "buffer_store_dwordx4 v[140:143], v215, s[48:51], s70 offen offset:128\n"
    "v_mfma_f32_16x16x32_bf16 v[92:95], a[224:227], a[160:163], v[92:95]\n"
    "s_waitcnt lgkmcnt(9)\n"
    "ds_read_b128 a[224:227], v201 offset:8192\n"
    "ds_read_b128 a[228:231], v201 offset:8704\n"
    "ds_read_b128 v[140:143], v225 offset:3072\n"
    "v_exp_f32 v195, v195\n"
    "v_exp_f32 v196, v196\n"
    "v_add_f32_e64 v240, v192, v193\n"
    "v_mfma_f32_16x16x32_bf16 v[92:95], a[232:235], a[168:171], v[92:95]\n"
    "ds_write_b64 v221, v[122:123] offset:2048\n"
    "v_mfma_f32_16x16x32_bf16 v[92:95], a[236:239], a[172:175], v[92:95]\n"
    "v_cvt_pk_bf16_f32 v152, v192, v193\n"
    "v_cvt_pk_bf16_f32 v122, v108, v109\n"
    "v_cvt_pk_bf16_f32 v123, v110, v111\n"
    "v_fma_f32 v184, v80, s58, v233\n"
    "v_exp_f32 v197, v197\n"
    "v_add_f32 v240, v240, v194\n"
    "v_mfma_f32_16x16x32_bf16 v[88:91], a[236:239], a[140:143], v[88:91]\n"
    "s_nop 0\n"
    "ds_write_b64 v222, v[126:127] offset:2048\n"
    "v_mfma_f32_16x16x32_bf16 v[88:91], a[232:235], a[136:139], v[88:91]\n"
    "s_waitcnt lgkmcnt(11)\n"
    "ds_read_b128 a[232:235], v201 offset:9216\n"
    "ds_read_b128 a[236:239], v201 offset:9728\n"
    "buffer_store_dwordx4 v[128:131], v216, s[48:51], s55 offen offset:0\n"
    "v_cvt_pk_bf16_f32 v126, v116, v117\n"
    "v_cvt_pk_bf16_f32 v127, v118, v119\n"
    "v_fma_f32 v185, v81, s58, v233\n"
    "v_mfma_f32_16x16x32_bf16 v[88:91], a[240:243], a[144:147], v[88:91]\n"
    "s_waitcnt lgkmcnt(4)\n"
    "ds_write_b64 v223, v[120:121] offset:0\n"
    "v_exp_f32 v198, v198\n"
    "v_add_f32_e64 v240, v240, v195\n"
    "v_cvt_pk_bf16_f32 v120, v32, v33\n"
    "v_cvt_pk_bf16_f32 v121, v34, v35\n"
    "v_mfma_f32_16x16x32_bf16 v[88:91], a[244:247], a[148:151], v[88:91]\n"
    "ds_write_b64 v223, v[122:123] offset:2048\n"
    "v_mfma_f32_16x16x32_bf16 v[92:95], a[244:247], a[180:183], v[92:95]\n"
    "v_cvt_pk_bf16_f32 v153, v194, v195\n"
    "v_cvt_pk_bf16_f32 v122, v36, v37\n"
    "v_cvt_pk_bf16_f32 v123, v38, v39\n"
    "v_fma_f32 v186, v82, s58, v233\n"
    "v_exp_f32_e64 v199, v199\n"
    "v_add_f32 v238, v238, v196\n"
    "v_mfma_f32_16x16x32_bf16 v[92:95], a[240:243], a[176:179], v[92:95]\n"
    "s_waitcnt lgkmcnt(11)\n"
    "buffer_store_dwordx4 v[136:139], v216, s[48:51], s56 offen offset:0\n"
    "v_mfma_f32_16x16x32_bf16 v[92:95], a[248:251], a[184:187], v[92:95]\n"
    "ds_read_b128 a[240:243], v201 offset:10240\n"
    "ds_read_b128 a[244:247], v201 offset:10752\n"
    "v_fma_f32 v187, v83, s58, v233\n"
    "v_exp_f32 v184, v184\n"
    "v_add_f32 v238, v238, v197\n"
    "v_cvt_pk_bf16_f32 v146, v196, v197\n"
    "v_fma_f32 v188, v84, s58, v234\n"
    "v_mfma_f32_16x16x32_bf16 v[92:95], a[252:255], a[188:191], v[92:95]\n"
    "ds_write_b64 v224, v[124:125] offset:0\n"
    "v_mfma_f32_16x16x32_bf16 v[88:91], a[252:255], a[156:159], v[88:91]\n"
    "v_exp_f32_e64 v185, v185\n"
    "v_cvt_pk_bf16_f32 v124, v40, v41\n"
    "v_cvt_pk_bf16_f32 v125, v42, v43\n"
    "v_add_f32_e64 v238, v238, v198\n"
    "v_fma_f32 v189, v85, s58, v234\n"
    "v_add_f32 v238, v238, v199\n"
    "v_mfma_f32_16x16x32_bf16 v[88:91], a[248:251], a[152:155], v[88:91]\n"
    "s_waitcnt lgkmcnt(10)\n"
    "ds_read_b128 a[248:251], v201 offset:11264\n"
    "ds_read_b128 a[252:255], v201 offset:11776\n"
    "v_mfma_f32_16x16x32_bf16 v[96:99], a[224:227], a[128:131], 0\n"
    "buffer_store_dwordx4 v[132:135], v216, s[48:51], s69 offen offset:0\n"
    "ds_write_b64 v224, v[126:127] offset:2048\n"
    "v_exp_f32_e64 v186, v186\n"
    "v_cvt_pk_bf16_f32 v147, v198, v199\n"
    "v_mfma_f32_16x16x32_bf16 v[96:99], a[228:231], a[132:135], v[96:99]\n"
    "ds_write_b64 v221, v[120:121] offset:4096\n"
    "v_cvt_pk_bf16_f32 v126, v44, v45\n"
    "v_cvt_pk_bf16_f32 v127, v46, v47\n"
    "v_cvt_pk_bf16_f32 v120, v48, v49\n"
    "v_cvt_pk_bf16_f32 v121, v50, v51\n"
    "v_fma_f32 v190, v86, s58, v234\n"
    "Lpad6_%=:\n"
    "s_cbranch_execz Lhopd1_%=\n"
    "v_mfma_f32_16x16x32_bf16 v[100:103], a[228:231], a[164:167], 0\n"
    "s_waitcnt vmcnt(17) lgkmcnt(0)\n"
    "ds_write_b64 v222, v[122:123] offset:4096\n"
    "v_mfma_f32_16x16x32_bf16 v[100:103], a[224:227], a[160:163], v[100:103]\n"
    "s_barrier\n"
    "ds_read_b128 a[224:227], v200 offset:32768\n"
    "ds_read_b128 a[228:231], v200 offset:33280\n"
    "buffer_store_dwordx4 v[140:143], v216, s[48:51], s70 offen offset:0\n"
    "v_cvt_pk_bf16_f32 v122, v52, v53\n"
    "v_cvt_pk_bf16_f32 v123, v54, v55\n"
    "v_add_f32 v239, v239, v184\n"
    "v_mfma_f32_16x16x32_bf16 v[100:103], a[232:235], a[168:171], v[100:103]\n"
    "s_add_u32 m0, s54, 0\n"
    "ds_write_b64 v223, v[124:125] offset:4096\n"
    "v_mfma_f32_16x16x32_bf16 v[100:103], a[236:239], a[172:175], v[100:103]\n"
    "v_exp_f32 v187, v187\n"
    "v_cvt_pk_bf16_f32 v124, v56, v57\n"
    "v_cvt_pk_bf16_f32 v125, v58, v59\n"
    "v_fma_f32 v191, v87, s58, v234\n"
    "v_exp_f32_e64 v188, v188\n"
    "v_mfma_f32_16x16x32_bf16 v[96:99], a[236:239], a[140:143], v[96:99]\n"
    "ds_write_b64 v224, v[126:127] offset:4096\n"
    "v_mfma_f32_16x16x32_bf16 v[96:99], a[232:235], a[136:139], v[96:99]\n"
    "ds_read_b128 a[232:235], v200 offset:33792\n"
    "v_cvt_pk_bf16_f32 v126, v60, v61\n"
    "v_cvt_pk_bf16_f32 v127, v62, v63\n"
    "v_add_f32_e64 v239, v239, v185\n"
    "v_cvt_pk_bf16_f32 v150, v184, v185\n"
    "v_fma_f32 v192, v88, s58, v232\n"
    "v_add_f32_e64 v239, v239, v186\n"
    "v_mfma_f32_16x16x32_bf16 v[96:99], a[240:243], a[144:147], v[96:99]\n"
    "buffer_load_dwordx4 v210, s[36:39], s52 offen offset:0 lds\n"
    "v_mfma_f32_16x16x32_bf16 v[96:99], a[244:247], a[148:151], v[96:99]\n"
    "s_add_u32 m0, s54, 896\n"
    "ds_read_b128 a[236:239], v200 offset:34304\n"
    "ds_write_b64 v221, v[120:121] offset:6144\n"
    "v_exp_f32_e64 v189, v189\n"
    "v_fma_f32 v193, v89, s58, v232\n"
    "v_add_f32_e64 v239, v239, v187\n"
    "v_mfma_f32_16x16x32_bf16 v[100:103], a[244:247], a[180:183], v[100:103]\n"
    "ds_write_b64 v222, v[122:123] offset:6144\n"
    "v_exp_f32_e64 v190, v190\n"
    "v_cvt_pk_bf16_f32 v151, v186, v187\n"
    "v_fma_f32 v194, v90, s58, v232\n"
    "v_add_f32_e64 v240, v240, v188\n"
    "v_mfma_f32_16x16x32_bf16 v[100:103], a[240:243], a[176:179], v[100:103]\n"
    "ds_write_b64 v223, v[124:125] offset:6144\n"
    "v_mfma_f32_16x16x32_bf16 v[100:103], a[248:251], a[184:187], v[100:103]\n"
    "ds_read_b128 a[240:243], v200 offset:34816\n"
    "v_exp_f32_e64 v191, v191\n"
    "v_fma_f32 v195, v91, s58, v232\n"
    "v_exp_f32 v192, v192\n"
    "v_add_f32 v240, v240, v189\n"
    "v_mfma_f32_16x16x32_bf16 v[100:103], a[252:255], a[188:191], v[100:103]\n"
    "buffer_load_dwordx4 v210, s[36:39], s52 offen offset:128 lds\n"
    "v_mfma_f32_16x16x32_bf16 v[96:99], a[252:255], a[156:159], v[96:99]\n"
    "s_add_u32 m0, s54, 1792\n"
    "ds_read_b128 a[244:247], v200 offset:35328\n"
    "ds_write_b64 v224, v[126:127] offset:6144\n"
    "v_cvt_pk_bf16_f32 v154, v188, v189\n"
    "v_fma_f32 v196, v92, s58, v233\n"
    "v_exp_f32 v193, v193\n"
    "v_mfma_f32_16x16x32_bf16 v[96:99], a[248:251], a[152:155], v[96:99]\n"
    "s_waitcnt lgkmcnt(10)\n"
    "ds_read_b128 a[248:251], v200 offset:35840\n"
    "v_add_f32_e64 v240, v240, v190\n"
    "v_mfma_f32_16x16x32_bf16 v[104:107], a[224:227], a[128:131], 0\n"
    "ds_read_b128 a[252:255], v200 offset:36352\n"
    "v_fma_f32 v197, v93, s58, v233\n"
    "v_mfma_f32_16x16x32_bf16 v[104:107], a[228:231], a[132:135], v[104:107]\n"
    "v_exp_f32_e64 v194, v194\n"
    "v_mfma_f32_16x16x32_bf16 v[108:111], a[228:231], a[164:167], 0\n"
    "v_add_f32_e64 v240, v240, v191\n"
    "v_cvt_pk_bf16_f32 v155, v190, v191\n"
    "v_mfma_f32_16x16x32_bf16 v[108:111], a[224:227], a[160:163], v[108:111]\n"
    "buffer_load_dwordx4 v210, s[36:39], s52 offen offset:256 lds\n"
    "v_mfma_f32_16x16x32_bf16 v[112:115], a[224:227], a[192:195], 0\n"
    "s_add_u32 m0, s54, 2688\n"
    "ds_read_b128 a[224:227], v200 offset:40960\n"
    "v_fma_f32 v198, v94, s58, v233\n"
    "v_mfma_f32_16x16x32_bf16 v[112:115], a[228:231], a[196:199], v[112:115]\n"
    "s_waitcnt lgkmcnt(9)\n"
    "v_exp_f32 v195, v195\n"
    "v_mfma_f32_16x16x32_bf16 v[112:115], a[232:235], a[200:203], v[112:115]\n"
    "ds_read_b128 a[228:231], v200 offset:41472\n"
    "v_add_f32_e64 v238, v238, v192\n"
    "v_mfma_f32_16x16x32_bf16 v[112:115], a[236:239], a[204:207], v[112:115]\n"
    "v_fma_f32 v199, v95, s58, v233\n"
    "v_add_f32_e64 v238, v238, v193\n"
    "v_mfma_f32_16x16x32_bf16 v[108:111], a[236:239], a[172:175], v[108:111]\n"
    "v_exp_f32_e64 v196, v196\n"
    "v_mfma_f32_16x16x32_bf16 v[108:111], a[232:235], a[168:171], v[108:111]\n"
    "v_cvt_pk_bf16_f32 v156, v192, v193\n"
    "v_fma_f32 v184, v96, s58, v232\n"
    "v_mfma_f32_16x16x32_bf16 v[104:107], a[232:235], a[136:139], v[104:107]\n"
    "v_exp_f32 v197, v197\n"
    "v_mfma_f32_16x16x32_bf16 v[104:107], a[236:239], a[140:143], v[104:107]\n"
    "s_waitcnt lgkmcnt(5)\n"
    "buffer_load_dwordx4 v210, s[36:39], s52 offen offset:384 lds\n"
    "v_mfma_f32_16x16x32_bf16 v[104:107], a[240:243], a[144:147], v[104:107]\n"
    "s_add_u32 s52, s52, 16384\n"
    "ds_read_b128 a[232:235], v200 offset:41984\n"
    "v_add_f32_e64 v238, v238, v194\n"
    "v_mfma_f32_16x16x32_bf16 v[104:107], a[244:247], a[148:151], v[104:107]\n"
    "s_add_u32 m0, s54, 16384\n"
    "ds_read_b128 a[236:239], v200 offset:42496\n"
    "v_fma_f32 v185, v97, s58, v232\n"
    "v_mfma_f32_16x16x32_bf16 v[108:111], a[244:247], a[180:183], v[108:111]\n"
    "v_exp_f32_e64 v198, v198\n"
    "v_mfma_f32_16x16x32_bf16 v[108:111], a[240:243], a[176:179], v[108:111]\n"
    "v_add_f32_e64 v238, v238, v195\n"
    "v_cvt_pk_bf16_f32 v157, v194, v195\n"
    "v_mfma_f32_16x16x32_bf16 v[112:115], a[240:243], a[208:211], v[112:115]\n"
    "ds_read_b128 a[240:243], v200 offset:43008\n"
    "v_fma_f32 v186, v98, s58, v232\n"
    "v_mfma_f32_16x16x32_bf16 v[112:115], a[244:247], a[212:215], v[112:115]\n"
    "s_waitcnt lgkmcnt(5)\n"
    "v_exp_f32 v199, v199\n"
    "v_mfma_f32_16x16x32_bf16 v[112:115], a[248:251], a[216:219], v[112:115]\n"
    "ds_read_b128 a[244:247], v200 offset:43520\n"
    "v_add_f32_e64 v239, v239, v196\n"
    "v_mfma_f32_16x16x32_bf16 v[112:115], a[252:255], a[220:223], v[112:115]\n"
    "v_fma_f32 v187, v99, s58, v232\n"
    "v_add_f32_e64 v239, v239, v197\n"
    "v_mfma_f32_16x16x32_bf16 v[108:111], a[252:255], a[188:191], v[108:111]\n"
    "buffer_load_dwordx4 v210, s[36:39], s52 offen offset:0 lds\n"
    "v_mfma_f32_16x16x32_bf16 v[108:111], a[248:251], a[184:187], v[108:111]\n"
    "s_add_u32 m0, s54, 17280\n"
    "v_exp_f32_e64 v184, v184\n"
    "v_mfma_f32_16x16x32_bf16 v[104:107], a[248:251], a[152:155], v[104:107]\n"
    "ds_read_b128 a[248:251], v200 offset:44032\n"
    "v_cvt_pk_bf16_f32 v160, v196, v197\n"
    "v_mfma_f32_16x16x32_bf16 v[104:107], a[252:255], a[156:159], v[104:107]\n"
    "s_waitcnt lgkmcnt(5)\n"
    "ds_read_b128 a[252:255], v200 offset:44544\n"
    "v_fma_f32 v188, v100, s58, v233\n"
    "v_mfma_f32_16x16x32_bf16 v[116:119], a[224:227], a[128:131], 0\n"
    "v_exp_f32 v185, v185\n"
    "v_mfma_f32_16x16x32_bf16 v[116:119], a[228:231], a[132:135], v[116:119]\n"
    "v_add_f32_e64 v239, v239, v198\n"
    "v_fma_f32 v189, v101, s58, v233\n"
    "v_mfma_f32_16x16x32_bf16 v[120:123], a[228:231], a[164:167], 0\n"
    "v_exp_f32_e64 v186, v186\n"
    "v_mfma_f32_16x16x32_bf16 v[120:123], a[224:227], a[160:163], v[120:123]\n"
    "v_add_f32_e64 v239, v239, v199\n"
    "v_cvt_pk_bf16_f32 v161, v198, v199\n"
    "v_mfma_f32_16x16x32_bf16 v[124:127], a[224:227], a[192:195], 0\n"
    "s_nop 0\n"
    "buffer_load_dwordx4 v210, s[36:39], s52 offen offset:128 lds\n"
    "v_mfma_f32_16x16x32_bf16 v[124:127], a[228:231], a[196:199], v[124:127]\n"
    "s_waitcnt lgkmcnt(4)\n"
    "ds_read_b128 a[224:227], v201 offset:32768\n"
    "v_fma_f32 v190, v102, s58, v233\n"
    "v_mfma_f32_16x16x32_bf16 v[124:127], a[232:235], a[200:203], v[124:127]\n"
    "s_add_u32 m0, s54, 18176\n"
    "v_exp_f32_e64 v187, v187\n"
    "v_mfma_f32_16x16x32_bf16 v[124:127], a[236:239], a[204:207], v[124:127]\n"
    "ds_read_b128 a[228:231], v201 offset:33280\n"
    "v_add_f32_e64 v238, v238, v184\n"
    "v_mfma_f32_16x16x32_bf16 v[120:123], a[236:239], a[172:175], v[120:123]\n"
    "v_fma_f32 v191, v103, s58, v233\n"
    "v_add_f32_e64 v238, v238, v185\n"
    "v_mfma_f32_16x16x32_bf16 v[120:123], a[232:235], a[168:171], v[120:123]\n"
    "v_exp_f32_e64 v188, v188\n"
    "v_mfma_f32_16x16x32_bf16 v[116:119], a[232:235], a[136:139], v[116:119]\n"
    "ds_read_b128 a[232:235], v201 offset:33792\n"
    "v_cvt_pk_bf16_f32 v158, v184, v185\n"
    "v_mfma_f32_16x16x32_bf16 v[116:119], a[236:239], a[140:143], v[116:119]\n"
    "s_waitcnt lgkmcnt(5)\n"
    "v_exp_f32 v189, v189\n"
    "v_mfma_f32_16x16x32_bf16 v[116:119], a[240:243], a[144:147], v[116:119]\n"
    "buffer_load_dwordx4 v210, s[36:39], s52 offen offset:256 lds\n"
    "v_mfma_f32_16x16x32_bf16 v[116:119], a[244:247], a[148:151], v[116:119]\n"
    "s_add_u32 m0, s54, 19072\n"
    "ds_read_b128 a[236:239], v201 offset:34304\n"
    "v_add_f32_e64 v238, v238, v186\n"
    "v_mfma_f32_16x16x32_bf16 v[120:123], a[244:247], a[180:183], v[120:123]\n"
    "v_exp_f32_e64 v190, v190\n"
    "v_mfma_f32_16x16x32_bf16 v[120:123], a[240:243], a[176:179], v[120:123]\n"
    "v_add_f32_e64 v238, v238, v187\n"
    "v_cvt_pk_bf16_f32 v159, v186, v187\n"
    "v_mfma_f32_16x16x32_bf16 v[124:127], a[240:243], a[208:211], v[124:127]\n"
    "v_exp_f32_e64 v191, v191\n"
    "v_mfma_f32_16x16x32_bf16 v[124:127], a[244:247], a[212:215], v[124:127]\n"
    "s_waitcnt lgkmcnt(4)\n"
    "ds_read_b128 a[240:243], v201 offset:34816\n"
    "v_add_f32 v239, v239, v188\n"
    "v_mfma_f32_16x16x32_bf16 v[124:127], a[248:251], a[216:219], v[124:127]\n"
    "ds_read_b128 a[244:247], v201 offset:35328\n"
    "v_add_f32_e64 v239, v239, v189\n"
    "v_mfma_f32_16x16x32_bf16 v[124:127], a[252:255], a[220:223], v[124:127]\n"
    "v_cvt_pk_bf16_f32 v162, v188, v189\n"
    "v_add_f32_e64 v239, v239, v190\n"
    "v_mfma_f32_16x16x32_bf16 v[120:123], a[252:255], a[188:191], v[120:123]\n"
    "buffer_load_dwordx4 v210, s[36:39], s52 offen offset:384 lds\n"
    "v_mfma_f32_16x16x32_bf16 v[120:123], a[248:251], a[184:187], v[120:123]\n"
    "s_add_u32 s52, s52, 16384\n"
    "v_add_f32_e64 v239, v239, v191\n"
    "v_cvt_pk_bf16_f32 v163, v190, v191\n"
    "v_mfma_f32_16x16x32_bf16 v[116:119], a[248:251], a[152:155], v[116:119]\n"
    "s_add_u32 m0, s54, 98304\n"
    "ds_read_b128 a[248:251], v201 offset:35840\n"
    "v_max3_f32 v243, v238, v239, v240\n"
    "v_mfma_f32_16x16x32_bf16 v[116:119], a[252:255], a[156:159], v[116:119]\n"
    "s_waitcnt lgkmcnt(5)\n"
    "ds_read_b128 a[252:255], v201 offset:36352\n"
    "v_cmp_lt_f32 vcc, s59, v243\n"
    "v_mfma_f32_16x16x32_bf16 v[128:131], a[224:227], a[128:131], 0\n"
    "v_mfma_f32_16x16x32_bf16 v[128:131], a[228:231], a[132:135], v[128:131]\n"
    "v_mfma_f32_16x16x32_bf16 v[132:135], a[228:231], a[164:167], 0\n"
    "s_nop 0\n"
    "v_mfma_f32_16x16x32_bf16 v[132:135], a[224:227], a[160:163], v[132:135]\n"
    "s_waitcnt lgkmcnt(4)\n"
    "ds_read_b128 a[224:227], v201 offset:40960\n"
    "ds_read_b128 a[228:231], v201 offset:41472\n"
    "v_mfma_f32_16x16x32_bf16 v[132:135], a[232:235], a[168:171], v[132:135]\n"
    "buffer_load_dwordx4 v211, s[40:43], s53 offen offset:0 lds\n"
    "v_mfma_f32_16x16x32_bf16 v[132:135], a[236:239], a[172:175], v[132:135]\n"
    "s_add_u32 m0, s54, 99200\n"
    "v_mfma_f32_16x16x32_bf16 v[128:131], a[236:239], a[140:143], v[128:131]\n"
    "s_nop 0\n"
    "v_mfma_f32_16x16x32_bf16 v[128:131], a[232:235], a[136:139], v[128:131]\n"
    "s_waitcnt lgkmcnt(4)\n"
    "ds_read_b128 a[232:235], v201 offset:41984\n"
    "ds_read_b128 a[236:239], v201 offset:42496\n"
    "v_mfma_f32_16x16x32_bf16 v[128:131], a[240:243], a[144:147], v[128:131]\n"
    "v_mfma_f32_16x16x32_bf16 v[128:131], a[244:247], a[148:151], v[128:131]\n"
    "v_mfma_f32_16x16x32_bf16 v[132:135], a[244:247], a[180:183], v[132:135]\n"
    "s_nop 0\n"
    "v_mfma_f32_16x16x32_bf16 v[132:135], a[240:243], a[176:179], v[132:135]\n"
    "s_waitcnt lgkmcnt(4)\n"
    "ds_read_b128 a[240:243], v201 offset:43008\n"
    "ds_read_b128 a[244:247], v201 offset:43520\n"
    "v_mfma_f32_16x16x32_bf16 v[132:135], a[248:251], a[184:187], v[132:135]\n"
    "v_mfma_f32_16x16x32_bf16 v[132:135], a[252:255], a[188:191], v[132:135]\n"
    "buffer_load_dwordx4 v211, s[40:43], s53 offen offset:128 lds\n"
    "v_mfma_f32_16x16x32_bf16 v[128:131], a[252:255], a[156:159], v[128:131]\n"
    "s_add_u32 m0, s54, 100096\n"
    "v_mfma_f32_16x16x32_bf16 v[128:131], a[248:251], a[152:155], v[128:131]\n"
    "s_waitcnt lgkmcnt(4)\n"
    "ds_read_b128 a[248:251], v201 offset:44032\n"
    "ds_read_b128 a[252:255], v201 offset:44544\n"
    "v_mfma_f32_16x16x32_bf16 v[136:139], a[224:227], a[128:131], 0\n"
    "s_nop 0\n"
    "v_mfma_f32_16x16x32_bf16 v[136:139], a[228:231], a[132:135], v[136:139]\n"
    "v_mfma_f32_16x16x32_bf16 v[140:143], a[228:231], a[164:167], 0\n"
    "s_nop 0\n"
    "v_mfma_f32_16x16x32_bf16 v[140:143], a[224:227], a[160:163], v[140:143]\n"
    "s_waitcnt lgkmcnt(4)\n"
    "v_mfma_f32_16x16x32_bf16 v[140:143], a[232:235], a[168:171], v[140:143]\n"
    "v_mfma_f32_16x16x32_bf16 v[140:143], a[236:239], a[172:175], v[140:143]\n"
    "s_waitcnt vmcnt(18) lgkmcnt(0)\n"
    "buffer_load_dwordx4 v211, s[40:43], s53 offen offset:256 lds\n"
    "v_mfma_f32_16x16x32_bf16 v[136:139], a[236:239], a[140:143], v[136:139]\n"
    "s_barrier\n"
    "ds_read_b64_tr_b16 a[224:225], v202 offset:0\n"
    "ds_read_b64_tr_b16 a[226:227], v202 offset:8192\n"
    "v_mfma_f32_16x16x32_bf16 v[136:139], a[232:235], a[136:139], v[136:139]\n"
    "s_add_u32 m0, s54, 100992\n"
    "ds_read_b64_tr_b16 a[228:229], v206 offset:0\n"
    "ds_read_b64_tr_b16 a[230:231], v206 offset:8192\n"
    "v_mfma_f32_16x16x32_bf16 v[136:139], a[240:243], a[144:147], v[136:139]\n"
    "ds_read_b64_tr_b16 a[232:233], v204 offset:0\n"
    "ds_read_b64_tr_b16 a[234:235], v204 offset:8192\n"
    "v_mfma_f32_16x16x32_bf16 v[136:139], a[244:247], a[148:151], v[136:139]\n"
    "ds_read_b64_tr_b16 a[236:237], v208 offset:0\n"
    "ds_read_b64_tr_b16 a[238:239], v208 offset:8192\n"
    "v_mfma_f32_16x16x32_bf16 v[140:143], a[244:247], a[180:183], v[140:143]\n"
    "v_mfma_f32_16x16x32_bf16 v[140:143], a[240:243], a[176:179], v[140:143]\n"
    "ds_read_b64_tr_b16 a[240:241], v202 offset:512\n"
    "ds_read_b64_tr_b16 a[242:243], v202 offset:8704\n"
    "v_mfma_f32_16x16x32_bf16 v[140:143], a[248:251], a[184:187], v[140:143]\n"
    "buffer_load_dwordx4 v211, s[40:43], s53 offen offset:384 lds\n"
    "v_mfma_f32_16x16x32_bf16 v[140:143], a[252:255], a[188:191], v[140:143]\n"
    "s_add_u32 s53, s53, 16384\n"
    "ds_read_b64_tr_b16 a[244:245], v206 offset:512\n"
    "ds_read_b64_tr_b16 a[246:247], v206 offset:8704\n"
    "v_mfma_f32_16x16x32_bf16 v[136:139], a[252:255], a[156:159], v[136:139]\n"
    "v_mfma_f32_16x16x32_bf16 v[136:139], a[248:251], a[152:155], v[136:139]\n"
    "s_cbranch_vccnz LrareA_%=\n"
    "s_branch LbackA_%=\n"
    "Lfinal_%=:\n"
    "s_add_u32 s48, s48, 319488\n"
    "s_addc_u32 s49, s49, 0\n"
    "s_waitcnt lgkmcnt(8)\n"
    "v_mfma_f32_16x16x32_bf16 a[0:3], a[224:227], v[144:147], a[0:3]\n"
    "Lpad7_%=:\n"
    "s_cbranch_execz Lhopd1_%=\n"
    "ds_read_b64_tr_b16 a[248:249], v204 offset:512\n"
    "v_add_f32 v236, v236, v238\n"
    "v_mfma_f32_16x16x32_bf16 a[0:3], a[228:231], v[156:159], a[0:3]\n"
    "ds_read_b64_tr_b16 a[250:251], v204 offset:8704\n"
    "v_add_f32_e64 v237, v237, v239\n"
    "v_mfma_f32_16x16x32_bf16 a[64:67], a[228:231], v[160:163], a[64:67]\n"
    "ds_read_b64_tr_b16 a[252:253], v208 offset:512\n"
    "v_add_f32_e64 v235, v235, v240\n"
    "v_mfma_f32_16x16x32_bf16 a[64:67], a[224:227], v[148:151], a[64:67]\n"
    "s_nop 0\n"
    "ds_read_b64_tr_b16 a[254:255], v208 offset:8704\n"
    "v_fma_f32 v184, v104, s58, v232\n"
    "v_mfma_f32_16x16x32_bf16 v[0:3], a[224:227], v[152:155], v[0:3]\n"
    "s_waitcnt lgkmcnt(8)\n"
    "ds_read_b64_tr_b16 a[224:225], v202 offset:1024\n"
    "v_fma_f32 v185, v105, s58, v232\n"
    "v_mfma_f32_16x16x32_bf16 v[4:7], a[232:235], v[152:155], v[4:7]\n"
    "ds_read_b64_tr_b16 a[226:227], v202 offset:9216\n"
    "v_fma_f32 v186, v106, s58, v232\n"
    "v_mfma_f32_16x16x32_bf16 a[68:71], a[232:235], v[148:151], a[68:71]\n"
    "ds_read_b64_tr_b16 a[228:229], v206 offset:1024\n"
    "v_fma_f32 v187, v107, s58, v232\n"
    "v_mfma_f32_16x16x32_bf16 a[68:71], a[236:239], v[160:163], a[68:71]\n"
    "v_exp_f32_e64 v184, v184\n"
    "v_mfma_f32_16x16x32_bf16 a[4:7], a[236:239], v[156:159], a[4:7]\n"
    "ds_read_b64_tr_b16 a[230:231], v206 offset:9216\n"
    "v_fma_f32 v188, v116, s58, v232\n"
    "v_mfma_f32_16x16x32_bf16 a[4:7], a[232:235], v[144:147], a[4:7]\n"
    "s_waitcnt lgkmcnt(8)\n"
    "v_exp_f32 v185, v185\n"
    "v_mfma_f32_16x16x32_bf16 a[8:11], a[240:243], v[144:147], a[8:11]\n"
    "ds_read_b64_tr_b16 a[232:233], v204 offset:1024\n"
    "v_fma_f32 v189, v117, s58, v232\n"
    "v_mfma_f32_16x16x32_bf16 a[8:11], a[244:247], v[156:159], a[8:11]\n"
    "v_exp_f32_e64 v186, v186\n"
    "v_mfma_f32_16x16x32_bf16 a[72:75], a[244:247], v[160:163], a[72:75]\n"
    "ds_read_b64_tr_b16 a[234:235], v204 offset:9216\n"
    "v_fma_f32 v190, v118, s58, v232\n"
    "v_mfma_f32_16x16x32_bf16 a[72:75], a[240:243], v[148:151], a[72:75]\n"
    "v_exp_f32 v187, v187\n"
    "v_mfma_f32_16x16x32_bf16 v[8:11], a[240:243], v[152:155], v[8:11]\n"
    "s_waitcnt lgkmcnt(6)\n"
    "ds_read_b64_tr_b16 a[236:237], v208 offset:1024\n"
    "ds_read_b64_tr_b16 a[238:239], v208 offset:9216\n"
    "v_mfma_f32_16x16x32_bf16 v[12:15], a[248:251], v[152:155], v[12:15]\n"
    "ds_read_b64_tr_b16 a[240:241], v202 offset:1536\n"
    "v_fma_f32 v191, v119, s58, v232\n"
    "v_mfma_f32_16x16x32_bf16 a[76:79], a[248:251], v[148:151], a[76:79]\n"
    "v_exp_f32_e64 v188, v188\n"
    "v_mfma_f32_16x16x32_bf16 a[76:79], a[252:255], v[160:163], a[76:79]\n"
    "ds_read_b64_tr_b16 a[242:243], v202 offset:9728\n"
    "ds_read_b64_tr_b16 a[244:245], v206 offset:1536\n"
    "v_mfma_f32_16x16x32_bf16 a[12:15], a[252:255], v[156:159], a[12:15]\n"
    "ds_read_b64_tr_b16 a[246:247], v206 offset:9728\n"
    "v_add_f32 v238, v184, v185\n"
    "v_mfma_f32_16x16x32_bf16 a[12:15], a[248:251], v[144:147], a[12:15]\n"
    "s_waitcnt lgkmcnt(8)\n"
    "ds_read_b64_tr_b16 a[248:249], v204 offset:1536\n"
    "v_cvt_pk_bf16_f32 v164, v184, v185\n"
    "v_mfma_f32_16x16x32_bf16 a[16:19], a[224:227], v[144:147], a[16:19]\n"
    "ds_read_b64_tr_b16 a[250:251], v204 offset:9728\n"
    "v_fma_f32 v192, v108, s58, v233\n"
    "v_mfma_f32_16x16x32_bf16 a[16:19], a[228:231], v[156:159], a[16:19]\n"
    "v_exp_f32_e64 v189, v189\n"
    "v_mfma_f32_16x16x32_bf16 a[80:83], a[228:231], v[160:163], a[80:83]\n"
    "ds_read_b64_tr_b16 a[252:253], v208 offset:1536\n"
    "v_add_f32_e64 v238, v238, v186\n"
    "v_mfma_f32_16x16x32_bf16 a[80:83], a[224:227], v[148:151], a[80:83]\n"
    "ds_read_b64_tr_b16 a[254:255], v208 offset:9728\n"
    "v_fma_f32 v193, v109, s58, v233\n"
    "v_mfma_f32_16x16x32_bf16 v[16:19], a[224:227], v[152:155], v[16:19]\n"
    "s_waitcnt lgkmcnt(8)\n"
    "v_exp_f32 v190, v190\n"
    "v_mfma_f32_16x16x32_bf16 v[20:23], a[232:235], v[152:155], v[20:23]\n"
    "ds_read_b64_tr_b16 a[224:225], v203 offset:0\n"
    "v_add_f32_e64 v238, v238, v187\n"
    "v_mfma_f32_16x16x32_bf16 a[84:87], a[232:235], v[148:151], a[84:87]\n"
    "ds_read_b64_tr_b16 a[226:227], v203 offset:8192\n"
    "v_cvt_pk_bf16_f32 v165, v186, v187\n"
    "v_mfma_f32_16x16x32_bf16 a[84:87], a[236:239], v[160:163], a[84:87]\n"
    "ds_read_b64_tr_b16 a[228:229], v207 offset:0\n"
    "ds_read_b64_tr_b16 a[230:231], v207 offset:8192\n"
    "v_mfma_f32_16x16x32_bf16 a[20:23], a[236:239], v[156:159], a[20:23]\n"
    "v_fma_f32 v194, v110, s58, v233\n"
    "v_add_f32_e64 v238, v238, v188\n"
    "v_mfma_f32_16x16x32_bf16 a[20:23], a[232:235], v[144:147], a[20:23]\n"
    "s_waitcnt lgkmcnt(8)\n"
    "v_exp_f32 v191, v191\n"
    "v_mfma_f32_16x16x32_bf16 a[24:27], a[240:243], v[144:147], a[24:27]\n"
    "ds_read_b64_tr_b16 a[232:233], v205 offset:0\n"
    "v_fma_f32 v195, v111, s58, v233\n"
    "v_mfma_f32_16x16x32_bf16 a[24:27], a[244:247], v[156:159], a[24:27]\n"
    "v_exp_f32_e64 v192, v192\n"
    "v_mfma_f32_16x16x32_bf16 a[88:91], a[244:247], v[160:163], a[88:91]\n"
    "ds_read_b64_tr_b16 a[234:235], v205 offset:8192\n"
    "v_add_f32_e64 v238, v238, v189\n"
    "v_mfma_f32_16x16x32_bf16 a[88:91], a[240:243], v[148:151], a[88:91]\n"
    "s_nop 0\n"
    "ds_read_b64_tr_b16 a[236:237], v209 offset:0\n"
    "ds_read_b64_tr_b16 a[238:239], v209 offset:8192\n"
    "v_mfma_f32_16x16x32_bf16 v[24:27], a[240:243], v[152:155], v[24:27]\n"
    "s_waitcnt lgkmcnt(8)\n"
    "ds_read_b64_tr_b16 a[240:241], v203 offset:512\n"
    "v_cvt_pk_bf16_f32 v166, v188, v189\n"
    "v_mfma_f32_16x16x32_bf16 v[28:31], a[248:251], v[152:155], v[28:31]\n"
    "ds_read_b64_tr_b16 a[242:243], v203 offset:8704\n"
    "v_fma_f32 v196, v120, s58, v233\n"
    "v_mfma_f32_16x16x32_bf16 a[92:95], a[248:251], v[148:151], a[92:95]\n"
    "v_exp_f32_e64 v193, v193\n"
    "v_mfma_f32_16x16x32_bf16 a[92:95], a[252:255], v[160:163], a[92:95]\n"
    "ds_read_b64_tr_b16 a[244:245], v207 offset:512\n"
    "ds_read_b64_tr_b16 a[246:247], v207 offset:8704\n"
    "v_mfma_f32_16x16x32_bf16 a[28:31], a[252:255], v[156:159], a[28:31]\n"
    "v_add_f32_e64 v238, v238, v190\n"
    "v_fma_f32 v197, v121, s58, v233\n"
    "v_mfma_f32_16x16x32_bf16 a[28:31], a[248:251], v[144:147], a[28:31]\n"
    "s_waitcnt lgkmcnt(8)\n"
    "v_exp_f32 v194, v194\n"
    "v_mfma_f32_16x16x32_bf16 a[32:35], a[224:227], v[144:147], a[32:35]\n"
    "ds_read_b64_tr_b16 a[248:249], v205 offset:512\n"
    "v_add_f32_e64 v238, v238, v191\n"
    "v_mfma_f32_16x16x32_bf16 a[32:35], a[228:231], v[156:159], a[32:35]\n"
    "ds_read_b64_tr_b16 a[250:251], v205 offset:8704\n"
    "v_cvt_pk_bf16_f32 v167, v190, v191\n"
    "v_mfma_f32_16x16x32_bf16 a[96:99], a[228:231], v[160:163], a[96:99]\n"
    "ds_read_b64_tr_b16 a[252:253], v209 offset:512\n"
    "v_fma_f32 v198, v122, s58, v233\n"
    "v_mfma_f32_16x16x32_bf16 a[96:99], a[224:227], v[148:151], a[96:99]\n"
    "v_exp_f32 v195, v195\n"
    "v_mfma_f32_16x16x32_bf16 v[32:35], a[224:227], v[152:155], v[32:35]\n"
    "s_waitcnt lgkmcnt(7)\n"
    "ds_read_b64_tr_b16 a[254:255], v209 offset:8704\n"
    "v_fma_f32 v199, v123, s58, v233\n"
    "v_mfma_f32_16x16x32_bf16 v[36:39], a[232:235], v[152:155], v[36:39]\n"
    "v_exp_f32_e64 v196, v196\n"
    "v_mfma_f32_16x16x32_bf16 a[100:103], a[232:235], v[148:151], a[100:103]\n"
    "ds_read_b64_tr_b16 a[224:225], v203 offset:1024\n"
    "ds_read_b64_tr_b16 a[226:227], v203 offset:9216\n"
    "v_mfma_f32_16x16x32_bf16 a[100:103], a[236:239], v[160:163], a[100:103]\n"
    "ds_read_b64_tr_b16 a[228:229], v207 offset:1024\n"
    "ds_read_b64_tr_b16 a[230:231], v207 offset:9216\n"
    "v_mfma_f32_16x16x32_bf16 a[36:39], a[236:239], v[156:159], a[36:39]\n"
    "v_add_f32 v239, v192, v193\n"
    "v_cvt_pk_bf16_f32 v168, v192, v193\n"
    "v_mfma_f32_16x16x32_bf16 a[36:39], a[232:235], v[144:147], a[36:39]\n"
    "s_waitcnt lgkmcnt(8)\n"
    "ds_read_b64_tr_b16 a[232:233], v205 offset:1024\n"
    "v_fma_f32 v184, v112, s58, v234\n"
    "v_mfma_f32_16x16x32_bf16 a[40:43], a[240:243], v[144:147], a[40:43]\n"
    "v_exp_f32_e64 v197, v197\n"
    "v_mfma_f32_16x16x32_bf16 a[40:43], a[244:247], v[156:159], a[40:43]\n"
    "ds_read_b64_tr_b16 a[234:235], v205 offset:9216\n"
    "v_add_f32_e64 v239, v239, v194\n"
    "v_mfma_f32_16x16x32_bf16 a[104:107], a[244:247], v[160:163], a[104:107]\n"
    "ds_read_b64_tr_b16 a[236:237], v209 offset:1024\n"
    "v_fma_f32 v185, v113, s58, v234\n"
    "v_mfma_f32_16x16x32_bf16 a[104:107], a[240:243], v[148:151], a[104:107]\n"
    "v_exp_f32 v198, v198\n"
    "v_mfma_f32_16x16x32_bf16 v[40:43], a[240:243], v[152:155], v[40:43]\n"
    "s_waitcnt lgkmcnt(7)\n"
    "ds_read_b64_tr_b16 a[238:239], v209 offset:9216\n"
    "v_add_f32_e64 v239, v239, v195\n"
    "v_mfma_f32_16x16x32_bf16 v[44:47], a[248:251], v[152:155], v[44:47]\n"
    "ds_read_b64_tr_b16 a[240:241], v203 offset:1536\n"
    "v_cvt_pk_bf16_f32 v169, v194, v195\n"
    "v_mfma_f32_16x16x32_bf16 a[108:111], a[248:251], v[148:151], a[108:111]\n"
    "ds_read_b64_tr_b16 a[242:243], v203 offset:9728\n"
    "v_fma_f32 v186, v114, s58, v234\n"
    "v_mfma_f32_16x16x32_bf16 a[108:111], a[252:255], v[160:163], a[108:111]\n"
    "v_exp_f32_e64 v199, v199\n"
    "v_mfma_f32_16x16x32_bf16 a[44:47], a[252:255], v[156:159], a[44:47]\n"
    "s_nop 0\n"
    "ds_read_b64_tr_b16 a[244:245], v207 offset:1536\n"
    "ds_read_b64_tr_b16 a[246:247], v207 offset:9728\n"
    "v_mfma_f32_16x16x32_bf16 a[44:47], a[248:251], v[144:147], a[44:47]\n"
    "s_waitcnt lgkmcnt(8)\n"
    "ds_read_b64_tr_b16 a[248:249], v205 offset:1536\n"
    "ds_read_b64_tr_b16 a[250:251], v205 offset:9728\n"
    "v_mfma_f32_16x16x32_bf16 a[48:51], a[224:227], v[144:147], a[48:51]\n"
    "ds_read_b64_tr_b16 a[252:253], v209 offset:1536\n"
    "ds_read_b64_tr_b16 a[254:255], v209 offset:9728\n"
    "v_mfma_f32_16x16x32_bf16 a[48:51], a[228:231], v[156:159], a[48:51]\n"
    "v_add_f32_e64 v239, v239, v196\n"
    "v_fma_f32 v187, v115, s58, v234\n"
    "v_mfma_f32_16x16x32_bf16 a[112:115], a[228:231], v[160:163], a[112:115]\n"
    "v_exp_f32_e64 v184, v184\n"
    "v_mfma_f32_16x16x32_bf16 a[112:115], a[224:227], v[148:151], a[112:115]\n"
    "v_add_f32_e64 v239, v239, v197\n"
    "v_cvt_pk_bf16_f32 v170, v196, v197\n"
    "v_mfma_f32_16x16x32_bf16 v[48:51], a[224:227], v[152:155], v[48:51]\n"
    "s_waitcnt lgkmcnt(8)\n"
    "v_fma_f32 v188, v124, s58, v234\n"
    "v_add_f32 v239, v239, v198\n"
    "v_mfma_f32_16x16x32_bf16 v[52:55], a[232:235], v[152:155], v[52:55]\n"
    "v_exp_f32 v185, v185\n"
    "v_mfma_f32_16x16x32_bf16 a[116:119], a[232:235], v[148:151], a[116:119]\n"
    "s_waitcnt vmcnt(0) lgkmcnt(0)\n"
    "v_fma_f32 v189, v125, s58, v234\n"
    "v_add_f32_e64 v239, v239, v199\n"
    "v_mfma_f32_16x16x32_bf16 a[116:119], a[236:239], v[160:163], a[116:119]\n"
    "s_barrier\n"
    "v_exp_f32 v186, v186\n"
    "v_mfma_f32_16x16x32_bf16 a[52:55], a[236:239], v[156:159], a[52:55]\n"
    "ds_read_b64_tr_b16 a[224:225], v202 offset:32768\n"
    "ds_read_b64_tr_b16 a[226:227], v202 offset:40960\n"
    "v_mfma_f32_16x16x32_bf16 a[52:55], a[232:235], v[144:147], a[52:55]\n"
    "ds_read_b64_tr_b16 a[228:229], v206 offset:32768\n"
    "ds_read_b64_tr_b16 a[230:231], v206 offset:40960\n"
    "v_mfma_f32_16x16x32_bf16 a[56:59], a[240:243], v[144:147], a[56:59]\n"
    "ds_read_b64_tr_b16 a[232:233], v204 offset:32768\n"
    "v_cvt_pk_bf16_f32 v171, v198, v199\n"
    "v_mfma_f32_16x16x32_bf16 a[56:59], a[244:247], v[156:159], a[56:59]\n"
    "ds_read_b64_tr_b16 a[234:235], v204 offset:40960\n"
    "ds_read_b64_tr_b16 a[236:237], v208 offset:32768\n"
    "v_mfma_f32_16x16x32_bf16 a[120:123], a[244:247], v[160:163], a[120:123]\n"
    "ds_read_b64_tr_b16 a[238:239], v208 offset:40960\n"
    "v_fma_f32 v190, v126, s58, v234\n"
    "v_mfma_f32_16x16x32_bf16 a[120:123], a[240:243], v[148:151], a[120:123]\n"
    "v_exp_f32_e64 v187, v187\n"
    "v_mfma_f32_16x16x32_bf16 v[56:59], a[240:243], v[152:155], v[56:59]\n"
    "ds_read_b64_tr_b16 a[240:241], v202 offset:33280\n"
    "v_fma_f32 v191, v127, s58, v234\n"
    "v_mfma_f32_16x16x32_bf16 v[60:63], a[248:251], v[152:155], v[60:63]\n"
    "v_exp_f32_e64 v188, v188\n"
    "v_mfma_f32_16x16x32_bf16 a[124:127], a[248:251], v[148:151], a[124:127]\n"
    "ds_read_b64_tr_b16 a[242:243], v202 offset:41472\n"
    "ds_read_b64_tr_b16 a[244:245], v206 offset:33280\n"
    "v_mfma_f32_16x16x32_bf16 a[124:127], a[252:255], v[160:163], a[124:127]\n"
    "ds_read_b64_tr_b16 a[246:247], v206 offset:41472\n"
    "v_add_f32_e64 v240, v184, v185\n"
    "v_mfma_f32_16x16x32_bf16 a[60:63], a[252:255], v[156:159], a[60:63]\n"
    "v_cvt_pk_bf16_f32 v172, v184, v185\n"
    "v_fma_f32 v192, v128, s58, v232\n"
    "v_mfma_f32_16x16x32_bf16 a[60:63], a[248:251], v[144:147], a[60:63]\n"
    "v_exp_f32 v189, v189\n"
    "v_add_f32 v240, v240, v186\n"
    "v_fma_f32 v193, v129, s58, v232\n"
    "v_exp_f32 v190, v190\n"
    "v_add_f32 v240, v240, v187\n"
    "v_cvt_pk_bf16_f32 v173, v186, v187\n"
    "v_fma_f32 v194, v130, s58, v232\n"
    "v_add_f32 v240, v240, v188\n"
    "v_exp_f32 v191, v191\n"
    "v_fma_f32 v195, v131, s58, v232\n"
    "v_exp_f32 v192, v192\n"
    "v_add_f32 v240, v240, v189\n"
    "v_cvt_pk_bf16_f32 v174, v188, v189\n"
    "v_fma_f32 v196, v136, s58, v232\n"
    "v_exp_f32 v193, v193\n"
    "v_add_f32 v240, v240, v190\n"
    "v_fma_f32 v197, v137, s58, v232\n"
    "v_exp_f32 v194, v194\n"
    "v_add_f32 v240, v240, v191\n"
    "v_cvt_pk_bf16_f32 v175, v190, v191\n"
    "v_fma_f32 v198, v138, s58, v232\n"
    "v_exp_f32 v195, v195\n"
    "v_add_f32 v238, v238, v192\n"
    "v_fma_f32 v199, v139, s58, v232\n"
    "v_exp_f32 v196, v196\n"
    "v_add_f32 v238, v238, v193\n"
    "v_cvt_pk_bf16_f32 v176, v192, v193\n"
    "v_fma_f32 v184, v132, s58, v233\n"
    "v_exp_f32 v197, v197\n"
    "v_add_f32 v238, v238, v194\n"
    "v_fma_f32 v185, v133, s58, v233\n"
    "v_exp_f32 v198, v198\n"
    "v_add_f32 v238, v238, v195\n"
    "v_cvt_pk_bf16_f32 v177, v194, v195\n"
    "v_fma_f32 v186, v134, s58, v233\n"
    "v_exp_f32 v199, v199\n"
    "v_add_f32 v238, v238, v196\n"
    "v_fma_f32 v187, v135, s58, v233\n"
    "v_add_f32 v238, v238, v197\n"
    "v_exp_f32 v184, v184\n"
    "Lpad8_%=:\n"
    "s_cbranch_execz Lhopd2_%=\n"
    "v_cvt_pk_bf16_f32 v178, v196, v197\n"
    "v_fma_f32 v188, v140, s58, v233\n"
    "v_exp_f32 v185, v185\n"
    "v_add_f32_e64 v238, v238, v198\n"
    "v_fma_f32 v189, v141, s58, v233\n"
    "v_exp_f32 v186, v186\n"
    "v_add_f32 v238, v238, v199\n"
    "v_cvt_pk_bf16_f32 v179, v198, v199\n"
    "v_fma_f32 v190, v142, s58, v233\n"
    "v_exp_f32 v187, v187\n"
    "v_add_f32 v239, v239, v184\n"
    "v_fma_f32 v191, v143, s58, v233\n"
    "v_exp_f32 v188, v188\n"
    "v_add_f32 v239, v239, v185\n"
    "v_cvt_pk_bf16_f32 v180, v184, v185\n"
    "v_exp_f32 v189, v189\n"
    "v_add_f32 v239, v239, v186\n"
    "v_exp_f32 v190, v190\n"
    "v_add_f32 v239, v239, v187\n"
    "v_cvt_pk_bf16_f32 v181, v186, v187\n"
    "v_exp_f32 v191, v191\n"
    "v_add_f32 v239, v239, v188\n"
    "v_add_f32_e64 v239, v239, v189\n"
    "v_cvt_pk_bf16_f32 v182, v188, v189\n"
    "v_add_f32 v239, v239, v190\n"
    "v_add_f32 v239, v239, v191\n"
    "v_cvt_pk_bf16_f32 v183, v190, v191\n"
    "v_max3_f32 v243, v238, v239, v240\n"
    "v_cmp_lt_f32 vcc, s59, v243\n"
    "s_cbranch_vccnz LrareP14f_%=\n"
    "LbackP14f_%=:\n"
    "s_waitcnt lgkmcnt(8)\n"
    "v_mfma_f32_16x16x32_bf16 a[0:3], a[224:227], v[164:167], a[0:3]\n"
    "s_nop 0\n"
    "ds_read_b64_tr_b16 a[248:249], v204 offset:33280\n"
    "v_add_f32 v236, v236, v238\n"
    "v_add_f32 v237, v237, v239\n"
    "v_add_f32 v235, v235, v240\n"
    "v_mov_b32 v247, v235\n"
    "v_mov_b32_e64 v243, v236\n"
    "v_mov_b32 v245, v237\n"
    "v_permlane32_swap_b32 v235, v247\n"
    "v_mfma_f32_16x16x32_bf16 a[0:3], a[228:231], v[176:179], a[0:3]\n"
    "ds_read_b64_tr_b16 a[250:251], v204 offset:41472\n"
    "v_permlane32_swap_b32 v236, v243\n"
    "v_permlane32_swap_b32 v237, v245\n"
    "v_add_f32 v235, v235, v247\n"
    "v_add_f32 v236, v236, v243\n"
    "v_add_f32 v237, v237, v245\n"
    "v_mov_b32 v247, v235\n"
    "v_mfma_f32_16x16x32_bf16 a[64:67], a[228:231], v[180:183], a[64:67]\n"
    "ds_read_b64_tr_b16 a[252:253], v208 offset:33280\n"
    "v_mov_b32_e64 v243, v236\n"
    "v_mov_b32 v245, v237\n"
    "v_permlane16_swap_b32 v235, v247\n"
    "v_permlane16_swap_b32 v236, v243\n"
    "v_permlane16_swap_b32 v237, v245\n"
    "v_mfma_f32_16x16x32_bf16 a[64:67], a[224:227], v[168:171], a[64:67]\n"
    "ds_read_b64_tr_b16 a[254:255], v208 offset:41472\n"
    "v_add_f32 v235, v235, v247\n"
    "v_add_f32 v236, v236, v243\n"
    "v_add_f32 v237, v237, v245\n"
    "v_rcp_f32 v242, v236\n"
    "v_rcp_f32 v244, v237\n"
    "v_accvgpr_read_b32 v64, a0\n"
    "v_mfma_f32_16x16x32_bf16 v[0:3], a[224:227], v[172:175], v[0:3]\n"
    "s_waitcnt lgkmcnt(8)\n"
    "ds_read_b64_tr_b16 a[224:225], v202 offset:33792\n"
    "ds_write_b64 v219, v[234:235] offset:6144\n"
    "v_accvgpr_read_b32 v68, a64\n"
    "v_accvgpr_read_b32 v65, a1\n"
    "v_pk_mul_f32 v[64:65], v[64:65], v[242:243] op_sel_hi:[1,0]\n"
    "v_accvgpr_read_b32 v69, a65\n"
    "v_pk_mul_f32 v[68:69], v[68:69], v[244:245] op_sel_hi:[1,0]\n"
    "v_mfma_f32_16x16x32_bf16 v[4:7], a[232:235], v[172:175], v[4:7]\n"
    "ds_read_b64_tr_b16 a[226:227], v202 offset:41984\n"
    "ds_write_b128 v217, v[0:3] offset:0\n"
    "v_accvgpr_read_b32 v66, a2\n"
    "v_accvgpr_read_b32 v70, a66\n"
    "v_accvgpr_read_b32 v67, a3\n"
    "v_mfma_f32_16x16x32_bf16 a[68:71], a[232:235], v[168:171], a[68:71]\n"
    "ds_read_b64_tr_b16 a[228:229], v206 offset:33792\n"
    "v_accvgpr_read_b32 v71, a67\n"
    "v_cvt_pk_bf16_f32 v96, v64, v65\n"
    "v_cvt_pk_bf16_f32 v98, v68, v69\n"
    "v_mfma_f32_16x16x32_bf16 a[68:71], a[236:239], v[180:183], a[68:71]\n"
    "ds_read_b64_tr_b16 a[230:231], v206 offset:41984\n"
    "ds_write_b128 v217, v[4:7] offset:1024\n"
    "v_pk_mul_f32 v[66:67], v[66:67], v[242:243] op_sel_hi:[1,0]\n"
    "v_pk_mul_f32 v[70:71], v[70:71], v[244:245] op_sel_hi:[1,0]\n"
    "v_cvt_pk_bf16_f32 v97, v66, v67\n"
    "v_mfma_f32_16x16x32_bf16 a[4:7], a[236:239], v[176:179], a[4:7]\n"
    "s_nop 0\n"
    "v_cvt_pk_bf16_f32 v99, v70, v71\n"
    "v_mfma_f32_16x16x32_bf16 a[4:7], a[232:235], v[164:167], a[4:7]\n"
    "s_waitcnt lgkmcnt(11)\n"
    "ds_read_b64_tr_b16 a[232:233], v204 offset:33792\n"
    "ds_write_b64 v221, v[96:97] offset:0\n"
    "v_accvgpr_read_b32 v76, a68\n"
    "v_accvgpr_read_b32 v77, a69\n"
    "v_pk_mul_f32 v[76:77], v[76:77], v[244:245] op_sel_hi:[1,0]\n"
    "v_accvgpr_read_b32 v78, a70\n"
    "v_accvgpr_read_b32 v79, a71\n"
    "v_mfma_f32_16x16x32_bf16 a[8:11], a[240:243], v[164:167], a[8:11]\n"
    "ds_read_b64_tr_b16 a[234:235], v204 offset:41984\n"
    "ds_write_b64 v221, v[98:99] offset:2048\n"
    "v_accvgpr_read_b32 v72, a4\n"
    "v_pk_mul_f32 v[78:79], v[78:79], v[244:245] op_sel_hi:[1,0]\n"
    "v_accvgpr_read_b32 v73, a5\n"
    "v_pk_mul_f32 v[72:73], v[72:73], v[242:243] op_sel_hi:[1,0]\n"
    "v_accvgpr_read_b32 v74, a6\n"
    "v_mfma_f32_16x16x32_bf16 a[8:11], a[244:247], v[176:179], a[8:11]\n"
    "ds_read_b64_tr_b16 a[236:237], v208 offset:33792\n"
    "v_accvgpr_read_b32 v75, a7\n"
    "v_cvt_pk_bf16_f32 v100, v72, v73\n"
    "v_cvt_pk_bf16_f32 v102, v76, v77\n"
    "v_cvt_pk_bf16_f32 v103, v78, v79\n"
    "v_pk_mul_f32 v[74:75], v[74:75], v[242:243] op_sel_hi:[1,0]\n"
    "v_cvt_pk_bf16_f32 v101, v74, v75\n"
    "v_mfma_f32_16x16x32_bf16 a[72:75], a[244:247], v[180:183], a[72:75]\n"
    "ds_read_b64_tr_b16 a[238:239], v208 offset:41984\n"
    "ds_write_b64 v222, v[100:101] offset:0\n"
    "v_accvgpr_read_b32 v80, a8\n"
    "v_accvgpr_read_b32 v81, a9\n"
    "v_pk_mul_f32 v[80:81], v[80:81], v[242:243] op_sel_hi:[1,0]\n"
    "v_accvgpr_read_b32 v82, a10\n"
    "v_accvgpr_read_b32 v83, a11\n"
    "v_mfma_f32_16x16x32_bf16 a[72:75], a[240:243], v[168:171], a[72:75]\n"
    "s_nop 0\n"
    "ds_write_b64 v222, v[102:103] offset:2048\n"
    "v_cvt_pk_bf16_f32 v104, v80, v81\n"
    "v_mfma_f32_16x16x32_bf16 v[8:11], a[240:243], v[172:175], v[8:11]\n"
    "s_waitcnt lgkmcnt(15)\n"
    "ds_read_b64_tr_b16 a[240:241], v202 offset:34304\n"
    "ds_read_b64_tr_b16 a[242:243], v202 offset:42496\n"
    "ds_read_b64_tr_b16 a[244:245], v206 offset:34304\n"
    "ds_read_b64_tr_b16 a[246:247], v206 offset:42496\n"
    "v_accvgpr_read_b32 v84, a72\n"
    "v_pk_mul_f32 v[82:83], v[82:83], v[242:243] op_sel_hi:[1,0]\n"
    "v_accvgpr_read_b32 v85, a73\n"
    "v_pk_mul_f32 v[84:85], v[84:85], v[244:245] op_sel_hi:[1,0]\n"
    "v_accvgpr_read_b32 v86, a74\n"
    "v_mfma_f32_16x16x32_bf16 v[12:15], a[248:251], v[172:175], v[12:15]\n"
    "ds_write_b128 v217, v[8:11] offset:2048\n"
    "v_accvgpr_read_b32 v87, a75\n"
    "v_pk_mul_f32 v[86:87], v[86:87], v[244:245] op_sel_hi:[1,0]\n"
    "v_cvt_pk_bf16_f32 v106, v84, v85\n"
    "v_cvt_pk_bf16_f32 v105, v82, v83\n"
    "v_mfma_f32_16x16x32_bf16 a[76:79], a[248:251], v[168:171], a[76:79]\n"
    "v_cvt_pk_bf16_f32 v107, v86, v87\n"
    "v_mfma_f32_16x16x32_bf16 a[76:79], a[252:255], v[180:183], a[76:79]\n"
    "ds_write_b64 v223, v[104:105] offset:0\n"
    "v_mfma_f32_16x16x32_bf16 a[12:15], a[252:255], v[176:179], a[12:15]\n"
    "s_nop 0\n"
    "v_mfma_f32_16x16x32_bf16 a[12:15], a[248:251], v[164:167], a[12:15]\n"
    "s_waitcnt lgkmcnt(15)\n"
    "ds_read_b64_tr_b16 a[248:249], v204 offset:34304\n"
    "ds_read_b64_tr_b16 a[250:251], v204 offset:42496\n"
    "ds_read_b64_tr_b16 a[252:253], v208 offset:34304\n"
    "ds_read_b64_tr_b16 a[254:255], v208 offset:42496\n"
    "v_accvgpr_read_b32 v92, a76\n"
    "v_accvgpr_read_b32 v93, a77\n"
    "v_pk_mul_f32 v[92:93], v[92:93], v[244:245] op_sel_hi:[1,0]\n"
    "v_accvgpr_read_b32 v88, a12\n"
    "v_accvgpr_read_b32 v89, a13\n"
    "v_mfma_f32_16x16x32_bf16 a[16:19], a[224:227], v[164:167], a[16:19]\n"
    "ds_write_b64 v223, v[106:107] offset:2048\n"
    "v_accvgpr_read_b32 v90, a14\n"
    "v_accvgpr_read_b32 v94, a78\n"
    "v_pk_mul_f32 v[88:89], v[88:89], v[242:243] op_sel_hi:[1,0]\n"
    "v_accvgpr_read_b32 v91, a15\n"
    "v_pk_mul_f32 v[90:91], v[90:91], v[242:243] op_sel_hi:[1,0]\n"
    "v_accvgpr_read_b32 v95, a79\n"
    "v_mfma_f32_16x16x32_bf16 a[16:19], a[228:231], v[176:179], a[16:19]\n"
    "ds_write_b128 v217, v[12:15] offset:3072\n"
    "v_pk_mul_f32 v[94:95], v[94:95], v[244:245] op_sel_hi:[1,0]\n"
    "v_cvt_pk_bf16_f32 v108, v88, v89\n"
    "v_cvt_pk_bf16_f32 v110, v92, v93\n"
    "v_cvt_pk_bf16_f32 v109, v90, v91\n"
    "v_mfma_f32_16x16x32_bf16 a[80:83], a[228:231], v[180:183], a[80:83]\n"
    "v_cvt_pk_bf16_f32 v111, v94, v95\n"
    "v_mfma_f32_16x16x32_bf16 a[80:83], a[224:227], v[168:171], a[80:83]\n"
    "s_nop 0\n"
    "ds_write_b64 v224, v[108:109] offset:0\n"
    "ds_read_b128 v[112:115], v225 offset:0\n"
    "ds_read_b128 v[116:119], v225 offset:1024\n"
    "v_accvgpr_read_b32 v64, a16\n"
    "v_accvgpr_read_b32 v65, a17\n"
    "v_pk_mul_f32 v[64:65], v[64:65], v[242:243] op_sel_hi:[1,0]\n"
    "v_accvgpr_read_b32 v66, a18\n"
    "v_mfma_f32_16x16x32_bf16 v[16:19], a[224:227], v[172:175], v[16:19]\n"
    "s_waitcnt lgkmcnt(15)\n"
    "ds_read_b64_tr_b16 a[224:225], v203 offset:32768\n"
    "v_accvgpr_read_b32 v68, a80\n"
    "v_accvgpr_read_b32 v69, a81\n"
    "v_accvgpr_read_b32 v70, a82\n"
    "v_accvgpr_read_b32 v67, a19\n"
    "v_pk_mul_f32 v[68:69], v[68:69], v[244:245] op_sel_hi:[1,0]\n"
    "v_pk_mul_f32 v[66:67], v[66:67], v[242:243] op_sel_hi:[1,0]\n"
    "v_accvgpr_read_b32 v71, a83\n"
    "v_pk_mul_f32 v[70:71], v[70:71], v[244:245] op_sel_hi:[1,0]\n"
    "v_mfma_f32_16x16x32_bf16 v[20:23], a[232:235], v[172:175], v[20:23]\n"
    "ds_read_b64_tr_b16 a[226:227], v203 offset:40960\n"
    "ds_write_b64 v224, v[110:111] offset:2048\n"
    "ds_read_b128 v[120:123], v225 offset:2048\n"
    "ds_read_b128 v[124:127], v225 offset:3072\n"
    "v_cvt_pk_bf16_f32 v96, v64, v65\n"
    "v_cvt_pk_bf16_f32 v98, v68, v69\n"
    "v_cvt_pk_bf16_f32 v97, v66, v67\n"
    "v_mfma_f32_16x16x32_bf16 a[84:87], a[232:235], v[168:171], a[84:87]\n"
    "s_waitcnt lgkmcnt(5)\n"
    "ds_read_b64_tr_b16 a[228:229], v207 offset:32768\n"
    "buffer_store_dwordx4 v[112:115], v215, s[48:51], s55 offen offset:0\n"
    "v_cvt_pk_bf16_f32 v99, v70, v71\n"
    "v_mfma_f32_16x16x32_bf16 a[84:87], a[236:239], v[180:183], a[84:87]\n"
    "s_nop 0\n"
    "ds_read_b64_tr_b16 a[230:231], v207 offset:40960\n"
    "v_mfma_f32_16x16x32_bf16 a[20:23], a[236:239], v[176:179], a[20:23]\n"
    "ds_write_b128 v217, v[16:19] offset:16384\n"
    "v_mfma_f32_16x16x32_bf16 a[20:23], a[232:235], v[164:167], a[20:23]\n"
    "s_waitcnt lgkmcnt(15)\n"
    "ds_read_b64_tr_b16 a[232:233], v205 offset:32768\n"
    "ds_read_b64_tr_b16 a[234:235], v205 offset:40960\n"
    "ds_read_b64_tr_b16 a[236:237], v209 offset:32768\n"
    "ds_read_b64_tr_b16 a[238:239], v209 offset:40960\n"
    "buffer_store_dwordx4 v[116:119], v215, s[48:51], s69 offen offset:0\n"
    "v_accvgpr_read_b32 v76, a84\n"
    "v_accvgpr_read_b32 v77, a85\n"
    "v_pk_mul_f32 v[76:77], v[76:77], v[244:245] op_sel_hi:[1,0]\n"
    "v_mfma_f32_16x16x32_bf16 a[24:27], a[240:243], v[164:167], a[24:27]\n"
    "s_waitcnt lgkmcnt(7)\n"
    "v_accvgpr_read_b32 v72, a20\n"
    "v_accvgpr_read_b32 v73, a21\n"
    "v_accvgpr_read_b32 v74, a22\n"
    "v_accvgpr_read_b32 v78, a86\n"
    "v_accvgpr_read_b32 v75, a23\n"
    "v_pk_mul_f32 v[72:73], v[72:73], v[242:243] op_sel_hi:[1,0]\n"
    "v_pk_mul_f32 v[74:75], v[74:75], v[242:243] op_sel_hi:[1,0]\n"
    "v_accvgpr_read_b32 v79, a87\n"
    "v_pk_mul_f32 v[78:79], v[78:79], v[244:245] op_sel_hi:[1,0]\n"
    "v_mfma_f32_16x16x32_bf16 a[24:27], a[244:247], v[176:179], a[24:27]\n"
    "buffer_store_dwordx4 v[120:123], v215, s[48:51], s56 offen offset:0\n"
    "ds_write_b64 v221, v[96:97] offset:0\n"
    "v_cvt_pk_bf16_f32 v100, v72, v73\n"
    "v_cvt_pk_bf16_f32 v102, v76, v77\n"
    "v_cvt_pk_bf16_f32 v101, v74, v75\n"
    "v_cvt_pk_bf16_f32 v103, v78, v79\n"
    "v_mfma_f32_16x16x32_bf16 a[88:91], a[244:247], v[180:183], a[88:91]\n"
    "ds_write_b64 v221, v[98:99] offset:2048\n"
    "v_accvgpr_read_b32 v80, a24\n"
    "v_accvgpr_read_b32 v81, a25\n"
    "v_pk_mul_f32 v[80:81], v[80:81], v[242:243] op_sel_hi:[1,0]\n"
    "v_accvgpr_read_b32 v82, a26\n"
    "v_accvgpr_read_b32 v83, a27\n"
    "v_pk_mul_f32 v[82:83], v[82:83], v[242:243] op_sel_hi:[1,0]\n"
    "v_mfma_f32_16x16x32_bf16 a[88:91], a[240:243], v[168:171], a[88:91]\n"
    "s_nop 0\n"
    "buffer_store_dwordx4 v[124:127], v215, s[48:51], s70 offen offset:0\n"
    "ds_write_b128 v217, v[20:23] offset:17408\n"
    "v_cvt_pk_bf16_f32 v104, v80, v81\n"
    "v_cvt_pk_bf16_f32 v105, v82, v83\n"
    "v_mfma_f32_16x16x32_bf16 v[24:27], a[240:243], v[172:175], v[24:27]\n"
    "s_waitcnt lgkmcnt(15)\n"
    "ds_read_b64_tr_b16 a[240:241], v203 offset:33280\n"
    "ds_read_b64_tr_b16 a[242:243], v203 offset:41472\n"
    "ds_read_b64_tr_b16 a[244:245], v207 offset:33280\n"
    "ds_read_b64_tr_b16 a[246:247], v207 offset:41472\n"
    "v_accvgpr_read_b32 v84, a88\n"
    "v_accvgpr_read_b32 v85, a89\n"
    "v_pk_mul_f32 v[84:85], v[84:85], v[244:245] op_sel_hi:[1,0]\n"
    "v_accvgpr_read_b32 v86, a90\n"
    "v_accvgpr_read_b32 v87, a91\n"
    "v_mfma_f32_16x16x32_bf16 v[28:31], a[248:251], v[172:175], v[28:31]\n"
    "ds_write_b64 v222, v[100:101] offset:0\n"
    "v_cvt_pk_bf16_f32 v106, v84, v85\n"
    "v_mfma_f32_16x16x32_bf16 a[92:95], a[248:251], v[168:171], a[92:95]\n"
    "v_mfma_f32_16x16x32_bf16 a[92:95], a[252:255], v[180:183], a[92:95]\n"
    "ds_write_b64 v222, v[102:103] offset:2048\n"
    "v_mfma_f32_16x16x32_bf16 a[28:31], a[252:255], v[176:179], a[28:31]\n"
    "s_nop 0\n"
    "v_mfma_f32_16x16x32_bf16 a[28:31], a[248:251], v[164:167], a[28:31]\n"
    "s_waitcnt lgkmcnt(14)\n"
    "ds_read_b64_tr_b16 a[248:249], v205 offset:33280\n"
    "ds_read_b64_tr_b16 a[250:251], v205 offset:41472\n"
    "ds_read_b64_tr_b16 a[252:253], v209 offset:33280\n"
    "ds_read_b64_tr_b16 a[254:255], v209 offset:41472\n"
    "v_accvgpr_read_b32 v92, a92\n"
    "v_pk_mul_f32 v[86:87], v[86:87], v[244:245] op_sel_hi:[1,0]\n"
    "v_cvt_pk_bf16_f32 v107, v86, v87\n"
    "v_accvgpr_read_b32 v88, a28\n"
    "v_accvgpr_read_b32 v89, a29\n"
    "v_pk_mul_f32 v[88:89], v[88:89], v[242:243] op_sel_hi:[1,0]\n"
    "v_mfma_f32_16x16x32_bf16 a[32:35], a[224:227], v[164:167], a[32:35]\n"
    "ds_write_b128 v217, v[24:27] offset:18432\n"
    "v_accvgpr_read_b32 v93, a93\n"
    "v_pk_mul_f32 v[92:93], v[92:93], v[244:245] op_sel_hi:[1,0]\n"
    "v_accvgpr_read_b32 v90, a30\n"
    "v_accvgpr_read_b32 v94, a94\n"
    "v_mfma_f32_16x16x32_bf16 a[32:35], a[228:231], v[176:179], a[32:35]\n"
    "v_accvgpr_read_b32 v91, a31\n"
    "v_accvgpr_read_b32 v95, a95\n"
    "v_cvt_pk_bf16_f32 v108, v88, v89\n"
    "v_cvt_pk_bf16_f32 v110, v92, v93\n"
    "v_mfma_f32_16x16x32_bf16 a[96:99], a[228:231], v[180:183], a[96:99]\n"
    "ds_write_b64 v223, v[104:105] offset:0\n"
    "v_mfma_f32_16x16x32_bf16 a[96:99], a[224:227], v[168:171], a[96:99]\n"
    "s_nop 0\n"
    "v_mfma_f32_16x16x32_bf16 v[32:35], a[224:227], v[172:175], v[32:35]\n"
    "s_waitcnt lgkmcnt(15)\n"
    "ds_read_b64_tr_b16 a[224:225], v203 offset:33792\n"
    "ds_read_b64_tr_b16 a[226:227], v203 offset:41984\n"
    "ds_read_b64_tr_b16 a[228:229], v207 offset:33792\n"
    "ds_read_b64_tr_b16 a[230:231], v207 offset:41984\n"
    "v_accvgpr_read_b32 v64, a32\n"
    "v_pk_mul_f32 v[90:91], v[90:91], v[242:243] op_sel_hi:[1,0]\n"
    "v_pk_mul_f32 v[94:95], v[94:95], v[244:245] op_sel_hi:[1,0]\n"
    "v_cvt_pk_bf16_f32 v109, v90, v91\n"
    "v_cvt_pk_bf16_f32 v111, v94, v95\n"
    "v_mfma_f32_16x16x32_bf16 v[36:39], a[232:235], v[172:175], v[36:39]\n"
    "ds_write_b64 v223, v[106:107] offset:2048\n"
    "v_accvgpr_read_b32 v68, a96\n"
    "v_accvgpr_read_b32 v65, a33\n"
    "v_pk_mul_f32 v[64:65], v[64:65], v[242:243] op_sel_hi:[1,0]\n"
    "v_accvgpr_read_b32 v69, a97\n"
    "v_pk_mul_f32 v[68:69], v[68:69], v[244:245] op_sel_hi:[1,0]\n"
    "v_accvgpr_read_b32 v66, a34\n"
    "v_mfma_f32_16x16x32_bf16 a[100:103], a[232:235], v[168:171], a[100:103]\n"
    "ds_write_b128 v217, v[28:31] offset:19456\n"
    "v_accvgpr_read_b32 v70, a98\n"
    "v_accvgpr_read_b32 v67, a35\n"
    "v_pk_mul_f32 v[66:67], v[66:67], v[242:243] op_sel_hi:[1,0]\n"
    "v_accvgpr_read_b32 v71, a99\n"
    "v_mfma_f32_16x16x32_bf16 a[100:103], a[236:239], v[180:183], a[100:103]\n"
    "v_cvt_pk_bf16_f32 v96, v64, v65\n"
    "v_cvt_pk_bf16_f32 v98, v68, v69\n"
    "v_cvt_pk_bf16_f32 v97, v66, v67\n"
    "v_mfma_f32_16x16x32_bf16 a[36:39], a[236:239], v[176:179], a[36:39]\n"
    "ds_write_b64 v224, v[108:109] offset:0\n"
    "ds_read_b128 v[128:131], v225 offset:0\n"
    "ds_read_b128 v[132:135], v225 offset:1024\n"
    "v_pk_mul_f32 v[70:71], v[70:71], v[244:245] op_sel_hi:[1,0]\n"
    "v_cvt_pk_bf16_f32 v99, v70, v71\n"
    "v_accvgpr_read_b32 v76, a100\n"
    "v_accvgpr_read_b32 v77, a101\n"
    "v_mfma_f32_16x16x32_bf16 a[36:39], a[232:235], v[164:167], a[36:39]\n"
    "s_waitcnt lgkmcnt(15)\n"
    "s_waitcnt lgkmcnt(3)\n"
    "ds_read_b64_tr_b16 a[232:233], v205 offset:33792\n"
    "ds_read_b64_tr_b16 a[234:235], v205 offset:41984\n"
    "ds_read_b64_tr_b16 a[236:237], v209 offset:33792\n"
    "ds_read_b64_tr_b16 a[238:239], v209 offset:41984\n"
    "v_pk_mul_f32 v[76:77], v[76:77], v[244:245] op_sel_hi:[1,0]\n"
    "v_accvgpr_read_b32 v78, a102\n"
    "v_accvgpr_read_b32 v72, a36\n"
    "v_accvgpr_read_b32 v73, a37\n"
    "v_mfma_f32_16x16x32_bf16 a[40:43], a[240:243], v[164:167], a[40:43]\n"
    "s_barrier\n"
    "ds_read_b64 v[250:251], v220 offset:6144\n"
    "ds_read_b128 v[0:3], v218 offset:0\n"
    "ds_read_b128 v[4:7], v218 offset:1024\n"
    "ds_read_b128 v[8:11], v218 offset:2048\n"
    "ds_read_b128 v[12:15], v218 offset:3072\n"
    "ds_read_b128 v[16:19], v218 offset:16384\n"
    "v_pk_mul_f32 v[72:73], v[72:73], v[242:243] op_sel_hi:[1,0]\n"
    "v_accvgpr_read_b32 v74, a38\n"
    "v_accvgpr_read_b32 v75, a39\n"
    "v_mfma_f32_16x16x32_bf16 a[40:43], a[244:247], v[176:179], a[40:43]\n"
    "s_waitcnt lgkmcnt(10)\n"
    "ds_read_b128 v[20:23], v218 offset:17408\n"
    "buffer_store_dwordx4 v[128:131], v215, s[48:51], s55 offen offset:128\n"
    "v_accvgpr_read_b32 v79, a103\n"
    "v_cvt_pk_bf16_f32 v102, v72, v73\n"
    "v_pk_mul_f32 v[74:75], v[74:75], v[242:243] op_sel_hi:[1,0]\n"
    "v_pk_mul_f32 v[78:79], v[78:79], v[244:245] op_sel_hi:[1,0]\n"
    "v_cvt_pk_bf16_f32 v104, v76, v77\n"
    "v_cvt_pk_bf16_f32 v103, v74, v75\n"
    "v_mfma_f32_16x16x32_bf16 a[104:107], a[244:247], v[180:183], a[104:107]\n"
    "ds_read_b128 v[24:27], v218 offset:18432\n"
    "ds_read_b128 v[28:31], v218 offset:19456\n"
    "v_cvt_pk_bf16_f32 v105, v78, v79\n"
    "v_accvgpr_read_b32 v80, a40\n"
    "v_accvgpr_read_b32 v81, a41\n"
    "v_pk_mul_f32 v[80:81], v[80:81], v[242:243] op_sel_hi:[1,0]\n"
    "v_accvgpr_read_b32 v82, a42\n"
    "v_accvgpr_read_b32 v83, a43\n"
    "v_pk_mul_f32 v[82:83], v[82:83], v[242:243] op_sel_hi:[1,0]\n"
    "v_mfma_f32_16x16x32_bf16 a[104:107], a[240:243], v[168:171], a[104:107]\n"
    "s_waitcnt lgkmcnt(8)\n"
    "ds_write_b64 v224, v[110:111] offset:2048\n"
    "ds_read_b128 v[136:139], v225 offset:2048\n"
    "ds_read_b128 v[140:143], v225 offset:3072\n"
    "buffer_store_dwordx4 v[132:135], v215, s[48:51], s69 offen offset:128\n"
    "v_min_f32 v243, v234, v250\n"
    "v_sub_f32 v245, v243, v234\n"
    "v_sub_f32 v247, v243, v250\n"
    "v_mfma_f32_16x16x32_bf16 v[40:43], a[240:243], v[172:175], v[40:43]\n"
    "s_waitcnt lgkmcnt(15)\n"
    "ds_read_b64_tr_b16 a[240:241], v203 offset:34304\n"
    "v_exp_f32 v245, v245\n"
    "v_exp_f32 v247, v247\n"
    "v_mul_f32 v249, v235, v245\n"
    "v_fma_f32 v249, v251, v247, v249\n"
    "v_rcp_f32 v249, v249\n"
    "v_mfma_f32_16x16x32_bf16 v[44:47], a[248:251], v[172:175], v[44:47]\n"
    "s_waitcnt lgkmcnt(4)\n"
    "ds_read_b64_tr_b16 a[242:243], v203 offset:42496\n"
    "ds_write_b64 v221, v[96:97] offset:0\n"
    "v_mul_f32_e64 v246, v245, v249\n"
    "v_mul_f32 v248, v247, v249\n"
    "v_pk_mul_f32 v[0:1], v[0:1], v[248:249] op_sel_hi:[1,0]\n"
    "v_pk_mul_f32 v[2:3], v[2:3], v[248:249] op_sel_hi:[1,0]\n"
    "v_pk_fma_f32 v[32:33], v[32:33], v[246:247], v[0:1] op_sel_hi:[1,0,1]\n"
    "v_mfma_f32_16x16x32_bf16 a[108:111], a[248:251], v[168:171], a[108:111]\n"
    "s_waitcnt lgkmcnt(3)\n"
    "ds_read_b64_tr_b16 a[244:245], v207 offset:34304\n"
    "buffer_store_dwordx4 v[136:139], v215, s[48:51], s56 offen offset:128\n"
    "ds_write_b64 v221, v[98:99] offset:2048\n"
    "v_pk_fma_f32 v[34:35], v[34:35], v[246:247], v[2:3] op_sel_hi:[1,0,1]\n"
    "v_pk_mul_f32 v[4:5], v[4:5], v[248:249] op_sel_hi:[1,0]\n"
    "v_pk_mul_f32 v[6:7], v[6:7], v[248:249] op_sel_hi:[1,0]\n"
    "v_mfma_f32_16x16x32_bf16 a[108:111], a[252:255], v[180:183], a[108:111]\n"
    "ds_read_b64_tr_b16 a[246:247], v207 offset:42496\n"
    "ds_write_b64 v222, v[102:103] offset:0\n"
    "v_cvt_pk_bf16_f32 v100, v32, v33\n"
    "v_pk_fma_f32 v[36:37], v[36:37], v[246:247], v[4:5] op_sel_hi:[1,0,1]\n"
    "v_pk_fma_f32 v[38:39], v[38:39], v[246:247], v[6:7] op_sel_hi:[1,0,1]\n"
    "v_pk_mul_f32 v[8:9], v[8:9], v[248:249] op_sel_hi:[1,0]\n"
    "v_pk_mul_f32 v[10:11], v[10:11], v[248:249] op_sel_hi:[1,0]\n"
    "v_mfma_f32_16x16x32_bf16 a[44:47], a[252:255], v[176:179], a[44:47]\n"
    "s_nop 0\n"
    "buffer_store_dwordx4 v[140:143], v215, s[48:51], s70 offen offset:128\n"
    "ds_write_b64 v222, v[104:105] offset:2048\n"
    "v_pk_fma_f32 v[40:41], v[40:41], v[246:247], v[8:9] op_sel_hi:[1,0,1]\n"
    "v_pk_fma_f32 v[42:43], v[42:43], v[246:247], v[10:11] op_sel_hi:[1,0,1]\n"
    "v_pk_mul_f32 v[12:13], v[12:13], v[248:249] op_sel_hi:[1,0]\n"
    "v_pk_mul_f32 v[14:15], v[14:15], v[248:249] op_sel_hi:[1,0]\n"
    "v_mfma_f32_16x16x32_bf16 a[44:47], a[248:251], v[164:167], a[44:47]\n"
    "s_waitcnt lgkmcnt(15)\n"
    "ds_read_b64_tr_b16 a[248:249], v205 offset:34304\n"
    "ds_read_b64_tr_b16 a[250:251], v205 offset:42496\n"
    "ds_read_b64_tr_b16 a[252:253], v209 offset:34304\n"
    "ds_read_b64_tr_b16 a[254:255], v209 offset:42496\n"
    "v_cvt_pk_bf16_f32 v101, v34, v35\n"
    "v_pk_fma_f32 v[44:45], v[44:45], v[246:247], v[12:13] op_sel_hi:[1,0,1]\n"
    "v_pk_fma_f32 v[46:47], v[46:47], v[246:247], v[14:15] op_sel_hi:[1,0,1]\n"
    "v_pk_mul_f32 v[16:17], v[16:17], v[248:249] op_sel_hi:[1,0]\n"
    "v_pk_mul_f32 v[18:19], v[18:19], v[248:249] op_sel_hi:[1,0]\n"
    "v_mfma_f32_16x16x32_bf16 a[48:51], a[224:227], v[164:167], a[48:51]\n"
    "ds_write_b64 v221, v[100:101] offset:4096\n"
    "v_cvt_pk_bf16_f32 v106, v36, v37\n"
    "v_cvt_pk_bf16_f32 v107, v38, v39\n"
    "v_pk_mul_f32 v[20:21], v[20:21], v[248:249] op_sel_hi:[1,0]\n"
    "v_pk_mul_f32 v[22:23], v[22:23], v[248:249] op_sel_hi:[1,0]\n"
    "v_accvgpr_read_b32 v84, a104\n"
    "v_accvgpr_read_b32 v85, a105\n"
    "v_mfma_f32_16x16x32_bf16 a[48:51], a[228:231], v[176:179], a[48:51]\n"
    "ds_write_b64 v222, v[106:107] offset:4096\n"
    "v_accvgpr_read_b32 v86, a106\n"
    "v_accvgpr_read_b32 v87, a107\n"
    "v_pk_mul_f32 v[84:85], v[84:85], v[244:245] op_sel_hi:[1,0]\n"
    "v_pk_mul_f32 v[86:87], v[86:87], v[244:245] op_sel_hi:[1,0]\n"
    "v_cvt_pk_bf16_f32 v96, v40, v41\n"
    "v_cvt_pk_bf16_f32 v97, v42, v43\n"
    "v_mfma_f32_16x16x32_bf16 a[112:115], a[228:231], v[180:183], a[112:115]\n"
    "ds_write_b64 v223, v[96:97] offset:4096\n"
    "v_cvt_pk_bf16_f32 v108, v80, v81\n"
    "v_cvt_pk_bf16_f32 v110, v84, v85\n"
    "v_cvt_pk_bf16_f32 v109, v82, v83\n"
    "v_cvt_pk_bf16_f32 v111, v86, v87\n"
    "v_accvgpr_read_b32 v88, a44\n"
    "v_accvgpr_read_b32 v92, a108\n"
    "v_mfma_f32_16x16x32_bf16 a[112:115], a[224:227], v[168:171], a[112:115]\n"
    "s_nop 0\n"
    "ds_write_b64 v223, v[108:109] offset:0\n"
    "v_accvgpr_read_b32 v89, a45\n"
    "v_accvgpr_read_b32 v93, a109\n"
    "v_pk_mul_f32 v[88:89], v[88:89], v[242:243] op_sel_hi:[1,0]\n"
    "v_pk_mul_f32 v[92:93], v[92:93], v[244:245] op_sel_hi:[1,0]\n"
    "v_pk_mul_f32 v[24:25], v[24:25], v[248:249] op_sel_hi:[1,0]\n"
    "v_accvgpr_read_b32 v90, a46\n"
    "v_mfma_f32_16x16x32_bf16 v[48:51], a[224:227], v[172:175], v[48:51]\n"
    "s_waitcnt lgkmcnt(15)\n"
    "ds_write_b64 v223, v[110:111] offset:2048\n"
    "v_accvgpr_read_b32 v94, a110\n"
    "v_accvgpr_read_b32 v91, a47\n"
    "v_pk_mul_f32 v[90:91], v[90:91], v[242:243] op_sel_hi:[1,0]\n"
    "v_pk_mul_f32 v[26:27], v[26:27], v[248:249] op_sel_hi:[1,0]\n"
    "v_accvgpr_read_b32 v95, a111\n"
    "v_pk_mul_f32 v[94:95], v[94:95], v[244:245] op_sel_hi:[1,0]\n"
    "v_mfma_f32_16x16x32_bf16 v[52:55], a[232:235], v[172:175], v[52:55]\n"
    "v_cvt_pk_bf16_f32 v102, v44, v45\n"
    "v_cvt_pk_bf16_f32 v103, v46, v47\n"
    "v_cvt_pk_bf16_f32 v98, v88, v89\n"
    "v_cvt_pk_bf16_f32 v100, v92, v93\n"
    "v_cvt_pk_bf16_f32 v99, v90, v91\n"
    "v_pk_fma_f32 v[48:49], v[48:49], v[246:247], v[16:17] op_sel_hi:[1,0,1]\n"
    "v_pk_fma_f32 v[50:51], v[50:51], v[246:247], v[18:19] op_sel_hi:[1,0,1]\n"
    "v_cvt_pk_bf16_f32 v101, v94, v95\n"
    "v_pk_fma_f32 v[52:53], v[52:53], v[246:247], v[20:21] op_sel_hi:[1,0,1]\n"
    "v_mfma_f32_16x16x32_bf16 a[116:119], a[232:235], v[168:171], a[116:119]\n"
    "ds_write_b64 v224, v[102:103] offset:4096\n"
    "ds_read_b128 v[128:131], v225 offset:4096\n"
    "ds_read_b128 v[132:135], v225 offset:5120\n"
    "v_pk_fma_f32 v[54:55], v[54:55], v[246:247], v[22:23] op_sel_hi:[1,0,1]\n"
    "v_accvgpr_read_b32 v64, a48\n"
    "v_accvgpr_read_b32 v68, a112\n"
    "v_accvgpr_read_b32 v65, a49\n"
    "v_pk_mul_f32 v[64:65], v[64:65], v[242:243] op_sel_hi:[1,0]\n"
    "v_mfma_f32_16x16x32_bf16 a[116:119], a[236:239], v[180:183], a[116:119]\n"
    "v_accvgpr_read_b32 v69, a113\n"
    "v_accvgpr_read_b32 v66, a50\n"
    "v_accvgpr_read_b32 v70, a114\n"
    "v_accvgpr_read_b32 v67, a51\n"
    "v_accvgpr_read_b32 v71, a115\n"
    "v_pk_mul_f32 v[68:69], v[68:69], v[244:245] op_sel_hi:[1,0]\n"
    "v_pk_mul_f32 v[66:67], v[66:67], v[242:243] op_sel_hi:[1,0]\n"
    "v_pk_mul_f32 v[70:71], v[70:71], v[244:245] op_sel_hi:[1,0]\n"
    "v_pk_mul_f32 v[28:29], v[28:29], v[248:249] op_sel_hi:[1,0]\n"
    "v_mfma_f32_16x16x32_bf16 a[52:55], a[236:239], v[176:179], a[52:55]\n"
    "ds_write_b64 v224, v[98:99] offset:0\n"
    "ds_read_b128 v[112:115], v225 offset:0\n"
    "ds_read_b128 v[116:119], v225 offset:1024\n"
    "v_pk_mul_f32 v[30:31], v[30:31], v[248:249] op_sel_hi:[1,0]\n"
    "v_cvt_pk_bf16_f32 v108, v48, v49\n"
    "v_cvt_pk_bf16_f32 v109, v50, v51\n"
    "v_cvt_pk_bf16_f32 v104, v64, v65\n"
    "v_mfma_f32_16x16x32_bf16 a[52:55], a[232:235], v[164:167], a[52:55]\n"
    "s_waitcnt lgkmcnt(15)\n"
    "s_waitcnt lgkmcnt(3)\n"
    "buffer_store_dwordx4 v[128:131], v216, s[48:51], s57 offen offset:0\n"
    "ds_write_b64 v221, v[108:109] offset:6144\n"
    "v_cvt_pk_bf16_f32 v106, v68, v69\n"
    "v_cvt_pk_bf16_f32 v105, v66, v67\n"
    "v_cvt_pk_bf16_f32 v107, v70, v71\n"
    "v_mfma_f32_16x16x32_bf16 a[56:59], a[240:243], v[164:167], a[56:59]\n"
    "s_nop 0\n"
    "ds_write_b64 v224, v[100:101] offset:2048\n"
    "ds_read_b128 v[120:123], v225 offset:2048\n"
    "ds_read_b128 v[124:127], v225 offset:3072\n"
    "buffer_store_dwordx4 v[132:135], v216, s[48:51], s71 offen offset:0\n"
    "v_accvgpr_read_b32 v72, a52\n"
    "v_accvgpr_read_b32 v76, a116\n"
    "v_mfma_f32_16x16x32_bf16 a[56:59], a[244:247], v[176:179], a[56:59]\n"
    "s_waitcnt lgkmcnt(4)\n"
    "v_accvgpr_read_b32 v73, a53\n"
    "v_accvgpr_read_b32 v77, a117\n"
    "v_accvgpr_read_b32 v74, a54\n"
    "v_accvgpr_read_b32 v78, a118\n"
    "v_accvgpr_read_b32 v75, a55\n"
    "v_pk_mul_f32 v[72:73], v[72:73], v[242:243] op_sel_hi:[1,0]\n"
    "v_pk_mul_f32 v[76:77], v[76:77], v[244:245] op_sel_hi:[1,0]\n"
    "v_pk_mul_f32 v[74:75], v[74:75], v[242:243] op_sel_hi:[1,0]\n"
    "v_accvgpr_read_b32 v79, a119\n"
    "v_mfma_f32_16x16x32_bf16 a[120:123], a[244:247], v[180:183], a[120:123]\n"
    "Lpad9_%=:\n"
    "s_cbranch_execz Lhopd2_%=\n"
    "ds_write_b64 v221, v[104:105] offset:0\n"
    "v_cvt_pk_bf16_f32 v98, v52, v53\n"
    "v_cvt_pk_bf16_f32 v99, v54, v55\n"
    "v_pk_mul_f32 v[78:79], v[78:79], v[244:245] op_sel_hi:[1,0]\n"
    "v_cvt_pk_bf16_f32 v110, v72, v73\n"
    "v_cvt_pk_bf16_f32 v96, v76, v77\n"
    "v_cvt_pk_bf16_f32 v111, v74, v75\n"
    "v_mfma_f32_16x16x32_bf16 a[120:123], a[240:243], v[168:171], a[120:123]\n"
    "s_waitcnt lgkmcnt(1)\n"
    "buffer_store_dwordx4 v[112:115], v216, s[48:51], s55 offen offset:0\n"
    "ds_write_b64 v221, v[106:107] offset:2048\n"
    "v_cvt_pk_bf16_f32 v97, v78, v79\n"
    "v_accvgpr_read_b32 v80, a56\n"
    "v_accvgpr_read_b32 v81, a57\n"
    "v_pk_mul_f32 v[80:81], v[80:81], v[242:243] op_sel_hi:[1,0]\n"
    "v_mfma_f32_16x16x32_bf16 v[56:59], a[240:243], v[172:175], v[56:59]\n"
    "s_waitcnt lgkmcnt(15)\n"
    "ds_write_b64 v222, v[98:99] offset:6144\n"
    "v_accvgpr_read_b32 v84, a120\n"
    "v_accvgpr_read_b32 v85, a121\n"
    "v_pk_mul_f32 v[84:85], v[84:85], v[244:245] op_sel_hi:[1,0]\n"
    "v_accvgpr_read_b32 v82, a58\n"
    "v_accvgpr_read_b32 v86, a122\n"
    "v_accvgpr_read_b32 v83, a59\n"
    "v_mfma_f32_16x16x32_bf16 v[60:63], a[248:251], v[172:175], v[60:63]\n"
    "s_nop 0\n"
    "buffer_store_dwordx4 v[120:123], v216, s[48:51], s56 offen offset:0\n"
    "ds_write_b64 v222, v[110:111] offset:0\n"
    "v_pk_fma_f32 v[56:57], v[56:57], v[246:247], v[24:25] op_sel_hi:[1,0,1]\n"
    "v_pk_fma_f32 v[58:59], v[58:59], v[246:247], v[26:27] op_sel_hi:[1,0,1]\n"
    "v_pk_mul_f32 v[82:83], v[82:83], v[242:243] op_sel_hi:[1,0]\n"
    "v_accvgpr_read_b32 v87, a123\n"
    "v_mfma_f32_16x16x32_bf16 a[124:127], a[248:251], v[168:171], a[124:127]\n"
    "ds_write_b64 v222, v[96:97] offset:2048\n"
    "v_cvt_pk_bf16_f32 v104, v56, v57\n"
    "v_cvt_pk_bf16_f32 v105, v58, v59\n"
    "v_pk_mul_f32 v[86:87], v[86:87], v[244:245] op_sel_hi:[1,0]\n"
    "v_pk_fma_f32 v[60:61], v[60:61], v[246:247], v[28:29] op_sel_hi:[1,0,1]\n"
    "v_pk_fma_f32 v[62:63], v[62:63], v[246:247], v[30:31] op_sel_hi:[1,0,1]\n"
    "v_cvt_pk_bf16_f32 v100, v80, v81\n"
    "v_mfma_f32_16x16x32_bf16 a[124:127], a[252:255], v[180:183], a[124:127]\n"
    "buffer_store_dwordx4 v[116:119], v216, s[48:51], s69 offen offset:0\n"
    "ds_write_b64 v223, v[104:105] offset:6144\n"
    "v_cvt_pk_bf16_f32 v102, v84, v85\n"
    "v_cvt_pk_bf16_f32 v101, v82, v83\n"
    "v_cvt_pk_bf16_f32 v103, v86, v87\n"
    "v_cvt_pk_bf16_f32 v110, v60, v61\n"
    "v_mfma_f32_16x16x32_bf16 a[60:63], a[252:255], v[176:179], a[60:63]\n"
    "ds_write_b64 v223, v[100:101] offset:0\n"
    "v_accvgpr_read_b32 v92, a124\n"
    "v_accvgpr_read_b32 v93, a125\n"
    "v_pk_mul_f32 v[92:93], v[92:93], v[244:245] op_sel_hi:[1,0]\n"
    "v_accvgpr_read_b32 v94, a126\n"
    "v_accvgpr_read_b32 v95, a127\n"
    "v_pk_mul_f32 v[94:95], v[94:95], v[244:245] op_sel_hi:[1,0]\n"
    "v_mfma_f32_16x16x32_bf16 a[60:63], a[248:251], v[164:167], a[60:63]\n"
    "buffer_store_dwordx4 v[124:127], v216, s[48:51], s70 offen offset:0\n"
    "ds_write_b64 v223, v[102:103] offset:2048\n"
    "v_cvt_pk_bf16_f32 v111, v62, v63\n"
    "v_cvt_pk_bf16_f32 v108, v92, v93\n"
    "v_cvt_pk_bf16_f32 v109, v94, v95\n"
    "ds_write_b64 v224, v[110:111] offset:6144\n"
    "ds_read_b128 v[120:123], v225 offset:6144\n"
    "ds_read_b128 v[124:127], v225 offset:7168\n"
    "v_accvgpr_read_b32 v88, a60\n"
    "v_accvgpr_read_b32 v89, a61\n"
    "v_accvgpr_read_b32 v90, a62\n"
    "v_accvgpr_read_b32 v91, a63\n"
    "v_pk_mul_f32 v[88:89], v[88:89], v[242:243] op_sel_hi:[1,0]\n"
    "v_pk_mul_f32 v[90:91], v[90:91], v[242:243] op_sel_hi:[1,0]\n"
    "v_cvt_pk_bf16_f32 v106, v88, v89\n"
    "v_cvt_pk_bf16_f32 v107, v90, v91\n"
    "ds_write_b64 v224, v[106:107] offset:0\n"
    "ds_read_b128 v[136:139], v225 offset:0\n"
    "ds_read_b128 v[140:143], v225 offset:1024\n"
    "ds_write_b64 v224, v[108:109] offset:2048\n"
    "ds_read_b128 v[112:115], v225 offset:2048\n"
    "ds_read_b128 v[116:119], v225 offset:3072\n"
    "s_waitcnt lgkmcnt(6)\n"
    "buffer_store_dwordx4 v[120:123], v216, s[48:51], s57 offen offset:128\n"
    "s_waitcnt lgkmcnt(3)\n"
    "buffer_store_dwordx4 v[136:139], v216, s[48:51], s55 offen offset:128\n"
    "s_waitcnt lgkmcnt(0)\n"
    "buffer_store_dwordx4 v[112:115], v216, s[48:51], s56 offen offset:128\n"
    "buffer_store_dwordx4 v[140:143], v216, s[48:51], s69 offen offset:128\n"
    "buffer_store_dwordx4 v[116:119], v216, s[48:51], s70 offen offset:128\n"
    "buffer_store_dwordx4 v[124:127], v216, s[48:51], s71 offen offset:128\n"
    "s_endpgm\n"
    "LrareA_%=:\n"
    "s_nop 7\n"
    "v_max3_f32 v243, v64, v65, v66\n"
    "v_max3_f32 v243, v243, v67, v76\n"
    "v_max3_f32 v243, v243, v77, v78\n"
    "v_max3_f32 v243, v243, v79, v88\n"
    "v_max3_f32 v243, v243, v89, v90\n"
    "v_max3_f32 v243, v243, v91, v96\n"
    "v_max3_f32 v243, v243, v97, v98\n"
    "v_max_f32 v243, v243, v99\n"
    "v_mov_b32 v245, v243\n"
    "s_nop 1\n"
    "v_permlane32_swap_b32 v243, v245\n"
    "v_max_f32 v243, v243, v245\n"
    "v_mov_b32 v245, v243\n"
    "s_nop 1\n"
    "v_permlane16_swap_b32 v243, v245\n"
    "v_max_f32 v243, v243, v245\n"
    "v_mul_f32 v243, s64, v243\n"
    "v_min_f32 v243, v243, v232\n"
    "v_sub_f32 v245, v243, v232\n"
    "v_exp_f32 v245, v245\n"
    "v_mov_b32_e64 v232, v243\n"
    "v_max3_f32 v243, v68, v69, v70\n"
    "v_max3_f32 v243, v243, v71, v80\n"
    "v_max3_f32 v243, v243, v81, v82\n"
    "v_max3_f32 v243, v243, v83, v92\n"
    "v_max3_f32 v243, v243, v93, v94\n"
    "v_max3_f32 v243, v243, v95, v100\n"
    "v_max3_f32 v243, v243, v101, v102\n"
    "v_max_f32 v243, v243, v103\n"
    "v_mov_b32 v245, v243\n"
    "s_nop 1\n"
    "v_permlane32_swap_b32 v243, v245\n"
    "v_max_f32 v243, v243, v245\n"
    "v_mov_b32 v245, v243\n"
    "s_nop 1\n"
    "v_permlane16_swap_b32 v243, v245\n"
    "v_max_f32 v243, v243, v245\n"
    "v_mul_f32 v243, s64, v243\n"
    "v_min_f32 v243, v243, v233\n"
    "v_sub_f32 v245, v243, v233\n"
    "v_exp_f32 v245, v245\n"
    "v_mov_b32 v233, v243\n"
    "v_max3_f32 v243, v72, v73, v74\n"
    "v_max3_f32 v243, v243, v75, v84\n"
    "v_max3_f32 v243, v243, v85, v86\n"
    "v_max_f32 v243, v243, v87\n"
    "v_mov_b32 v245, v243\n"
    "s_nop 1\n"
    "v_permlane32_swap_b32 v243, v245\n"
    "v_max_f32 v243, v243, v245\n"
    "v_mov_b32 v245, v243\n"
    "s_nop 1\n"
    "v_permlane16_swap_b32 v243, v245\n"
    "v_max_f32 v243, v243, v245\n"
    "v_mul_f32 v243, s64, v243\n"
    "v_min_f32 v243, v243, v234\n"
    "v_sub_f32 v245, v243, v234\n"
    "v_exp_f32 v245, v245\n"
    "v_mov_b32 v234, v243\n"
    "v_fma_f32 v184, v64, s58, v232\n"
    "v_fma_f32 v185, v65, s58, v232\n"
    "v_fma_f32 v186, v66, s58, v232\n"
    "v_fma_f32 v187, v67, s58, v232\n"
    "v_exp_f32_e64 v184, v184\n"
    "v_fma_f32 v188, v76, s58, v232\n"
    "v_exp_f32_e64 v185, v185\n"
    "v_fma_f32 v189, v77, s58, v232\n"
    "v_exp_f32_e64 v186, v186\n"
    "v_fma_f32 v190, v78, s58, v232\n"
    "v_exp_f32_e64 v187, v187\n"
    "v_fma_f32 v191, v79, s58, v232\n"
    "v_exp_f32 v188, v188\n"
    "v_add_f32 v238, v184, v185\n"
    "v_cvt_pk_bf16_f32 v144, v184, v185\n"
    "v_fma_f32 v192, v68, s58, v233\n"
    "v_exp_f32 v189, v189\n"
    "v_add_f32 v238, v238, v186\n"
    "v_fma_f32 v193, v69, s58, v233\n"
    "v_exp_f32 v190, v190\n"
    "v_add_f32 v238, v238, v187\n"
    "v_cvt_pk_bf16_f32 v145, v186, v187\n"
    "v_fma_f32 v194, v70, s58, v233\n"
    "v_exp_f32 v191, v191\n"
    "v_add_f32 v238, v238, v188\n"
    "v_fma_f32 v195, v71, s58, v233\n"
    "v_exp_f32 v192, v192\n"
    "v_add_f32 v238, v238, v189\n"
    "v_cvt_pk_bf16_f32 v146, v188, v189\n"
    "v_fma_f32 v196, v80, s58, v233\n"
    "v_exp_f32 v193, v193\n"
    "v_add_f32 v238, v238, v190\n"
    "v_fma_f32 v197, v81, s58, v233\n"
    "v_exp_f32 v194, v194\n"
    "v_add_f32 v238, v238, v191\n"
    "v_cvt_pk_bf16_f32 v147, v190, v191\n"
    "v_fma_f32 v198, v82, s58, v233\n"
    "v_exp_f32_e64 v195, v195\n"
    "v_fma_f32 v199, v83, s58, v233\n"
    "v_exp_f32 v196, v196\n"
    "v_add_f32 v239, v192, v193\n"
    "v_cvt_pk_bf16_f32 v148, v192, v193\n"
    "v_fma_f32 v184, v72, s58, v234\n"
    "v_exp_f32 v197, v197\n"
    "v_add_f32 v239, v239, v194\n"
    "v_fma_f32 v185, v73, s58, v234\n"
    "v_exp_f32 v198, v198\n"
    "v_add_f32 v239, v239, v195\n"
    "v_cvt_pk_bf16_f32 v149, v194, v195\n"
    "v_fma_f32 v186, v74, s58, v234\n"
    "v_exp_f32 v199, v199\n"
    "v_add_f32 v239, v239, v196\n"
    "v_fma_f32 v187, v75, s58, v234\n"
    "v_exp_f32 v184, v184\n"
    "v_add_f32 v239, v239, v197\n"
    "v_cvt_pk_bf16_f32 v150, v196, v197\n"
    "v_fma_f32 v188, v84, s58, v234\n"
    "v_exp_f32 v185, v185\n"
    "v_add_f32 v239, v239, v198\n"
    "v_fma_f32 v189, v85, s58, v234\n"
    "v_exp_f32 v186, v186\n"
    "v_add_f32 v239, v239, v199\n"
    "v_cvt_pk_bf16_f32 v151, v198, v199\n"
    "v_fma_f32 v190, v86, s58, v234\n"
    "v_exp_f32_e64 v187, v187\n"
    "v_fma_f32 v191, v87, s58, v234\n"
    "v_exp_f32 v188, v188\n"
    "v_add_f32 v240, v184, v185\n"
    "v_cvt_pk_bf16_f32 v152, v184, v185\n"
    "v_fma_f32 v192, v88, s58, v232\n"
    "v_exp_f32 v189, v189\n"
    "v_add_f32 v240, v240, v186\n"
    "v_fma_f32 v193, v89, s58, v232\n"
    "v_exp_f32 v190, v190\n"
    "v_add_f32 v240, v240, v187\n"
    "v_cvt_pk_bf16_f32 v153, v186, v187\n"
    "v_fma_f32 v194, v90, s58, v232\n"
    "v_exp_f32 v191, v191\n"
    "v_add_f32 v240, v240, v188\n"
    "v_fma_f32 v195, v91, s58, v232\n"
    "v_exp_f32 v192, v192\n"
    "v_add_f32 v240, v240, v189\n"
    "v_cvt_pk_bf16_f32 v154, v188, v189\n"
    "v_fma_f32 v196, v96, s58, v232\n"
    "v_exp_f32 v193, v193\n"
    "v_add_f32 v240, v240, v190\n"
    "v_fma_f32 v197, v97, s58, v232\n"
    "v_exp_f32 v194, v194\n"
    "v_add_f32 v240, v240, v191\n"
    "v_cvt_pk_bf16_f32 v155, v190, v191\n"
    "v_fma_f32 v198, v98, s58, v232\n"
    "v_exp_f32 v195, v195\n"
    "v_add_f32 v238, v238, v192\n"
    "v_fma_f32 v199, v99, s58, v232\n"
    "v_exp_f32 v196, v196\n"
    "v_add_f32 v238, v238, v193\n"
    "v_cvt_pk_bf16_f32 v156, v192, v193\n"
    "v_fma_f32 v184, v92, s58, v233\n"
    "v_exp_f32 v197, v197\n"
    "v_add_f32 v238, v238, v194\n"
    "v_fma_f32 v185, v93, s58, v233\n"
    "v_exp_f32 v198, v198\n"
    "v_add_f32 v238, v238, v195\n"
    "v_cvt_pk_bf16_f32 v157, v194, v195\n"
    "v_fma_f32 v186, v94, s58, v233\n"
    "v_exp_f32 v199, v199\n"
    "v_add_f32 v238, v238, v196\n"
    "v_fma_f32 v187, v95, s58, v233\n"
    "v_exp_f32 v184, v184\n"
    "v_add_f32 v238, v238, v197\n"
    "v_cvt_pk_bf16_f32 v158, v196, v197\n"
    "v_fma_f32 v188, v100, s58, v233\n"
    "v_exp_f32 v185, v185\n"
    "v_add_f32 v238, v238, v198\n"
    "v_fma_f32 v189, v101, s58, v233\n"
    "v_exp_f32 v186, v186\n"
    "v_add_f32 v238, v238, v199\n"
    "v_cvt_pk_bf16_f32 v159, v198, v199\n"
    "v_fma_f32 v190, v102, s58, v233\n"
    "v_exp_f32 v187, v187\n"
    "v_add_f32 v239, v239, v184\n"
    "v_fma_f32 v191, v103, s58, v233\n"
    "v_exp_f32 v188, v188\n"
    "v_add_f32 v239, v239, v185\n"
    "v_cvt_pk_bf16_f32 v160, v184, v185\n"
    "v_exp_f32 v189, v189\n"
    "v_add_f32 v239, v239, v186\n"
    "v_exp_f32 v190, v190\n"
    "v_add_f32 v239, v239, v187\n"
    "v_cvt_pk_bf16_f32 v161, v186, v187\n"
    "v_exp_f32 v191, v191\n"
    "v_add_f32 v239, v239, v188\n"
    "v_add_f32_e64 v239, v239, v189\n"
    "v_cvt_pk_bf16_f32 v162, v188, v189\n"
    "v_add_f32_e64 v239, v239, v190\n"
    "v_add_f32 v239, v239, v191\n"
    "v_cvt_pk_bf16_f32 v163, v190, v191\n"
    "s_nop 3\n"
    "s_branch LbackA_%=\n"
    "LrareU0_%=:\n"
    "s_nop 7\n"
    "v_max3_f32 v243, v104, v105, v106\n"
    "v_max3_f32 v243, v243, v107, v116\n"
    "v_max3_f32 v243, v243, v117, v118\n"
    "v_max3_f32 v243, v243, v119, v128\n"
    "v_max3_f32 v243, v243, v129, v130\n"
    "v_max3_f32 v243, v243, v131, v136\n"
    "v_max3_f32 v243, v243, v137, v138\n"
    "v_max_f32 v243, v243, v139\n"
    "v_mov_b32 v245, v243\n"
    "s_nop 1\n"
    "v_permlane32_swap_b32 v243, v245\n"
    "v_max_f32 v243, v243, v245\n"
    "v_mov_b32 v245, v243\n"
    "s_nop 1\n"
    "v_permlane16_swap_b32 v243, v245\n"
    "v_max_f32 v243, v243, v245\n"
    "v_mul_f32 v243, s64, v243\n"
    "v_min_f32 v243, v243, v232\n"
    "v_sub_f32 v245, v243, v232\n"
    "v_exp_f32 v245, v245\n"
    "v_mov_b32 v232, v243\n"
    "v_mul_f32_e64 v236, v236, v245\n"
    "v_accvgpr_read_b32 v184, a0\n"
    "v_accvgpr_read_b32 v185, a1\n"
    "v_accvgpr_read_b32 v186, a2\n"
    "v_accvgpr_read_b32 v187, a3\n"
    "v_mul_f32 v184, v184, v245\n"
    "v_mul_f32 v185, v185, v245\n"
    "v_mul_f32 v186, v186, v245\n"
    "v_mul_f32 v187, v187, v245\n"
    "v_accvgpr_write_b32 a0, v184\n"
    "v_accvgpr_write_b32 a1, v185\n"
    "v_accvgpr_write_b32 a2, v186\n"
    "v_accvgpr_write_b32 a3, v187\n"
    "v_accvgpr_read_b32 v184, a4\n"
    "v_accvgpr_read_b32 v185, a5\n"
    "v_accvgpr_read_b32 v186, a6\n"
    "v_accvgpr_read_b32 v187, a7\n"
    "v_mul_f32 v184, v184, v245\n"
    "v_mul_f32 v185, v185, v245\n"
    "v_mul_f32 v186, v186, v245\n"
    "v_mul_f32 v187, v187, v245\n"
    "v_accvgpr_write_b32 a4, v184\n"
    "v_accvgpr_write_b32 a5, v185\n"
    "v_accvgpr_write_b32 a6, v186\n"
    "v_accvgpr_write_b32 a7, v187\n"
    "v_accvgpr_read_b32 v184, a8\n"
    "v_accvgpr_read_b32 v185, a9\n"
    "v_accvgpr_read_b32 v186, a10\n"
    "v_accvgpr_read_b32 v187, a11\n"
    "v_mul_f32 v184, v184, v245\n"
    "v_mul_f32 v185, v185, v245\n"
    "v_mul_f32 v186, v186, v245\n"
    "v_mul_f32 v187, v187, v245\n"
    "v_accvgpr_write_b32 a8, v184\n"
    "v_accvgpr_write_b32 a9, v185\n"
    "v_accvgpr_write_b32 a10, v186\n"
    "v_accvgpr_write_b32 a11, v187\n"
    "v_accvgpr_read_b32 v184, a12\n"
    "v_accvgpr_read_b32 v185, a13\n"
    "v_accvgpr_read_b32 v186, a14\n"
    "v_accvgpr_read_b32 v187, a15\n"
    "v_mul_f32 v184, v184, v245\n"
    "v_mul_f32 v185, v185, v245\n"
    "v_mul_f32 v186, v186, v245\n"
    "v_mul_f32 v187, v187, v245\n"
    "v_accvgpr_write_b32 a12, v184\n"
    "v_accvgpr_write_b32 a13, v185\n"
    "v_accvgpr_write_b32 a14, v186\n"
    "v_accvgpr_write_b32 a15, v187\n"
    "v_accvgpr_read_b32 v184, a16\n"
    "v_accvgpr_read_b32 v185, a17\n"
    "v_accvgpr_read_b32 v186, a18\n"
    "v_accvgpr_read_b32 v187, a19\n"
    "v_mul_f32 v184, v184, v245\n"
    "v_mul_f32 v185, v185, v245\n"
    "v_mul_f32 v186, v186, v245\n"
    "v_mul_f32 v187, v187, v245\n"
    "v_accvgpr_write_b32 a16, v184\n"
    "v_accvgpr_write_b32 a17, v185\n"
    "v_accvgpr_write_b32 a18, v186\n"
    "v_accvgpr_write_b32 a19, v187\n"
    "v_accvgpr_read_b32 v184, a20\n"
    "v_accvgpr_read_b32 v185, a21\n"
    "v_accvgpr_read_b32 v186, a22\n"
    "v_accvgpr_read_b32 v187, a23\n"
    "v_mul_f32 v184, v184, v245\n"
    "v_mul_f32 v185, v185, v245\n"
    "v_mul_f32 v186, v186, v245\n"
    "v_mul_f32 v187, v187, v245\n"
    "v_accvgpr_write_b32 a20, v184\n"
    "v_accvgpr_write_b32 a21, v185\n"
    "v_accvgpr_write_b32 a22, v186\n"
    "v_accvgpr_write_b32 a23, v187\n"
    "v_accvgpr_read_b32 v184, a24\n"
    "v_accvgpr_read_b32 v185, a25\n"
    "v_accvgpr_read_b32 v186, a26\n"
    "v_accvgpr_read_b32 v187, a27\n"
    "v_mul_f32 v184, v184, v245\n"
    "v_mul_f32 v185, v185, v245\n"
    "v_mul_f32 v186, v186, v245\n"
    "v_mul_f32 v187, v187, v245\n"
    "v_accvgpr_write_b32 a24, v184\n"
    "v_accvgpr_write_b32 a25, v185\n"
    "v_accvgpr_write_b32 a26, v186\n"
    "v_accvgpr_write_b32 a27, v187\n"
    "v_accvgpr_read_b32 v184, a28\n"
    "v_accvgpr_read_b32 v185, a29\n"
    "v_accvgpr_read_b32 v186, a30\n"
    "v_accvgpr_read_b32 v187, a31\n"
    "v_mul_f32 v184, v184, v245\n"
    "v_mul_f32 v185, v185, v245\n"
    "v_mul_f32 v186, v186, v245\n"
    "v_mul_f32 v187, v187, v245\n"
    "v_accvgpr_write_b32 a28, v184\n"
    "v_accvgpr_write_b32 a29, v185\n"
    "v_accvgpr_write_b32 a30, v186\n"
    "v_accvgpr_write_b32 a31, v187\n"
    "v_accvgpr_read_b32 v184, a32\n"
    "v_accvgpr_read_b32 v185, a33\n"
    "v_accvgpr_read_b32 v186, a34\n"
    "v_accvgpr_read_b32 v187, a35\n"
    "v_mul_f32 v184, v184, v245\n"
    "v_mul_f32 v185, v185, v245\n"
    "v_mul_f32 v186, v186, v245\n"
    "v_mul_f32 v187, v187, v245\n"
    "v_accvgpr_write_b32 a32, v184\n"
    "v_accvgpr_write_b32 a33, v185\n"
    "v_accvgpr_write_b32 a34, v186\n"
    "v_accvgpr_write_b32 a35, v187\n"
    "v_accvgpr_read_b32 v184, a36\n"
    "v_accvgpr_read_b32 v185, a37\n"
    "v_accvgpr_read_b32 v186, a38\n"
    "v_accvgpr_read_b32 v187, a39\n"
    "v_mul_f32 v184, v184, v245\n"
    "v_mul_f32 v185, v185, v245\n"
    "v_mul_f32 v186, v186, v245\n"
    "v_mul_f32 v187, v187, v245\n"
    "v_accvgpr_write_b32 a36, v184\n"
    "v_accvgpr_write_b32 a37, v185\n"
    "v_accvgpr_write_b32 a38, v186\n"
    "v_accvgpr_write_b32 a39, v187\n"
    "v_accvgpr_read_b32 v184, a40\n"
    "v_accvgpr_read_b32 v185, a41\n"
    "v_accvgpr_read_b32 v186, a42\n"
    "v_accvgpr_read_b32 v187, a43\n"
    "v_mul_f32 v184, v184, v245\n"
    "v_mul_f32 v185, v185, v245\n"
    "v_mul_f32 v186, v186, v245\n"
    "v_mul_f32 v187, v187, v245\n"
    "v_accvgpr_write_b32 a40, v184\n"
    "v_accvgpr_write_b32 a41, v185\n"
    "v_accvgpr_write_b32 a42, v186\n"
    "v_accvgpr_write_b32 a43, v187\n"
    "v_accvgpr_read_b32 v184, a44\n"
    "v_accvgpr_read_b32 v185, a45\n"
    "v_accvgpr_read_b32 v186, a46\n"
    "v_accvgpr_read_b32 v187, a47\n"
    "v_mul_f32 v184, v184, v245\n"
    "v_mul_f32 v185, v185, v245\n"
    "v_mul_f32 v186, v186, v245\n"
    "v_mul_f32 v187, v187, v245\n"
    "v_accvgpr_write_b32 a44, v184\n"
    "v_accvgpr_write_b32 a45, v185\n"
    "v_accvgpr_write_b32 a46, v186\n"
    "v_accvgpr_write_b32 a47, v187\n"
    "v_accvgpr_read_b32 v184, a48\n"
    "v_accvgpr_read_b32 v185, a49\n"
    "v_accvgpr_read_b32 v186, a50\n"
    "v_accvgpr_read_b32 v187, a51\n"
    "v_mul_f32 v184, v184, v245\n"
    "v_mul_f32 v185, v185, v245\n"
    "v_mul_f32 v186, v186, v245\n"
    "v_mul_f32 v187, v187, v245\n"
    "v_accvgpr_write_b32 a48, v184\n"
    "v_accvgpr_write_b32 a49, v185\n"
    "v_accvgpr_write_b32 a50, v186\n"
    "v_accvgpr_write_b32 a51, v187\n"
    "v_accvgpr_read_b32 v184, a52\n"
    "v_accvgpr_read_b32 v185, a53\n"
    "v_accvgpr_read_b32 v186, a54\n"
    "v_accvgpr_read_b32 v187, a55\n"
    "v_mul_f32 v184, v184, v245\n"
    "v_mul_f32 v185, v185, v245\n"
    "v_mul_f32 v186, v186, v245\n"
    "v_mul_f32 v187, v187, v245\n"
    "v_accvgpr_write_b32 a52, v184\n"
    "v_accvgpr_write_b32 a53, v185\n"
    "v_accvgpr_write_b32 a54, v186\n"
    "v_accvgpr_write_b32 a55, v187\n"
    "v_accvgpr_read_b32 v184, a56\n"
    "v_accvgpr_read_b32 v185, a57\n"
    "v_accvgpr_read_b32 v186, a58\n"
    "v_accvgpr_read_b32 v187, a59\n"
    "v_mul_f32 v184, v184, v245\n"
    "v_mul_f32 v185, v185, v245\n"
    "v_mul_f32 v186, v186, v245\n"
    "v_mul_f32 v187, v187, v245\n"
    "v_accvgpr_write_b32 a56, v184\n"
    "v_accvgpr_write_b32 a57, v185\n"
    "v_accvgpr_write_b32 a58, v186\n"
    "v_accvgpr_write_b32 a59, v187\n"
    "v_accvgpr_read_b32 v184, a60\n"
    "v_accvgpr_read_b32 v185, a61\n"
    "v_accvgpr_read_b32 v186, a62\n"
    "v_accvgpr_read_b32 v187, a63\n"
    "v_mul_f32 v184, v184, v245\n"
    "v_mul_f32 v185, v185, v245\n"
    "v_mul_f32 v186, v186, v245\n"
    "v_mul_f32 v187, v187, v245\n"
    "v_accvgpr_write_b32 a60, v184\n"
    "v_accvgpr_write_b32 a61, v185\n"
    "v_accvgpr_write_b32 a62, v186\n"
    "v_accvgpr_write_b32 a63, v187\n"
    "v_max3_f32 v243, v108, v109, v110\n"
    "v_max3_f32 v243, v243, v111, v120\n"
    "v_max3_f32 v243, v243, v121, v122\n"
    "v_max3_f32 v243, v243, v123, v132\n"
    "v_max3_f32 v243, v243, v133, v134\n"
    "v_max3_f32 v243, v243, v135, v140\n"
    "v_max3_f32 v243, v243, v141, v142\n"
    "v_max_f32 v243, v243, v143\n"
    "v_mov_b32 v245, v243\n"
    "s_nop 1\n"
    "v_permlane32_swap_b32 v243, v245\n"
    "v_max_f32 v243, v243, v245\n"
    "v_mov_b32 v245, v243\n"
    "s_nop 1\n"
    "v_permlane16_swap_b32 v243, v245\n"
    "v_max_f32 v243, v243, v245\n"
    "v_mul_f32 v243, s64, v243\n"
    "v_min_f32 v243, v243, v233\n"
    "v_sub_f32 v245, v243, v233\n"
    "v_exp_f32 v245, v245\n"
    "v_mov_b32 v233, v243\n"
    "v_mul_f32_e64 v237, v237, v245\n"
    "v_accvgpr_read_b32 v184, a64\n"
    "v_accvgpr_read_b32 v185, a65\n"
    "v_accvgpr_read_b32 v186, a66\n"
    "v_accvgpr_read_b32 v187, a67\n"
    "v_mul_f32 v184, v184, v245\n"
    "v_mul_f32 v185, v185, v245\n"
    "v_mul_f32 v186, v186, v245\n"
    "v_mul_f32 v187, v187, v245\n"
    "v_accvgpr_write_b32 a64, v184\n"
    "v_accvgpr_write_b32 a65, v185\n"
    "v_accvgpr_write_b32 a66, v186\n"
    "v_accvgpr_write_b32 a67, v187\n"
    "v_accvgpr_read_b32 v184, a68\n"
    "v_accvgpr_read_b32 v185, a69\n"
    "v_accvgpr_read_b32 v186, a70\n"
    "v_accvgpr_read_b32 v187, a71\n"
    "v_mul_f32 v184, v184, v245\n"
    "v_mul_f32 v185, v185, v245\n"
    "v_mul_f32 v186, v186, v245\n"
    "v_mul_f32 v187, v187, v245\n"
    "v_accvgpr_write_b32 a68, v184\n"
    "v_accvgpr_write_b32 a69, v185\n"
    "v_accvgpr_write_b32 a70, v186\n"
    "v_accvgpr_write_b32 a71, v187\n"
    "v_accvgpr_read_b32 v184, a72\n"
    "v_accvgpr_read_b32 v185, a73\n"
    "v_accvgpr_read_b32 v186, a74\n"
    "v_accvgpr_read_b32 v187, a75\n"
    "v_mul_f32 v184, v184, v245\n"
    "v_mul_f32 v185, v185, v245\n"
    "v_mul_f32 v186, v186, v245\n"
    "v_mul_f32 v187, v187, v245\n"
    "v_accvgpr_write_b32 a72, v184\n"
    "v_accvgpr_write_b32 a73, v185\n"
    "v_accvgpr_write_b32 a74, v186\n"
    "v_accvgpr_write_b32 a75, v187\n"
    "v_accvgpr_read_b32 v184, a76\n"
    "v_accvgpr_read_b32 v185, a77\n"
    "v_accvgpr_read_b32 v186, a78\n"
    "v_accvgpr_read_b32 v187, a79\n"
    "v_mul_f32 v184, v184, v245\n"
    "v_mul_f32 v185, v185, v245\n"
    "v_mul_f32 v186, v186, v245\n"
    "v_mul_f32 v187, v187, v245\n"
    "v_accvgpr_write_b32 a76, v184\n"
    "v_accvgpr_write_b32 a77, v185\n"
    "v_accvgpr_write_b32 a78, v186\n"
    "v_accvgpr_write_b32 a79, v187\n"
    "v_accvgpr_read_b32 v184, a80\n"
    "v_accvgpr_read_b32 v185, a81\n"
    "v_accvgpr_read_b32 v186, a82\n"
    "v_accvgpr_read_b32 v187, a83\n"
    "v_mul_f32 v184, v184, v245\n"
    "v_mul_f32 v185, v185, v245\n"
    "v_mul_f32 v186, v186, v245\n"
    "v_mul_f32 v187, v187, v245\n"
    "v_accvgpr_write_b32 a80, v184\n"
    "v_accvgpr_write_b32 a81, v185\n"
    "v_accvgpr_write_b32 a82, v186\n"
    "v_accvgpr_write_b32 a83, v187\n"
    "v_accvgpr_read_b32 v184, a84\n"
    "v_accvgpr_read_b32 v185, a85\n"
    "v_accvgpr_read_b32 v186, a86\n"
    "v_accvgpr_read_b32 v187, a87\n"
    "v_mul_f32 v184, v184, v245\n"
    "v_mul_f32 v185, v185, v245\n"
    "v_mul_f32 v186, v186, v245\n"
    "v_mul_f32 v187, v187, v245\n"
    "v_accvgpr_write_b32 a84, v184\n"
    "v_accvgpr_write_b32 a85, v185\n"
    "v_accvgpr_write_b32 a86, v186\n"
    "v_accvgpr_write_b32 a87, v187\n"
    "v_accvgpr_read_b32 v184, a88\n"
    "v_accvgpr_read_b32 v185, a89\n"
    "v_accvgpr_read_b32 v186, a90\n"
    "v_accvgpr_read_b32 v187, a91\n"
    "v_mul_f32 v184, v184, v245\n"
    "v_mul_f32 v185, v185, v245\n"
    "v_mul_f32 v186, v186, v245\n"
    "v_mul_f32 v187, v187, v245\n"
    "v_accvgpr_write_b32 a88, v184\n"
    "v_accvgpr_write_b32 a89, v185\n"
    "v_accvgpr_write_b32 a90, v186\n"
    "v_accvgpr_write_b32 a91, v187\n"
    "v_accvgpr_read_b32 v184, a92\n"
    "v_accvgpr_read_b32 v185, a93\n"
    "v_accvgpr_read_b32 v186, a94\n"
    "v_accvgpr_read_b32 v187, a95\n"
    "v_mul_f32 v184, v184, v245\n"
    "v_mul_f32 v185, v185, v245\n"
    "v_mul_f32 v186, v186, v245\n"
    "v_mul_f32 v187, v187, v245\n"
    "v_accvgpr_write_b32 a92, v184\n"
    "v_accvgpr_write_b32 a93, v185\n"
    "v_accvgpr_write_b32 a94, v186\n"
    "v_accvgpr_write_b32 a95, v187\n"
    "v_accvgpr_read_b32 v184, a96\n"
    "v_accvgpr_read_b32 v185, a97\n"
    "v_accvgpr_read_b32 v186, a98\n"
    "v_accvgpr_read_b32 v187, a99\n"
    "v_mul_f32 v184, v184, v245\n"
    "v_mul_f32 v185, v185, v245\n"
    "v_mul_f32 v186, v186, v245\n"
    "v_mul_f32 v187, v187, v245\n"
    "v_accvgpr_write_b32 a96, v184\n"
    "v_accvgpr_write_b32 a97, v185\n"
    "v_accvgpr_write_b32 a98, v186\n"
    "v_accvgpr_write_b32 a99, v187\n"
    "v_accvgpr_read_b32 v184, a100\n"
    "v_accvgpr_read_b32 v185, a101\n"
    "v_accvgpr_read_b32 v186, a102\n"
    "v_accvgpr_read_b32 v187, a103\n"
    "v_mul_f32 v184, v184, v245\n"
    "v_mul_f32 v185, v185, v245\n"
    "v_mul_f32 v186, v186, v245\n"
    "v_mul_f32 v187, v187, v245\n"
    "v_accvgpr_write_b32 a100, v184\n"
    "v_accvgpr_write_b32 a101, v185\n"
    "v_accvgpr_write_b32 a102, v186\n"
    "v_accvgpr_write_b32 a103, v187\n"
    "v_accvgpr_read_b32 v184, a104\n"
    "v_accvgpr_read_b32 v185, a105\n"
    "v_accvgpr_read_b32 v186, a106\n"
    "v_accvgpr_read_b32 v187, a107\n"
    "v_mul_f32 v184, v184, v245\n"
    "v_mul_f32 v185, v185, v245\n"
    "v_mul_f32 v186, v186, v245\n"
    "v_mul_f32 v187, v187, v245\n"
    "v_accvgpr_write_b32 a104, v184\n"
    "v_accvgpr_write_b32 a105, v185\n"
    "v_accvgpr_write_b32 a106, v186\n"
    "v_accvgpr_write_b32 a107, v187\n"
    "v_accvgpr_read_b32 v184, a108\n"
    "v_accvgpr_read_b32 v185, a109\n"
    "v_accvgpr_read_b32 v186, a110\n"
    "v_accvgpr_read_b32 v187, a111\n"
    "v_mul_f32 v184, v184, v245\n"
    "v_mul_f32 v185, v185, v245\n"
    "v_mul_f32 v186, v186, v245\n"
    "v_mul_f32 v187, v187, v245\n"
    "v_accvgpr_write_b32 a108, v184\n"
    "v_accvgpr_write_b32 a109, v185\n"
    "v_accvgpr_write_b32 a110, v186\n"
    "v_accvgpr_write_b32 a111, v187\n"
    "v_accvgpr_read_b32 v184, a112\n"
    "v_accvgpr_read_b32 v185, a113\n"
    "v_accvgpr_read_b32 v186, a114\n"
    "v_accvgpr_read_b32 v187, a115\n"
    "v_mul_f32 v184, v184, v245\n"
    "v_mul_f32 v185, v185, v245\n"
    "v_mul_f32 v186, v186, v245\n"
    "v_mul_f32 v187, v187, v245\n"
    "v_accvgpr_write_b32 a112, v184\n"
    "v_accvgpr_write_b32 a113, v185\n"
    "v_accvgpr_write_b32 a114, v186\n"
    "v_accvgpr_write_b32 a115, v187\n"
    "v_accvgpr_read_b32 v184, a116\n"
    "v_accvgpr_read_b32 v185, a117\n"
    "v_accvgpr_read_b32 v186, a118\n"
    "v_accvgpr_read_b32 v187, a119\n"
    "v_mul_f32 v184, v184, v245\n"
    "v_mul_f32 v185, v185, v245\n"
    "v_mul_f32 v186, v186, v245\n"
    "v_mul_f32 v187, v187, v245\n"
    "v_accvgpr_write_b32 a116, v184\n"
    "v_accvgpr_write_b32 a117, v185\n"
    "v_accvgpr_write_b32 a118, v186\n"
    "v_accvgpr_write_b32 a119, v187\n"
    "v_accvgpr_read_b32 v184, a120\n"
    "v_accvgpr_read_b32 v185, a121\n"
    "v_accvgpr_read_b32 v186, a122\n"
    "v_accvgpr_read_b32 v187, a123\n"
    "v_mul_f32 v184, v184, v245\n"
    "v_mul_f32 v185, v185, v245\n"
    "v_mul_f32 v186, v186, v245\n"
    "v_mul_f32 v187, v187, v245\n"
    "v_accvgpr_write_b32 a120, v184\n"
    "v_accvgpr_write_b32 a121, v185\n"
    "v_accvgpr_write_b32 a122, v186\n"
    "v_accvgpr_write_b32 a123, v187\n"
    "v_accvgpr_read_b32 v184, a124\n"
    "v_accvgpr_read_b32 v185, a125\n"
    "v_accvgpr_read_b32 v186, a126\n"
    "v_accvgpr_read_b32 v187, a127\n"
    "v_mul_f32 v184, v184, v245\n"
    "v_mul_f32 v185, v185, v245\n"
    "v_mul_f32 v186, v186, v245\n"
    "v_mul_f32 v187, v187, v245\n"
    "v_accvgpr_write_b32 a124, v184\n"
    "v_accvgpr_write_b32 a125, v185\n"
    "v_accvgpr_write_b32 a126, v186\n"
    "v_accvgpr_write_b32 a127, v187\n"
    "v_max3_f32 v243, v112, v113, v114\n"
    "v_max3_f32 v243, v243, v115, v124\n"
    "v_max3_f32 v243, v243, v125, v126\n"
    "v_max_f32 v243, v243, v127\n"
    "v_mov_b32 v245, v243\n"
    "s_nop 1\n"
    "v_permlane32_swap_b32 v243, v245\n"
    "v_max_f32 v243, v243, v245\n"
    "v_mov_b32 v245, v243\n"
    "s_nop 1\n"
    "v_permlane16_swap_b32 v243, v245\n"
    "v_max_f32 v243, v243, v245\n"
    "v_mul_f32 v243, s64, v243\n"
    "v_min_f32 v243, v243, v234\n"
    "v_sub_f32 v245, v243, v234\n"
    "v_exp_f32 v245, v245\n"
    "v_mov_b32 v234, v243\n"
    "v_mul_f32 v235, v235, v245\n"
    "v_mul_f32 v0, v0, v245\n"
    "v_mul_f32 v1, v1, v245\n"
    "v_mul_f32 v2, v2, v245\n"
    "v_mul_f32 v3, v3, v245\n"
    "v_mul_f32 v4, v4, v245\n"
    "v_mul_f32 v5, v5, v245\n"
    "v_mul_f32 v6, v6, v245\n"
    "v_mul_f32 v7, v7, v245\n"
    "v_mul_f32 v8, v8, v245\n"
    "v_mul_f32 v9, v9, v245\n"
    "v_mul_f32 v10, v10, v245\n"
    "v_mul_f32 v11, v11, v245\n"
    "v_mul_f32 v12, v12, v245\n"
    "v_mul_f32 v13, v13, v245\n"
    "v_mul_f32 v14, v14, v245\n"
    "v_mul_f32 v15, v15, v245\n"
    "v_mul_f32 v16, v16, v245\n"
    "v_mul_f32 v17, v17, v245\n"
    "v_mul_f32 v18, v18, v245\n"
    "v_mul_f32 v19, v19, v245\n"
    "v_mul_f32 v20, v20, v245\n"
    "v_mul_f32 v21, v21, v245\n"
    "v_mul_f32 v22, v22, v245\n"
    "v_mul_f32 v23, v23, v245\n"
    "v_mul_f32 v24, v24, v245\n"
    "v_mul_f32 v25, v25, v245\n"
    "v_mul_f32 v26, v26, v245\n"
    "v_mul_f32 v27, v27, v245\n"
    "v_mul_f32 v28, v28, v245\n"
    "v_mul_f32 v29, v29, v245\n"
    "v_mul_f32 v30, v30, v245\n"
    "v_mul_f32 v31, v31, v245\n"
    "v_mul_f32 v32, v32, v245\n"
    "v_mul_f32 v33, v33, v245\n"
    "v_mul_f32 v34, v34, v245\n"
    "v_mul_f32 v35, v35, v245\n"
    "v_mul_f32 v36, v36, v245\n"
    "v_mul_f32 v37, v37, v245\n"
    "v_mul_f32 v38, v38, v245\n"
    "v_mul_f32 v39, v39, v245\n"
    "v_mul_f32 v40, v40, v245\n"
    "v_mul_f32 v41, v41, v245\n"
    "v_mul_f32 v42, v42, v245\n"
    "v_mul_f32 v43, v43, v245\n"
    "v_mul_f32 v44, v44, v245\n"
    "v_mul_f32 v45, v45, v245\n"
    "v_mul_f32 v46, v46, v245\n"
    "v_mul_f32 v47, v47, v245\n"
    "v_mul_f32 v48, v48, v245\n"
    "v_mul_f32 v49, v49, v245\n"
    "v_mul_f32 v50, v50, v245\n"
    "v_mul_f32 v51, v51, v245\n"
    "v_mul_f32 v52, v52, v245\n"
    "v_mul_f32 v53, v53, v245\n"
    "v_mul_f32 v54, v54, v245\n"
    "v_mul_f32 v55, v55, v245\n"
    "v_mul_f32 v56, v56, v245\n"
    "v_mul_f32 v57, v57, v245\n"
    "v_mul_f32 v58, v58, v245\n"
    "v_mul_f32 v59, v59, v245\n"
    "v_mul_f32 v60, v60, v245\n"
    "v_mul_f32 v61, v61, v245\n"
    "v_mul_f32 v62, v62, v245\n"
    "v_mul_f32_e64 v63, v63, v245\n"
    "v_fma_f32 v184, v104, s58, v232\n"
    "v_fma_f32 v185, v105, s58, v232\n"
    "v_fma_f32 v186, v106, s58, v232\n"
    "v_fma_f32 v187, v107, s58, v232\n"
    "v_exp_f32_e64 v184, v184\n"
    "v_fma_f32 v188, v116, s58, v232\n"
    "v_exp_f32_e64 v185, v185\n"
    "v_fma_f32 v189, v117, s58, v232\n"
    "v_exp_f32_e64 v186, v186\n"
    "v_fma_f32 v190, v118, s58, v232\n"
    "v_exp_f32_e64 v187, v187\n"
    "v_fma_f32 v191, v119, s58, v232\n"
    "v_exp_f32 v188, v188\n"
    "v_add_f32 v238, v184, v185\n"
    "v_cvt_pk_bf16_f32 v164, v184, v185\n"
    "v_fma_f32 v192, v108, s58, v233\n"
    "v_exp_f32 v189, v189\n"
    "v_add_f32 v238, v238, v186\n"
    "v_fma_f32 v193, v109, s58, v233\n"
    "v_exp_f32 v190, v190\n"
    "v_add_f32 v238, v238, v187\n"
    "v_cvt_pk_bf16_f32 v165, v186, v187\n"
    "v_fma_f32 v194, v110, s58, v233\n"
    "v_exp_f32 v191, v191\n"
    "v_add_f32 v238, v238, v188\n"
    "v_fma_f32 v195, v111, s58, v233\n"
    "v_exp_f32 v192, v192\n"
    "v_add_f32 v238, v238, v189\n"
    "v_cvt_pk_bf16_f32 v166, v188, v189\n"
    "v_fma_f32 v196, v120, s58, v233\n"
    "v_exp_f32 v193, v193\n"
    "v_add_f32 v238, v238, v190\n"
    "v_fma_f32 v197, v121, s58, v233\n"
    "v_exp_f32 v194, v194\n"
    "v_add_f32 v238, v238, v191\n"
    "v_cvt_pk_bf16_f32 v167, v190, v191\n"
    "v_fma_f32 v198, v122, s58, v233\n"
    "v_exp_f32_e64 v195, v195\n"
    "v_fma_f32 v199, v123, s58, v233\n"
    "v_exp_f32 v196, v196\n"
    "v_add_f32 v239, v192, v193\n"
    "v_cvt_pk_bf16_f32 v168, v192, v193\n"
    "v_fma_f32 v184, v112, s58, v234\n"
    "v_exp_f32 v197, v197\n"
    "v_add_f32 v239, v239, v194\n"
    "v_fma_f32 v185, v113, s58, v234\n"
    "v_exp_f32 v198, v198\n"
    "v_add_f32 v239, v239, v195\n"
    "v_cvt_pk_bf16_f32 v169, v194, v195\n"
    "v_fma_f32 v186, v114, s58, v234\n"
    "v_exp_f32 v199, v199\n"
    "v_add_f32 v239, v239, v196\n"
    "v_fma_f32 v187, v115, s58, v234\n"
    "v_exp_f32 v184, v184\n"
    "v_add_f32 v239, v239, v197\n"
    "v_cvt_pk_bf16_f32 v170, v196, v197\n"
    "v_fma_f32 v188, v124, s58, v234\n"
    "v_exp_f32 v185, v185\n"
    "v_add_f32 v239, v239, v198\n"
    "v_fma_f32 v189, v125, s58, v234\n"
    "v_exp_f32 v186, v186\n"
    "v_add_f32 v239, v239, v199\n"
    "v_cvt_pk_bf16_f32 v171, v198, v199\n"
    "v_fma_f32 v190, v126, s58, v234\n"
    "v_exp_f32_e64 v187, v187\n"
    "v_fma_f32 v191, v127, s58, v234\n"
    "v_exp_f32 v188, v188\n"
    "v_add_f32 v240, v184, v185\n"
    "v_cvt_pk_bf16_f32 v172, v184, v185\n"
    "v_fma_f32 v192, v128, s58, v232\n"
    "v_exp_f32 v189, v189\n"
    "v_add_f32 v240, v240, v186\n"
    "v_fma_f32 v193, v129, s58, v232\n"
    "v_exp_f32 v190, v190\n"
    "v_add_f32 v240, v240, v187\n"
    "v_cvt_pk_bf16_f32 v173, v186, v187\n"
    "v_fma_f32 v194, v130, s58, v232\n"
    "v_exp_f32 v191, v191\n"
    "v_add_f32 v240, v240, v188\n"
    "v_fma_f32 v195, v131, s58, v232\n"
    "v_exp_f32 v192, v192\n"
    "v_add_f32 v240, v240, v189\n"
    "v_cvt_pk_bf16_f32 v174, v188, v189\n"
    "v_fma_f32 v196, v136, s58, v232\n"
    "v_exp_f32 v193, v193\n"
    "v_add_f32 v240, v240, v190\n"
    "v_fma_f32 v197, v137, s58, v232\n"
    "v_exp_f32 v194, v194\n"
    "v_add_f32 v240, v240, v191\n"
    "v_cvt_pk_bf16_f32 v175, v190, v191\n"
    "v_fma_f32 v198, v138, s58, v232\n"
    "v_exp_f32 v195, v195\n"
    "v_add_f32 v238, v238, v192\n"
    "v_fma_f32 v199, v139, s58, v232\n"
    "v_exp_f32 v196, v196\n"
    "v_add_f32 v238, v238, v193\n"
    "v_cvt_pk_bf16_f32 v176, v192, v193\n"
    "v_fma_f32 v184, v132, s58, v233\n"
    "v_exp_f32 v197, v197\n"
    "v_add_f32 v238, v238, v194\n"
    "v_fma_f32 v185, v133, s58, v233\n"
    "v_exp_f32 v198, v198\n"
    "v_add_f32 v238, v238, v195\n"
    "v_cvt_pk_bf16_f32 v177, v194, v195\n"
    "v_fma_f32 v186, v134, s58, v233\n"
    "v_exp_f32 v199, v199\n"
    "v_add_f32 v238, v238, v196\n"
    "v_fma_f32 v187, v135, s58, v233\n"
    "v_exp_f32 v184, v184\n"
    "v_add_f32 v238, v238, v197\n"
    "v_cvt_pk_bf16_f32 v178, v196, v197\n"
    "v_fma_f32 v188, v140, s58, v233\n"
    "v_exp_f32 v185, v185\n"
    "v_add_f32 v238, v238, v198\n"
    "v_fma_f32 v189, v141, s58, v233\n"
    "v_exp_f32 v186, v186\n"
    "v_add_f32 v238, v238, v199\n"
    "v_cvt_pk_bf16_f32 v179, v198, v199\n"
    "v_fma_f32 v190, v142, s58, v233\n"
    "v_exp_f32 v187, v187\n"
    "v_add_f32 v239, v239, v184\n"
    "v_fma_f32 v191, v143, s58, v233\n"
    "v_exp_f32 v188, v188\n"
    "v_add_f32 v239, v239, v185\n"
    "v_cvt_pk_bf16_f32 v180, v184, v185\n"
    "v_exp_f32 v189, v189\n"
    "v_add_f32 v239, v239, v186\n"
    "v_exp_f32 v190, v190\n"
    "v_add_f32 v239, v239, v187\n"
    "v_cvt_pk_bf16_f32 v181, v186, v187\n"
    "v_exp_f32 v191, v191\n"
    "v_add_f32 v239, v239, v188\n"
    "v_add_f32_e64 v239, v239, v189\n"
    "v_cvt_pk_bf16_f32 v182, v188, v189\n"
    "v_add_f32_e64 v239, v239, v190\n"
    "v_add_f32 v239, v239, v191\n"
    "v_cvt_pk_bf16_f32 v183, v190, v191\n"
    "s_nop 3\n"
    "s_branch LbackU0_%=\n"
    "LrareUo_%=:\n"
    "s_nop 7\n"
    "v_max3_f32 v243, v64, v65, v66\n"
    "v_max3_f32 v243, v243, v67, v76\n"
    "v_max3_f32 v243, v243, v77, v78\n"
    "v_max3_f32 v243, v243, v79, v88\n"
    "v_max3_f32 v243, v243, v89, v90\n"
    "v_max3_f32 v243, v243, v91, v96\n"
    "v_max3_f32 v243, v243, v97, v98\n"
    "v_max_f32 v243, v243, v99\n"
    "v_mov_b32 v245, v243\n"
    "s_nop 1\n"
    "v_permlane32_swap_b32 v243, v245\n"
    "v_max_f32 v243, v243, v245\n"
    "v_mov_b32 v245, v243\n"
    "s_nop 1\n"
    "v_permlane16_swap_b32 v243, v245\n"
    "v_max_f32 v243, v243, v245\n"
    "v_mul_f32 v243, s64, v243\n"
    "v_min_f32 v243, v243, v232\n"
    "v_sub_f32 v245, v243, v232\n"
    "v_exp_f32 v245, v245\n"
    "v_mov_b32 v232, v243\n"
    "v_mul_f32_e64 v236, v236, v245\n"
    "v_accvgpr_read_b32 v184, a0\n"
    "v_accvgpr_read_b32 v185, a1\n"
    "v_accvgpr_read_b32 v186, a2\n"
    "v_accvgpr_read_b32 v187, a3\n"
    "v_mul_f32 v184, v184, v245\n"
    "v_mul_f32 v185, v185, v245\n"
    "v_mul_f32 v186, v186, v245\n"
    "v_mul_f32 v187, v187, v245\n"
    "v_accvgpr_write_b32 a0, v184\n"
    "v_accvgpr_write_b32 a1, v185\n"
    "v_accvgpr_write_b32 a2, v186\n"
    "v_accvgpr_write_b32 a3, v187\n"
    "v_accvgpr_read_b32 v184, a4\n"
    "v_accvgpr_read_b32 v185, a5\n"
    "v_accvgpr_read_b32 v186, a6\n"
    "v_accvgpr_read_b32 v187, a7\n"
    "v_mul_f32 v184, v184, v245\n"
    "v_mul_f32 v185, v185, v245\n"
    "v_mul_f32 v186, v186, v245\n"
    "v_mul_f32 v187, v187, v245\n"
    "v_accvgpr_write_b32 a4, v184\n"
    "v_accvgpr_write_b32 a5, v185\n"
    "v_accvgpr_write_b32 a6, v186\n"
    "v_accvgpr_write_b32 a7, v187\n"
    "v_accvgpr_read_b32 v184, a8\n"
    "v_accvgpr_read_b32 v185, a9\n"
    "v_accvgpr_read_b32 v186, a10\n"
    "v_accvgpr_read_b32 v187, a11\n"
    "v_mul_f32 v184, v184, v245\n"
    "v_mul_f32 v185, v185, v245\n"
    "v_mul_f32 v186, v186, v245\n"
    "v_mul_f32 v187, v187, v245\n"
    "v_accvgpr_write_b32 a8, v184\n"
    "v_accvgpr_write_b32 a9, v185\n"
    "v_accvgpr_write_b32 a10, v186\n"
    "v_accvgpr_write_b32 a11, v187\n"
    "v_accvgpr_read_b32 v184, a12\n"
    "v_accvgpr_read_b32 v185, a13\n"
    "v_accvgpr_read_b32 v186, a14\n"
    "v_accvgpr_read_b32 v187, a15\n"
    "v_mul_f32 v184, v184, v245\n"
    "v_mul_f32 v185, v185, v245\n"
    "v_mul_f32 v186, v186, v245\n"
    "v_mul_f32 v187, v187, v245\n"
    "v_accvgpr_write_b32 a12, v184\n"
    "v_accvgpr_write_b32 a13, v185\n"
    "v_accvgpr_write_b32 a14, v186\n"
    "v_accvgpr_write_b32 a15, v187\n"
    "v_accvgpr_read_b32 v184, a16\n"
    "v_accvgpr_read_b32 v185, a17\n"
    "v_accvgpr_read_b32 v186, a18\n"
    "v_accvgpr_read_b32 v187, a19\n"
    "v_mul_f32 v184, v184, v245\n"
    "v_mul_f32 v185, v185, v245\n"
    "v_mul_f32 v186, v186, v245\n"
    "v_mul_f32 v187, v187, v245\n"
    "v_accvgpr_write_b32 a16, v184\n"
    "v_accvgpr_write_b32 a17, v185\n"
    "v_accvgpr_write_b32 a18, v186\n"
    "v_accvgpr_write_b32 a19, v187\n"
    "v_accvgpr_read_b32 v184, a20\n"
    "v_accvgpr_read_b32 v185, a21\n"
    "v_accvgpr_read_b32 v186, a22\n"
    "v_accvgpr_read_b32 v187, a23\n"
    "v_mul_f32 v184, v184, v245\n"
    "v_mul_f32 v185, v185, v245\n"
    "v_mul_f32 v186, v186, v245\n"
    "v_mul_f32 v187, v187, v245\n"
    "v_accvgpr_write_b32 a20, v184\n"
    "v_accvgpr_write_b32 a21, v185\n"
    "v_accvgpr_write_b32 a22, v186\n"
    "v_accvgpr_write_b32 a23, v187\n"
    "v_accvgpr_read_b32 v184, a24\n"
    "v_accvgpr_read_b32 v185, a25\n"
    "v_accvgpr_read_b32 v186, a26\n"
    "v_accvgpr_read_b32 v187, a27\n"
    "v_mul_f32 v184, v184, v245\n"
    "v_mul_f32 v185, v185, v245\n"
    "v_mul_f32 v186, v186, v245\n"
    "v_mul_f32 v187, v187, v245\n"
    "v_accvgpr_write_b32 a24, v184\n"
    "v_accvgpr_write_b32 a25, v185\n"
    "v_accvgpr_write_b32 a26, v186\n"
    "v_accvgpr_write_b32 a27, v187\n"
    "v_accvgpr_read_b32 v184, a28\n"
    "v_accvgpr_read_b32 v185, a29\n"
    "v_accvgpr_read_b32 v186, a30\n"
    "v_accvgpr_read_b32 v187, a31\n"
    "v_mul_f32 v184, v184, v245\n"
    "v_mul_f32 v185, v185, v245\n"
    "v_mul_f32 v186, v186, v245\n"
    "v_mul_f32 v187, v187, v245\n"
    "v_accvgpr_write_b32 a28, v184\n"
    "v_accvgpr_write_b32 a29, v185\n"
    "v_accvgpr_write_b32 a30, v186\n"
    "v_accvgpr_write_b32 a31, v187\n"
    "v_accvgpr_read_b32 v184, a32\n"
    "v_accvgpr_read_b32 v185, a33\n"
    "v_accvgpr_read_b32 v186, a34\n"
    "v_accvgpr_read_b32 v187, a35\n"
    "v_mul_f32 v184, v184, v245\n"
    "v_mul_f32 v185, v185, v245\n"
    "v_mul_f32 v186, v186, v245\n"
    "v_mul_f32 v187, v187, v245\n"
    "v_accvgpr_write_b32 a32, v184\n"
    "v_accvgpr_write_b32 a33, v185\n"
    "v_accvgpr_write_b32 a34, v186\n"
    "v_accvgpr_write_b32 a35, v187\n"
    "v_accvgpr_read_b32 v184, a36\n"
    "v_accvgpr_read_b32 v185, a37\n"
    "v_accvgpr_read_b32 v186, a38\n"
    "v_accvgpr_read_b32 v187, a39\n"
    "v_mul_f32 v184, v184, v245\n"
    "v_mul_f32 v185, v185, v245\n"
    "v_mul_f32 v186, v186, v245\n"
    "v_mul_f32 v187, v187, v245\n"
    "v_accvgpr_write_b32 a36, v184\n"
    "v_accvgpr_write_b32 a37, v185\n"
    "v_accvgpr_write_b32 a38, v186\n"
    "v_accvgpr_write_b32 a39, v187\n"
    "v_accvgpr_read_b32 v184, a40\n"
    "v_accvgpr_read_b32 v185, a41\n"
    "v_accvgpr_read_b32 v186, a42\n"
    "v_accvgpr_read_b32 v187, a43\n"
    "v_mul_f32 v184, v184, v245\n"
    "v_mul_f32 v185, v185, v245\n"
    "v_mul_f32 v186, v186, v245\n"
    "v_mul_f32 v187, v187, v245\n"
    "v_accvgpr_write_b32 a40, v184\n"
    "v_accvgpr_write_b32 a41, v185\n"
    "v_accvgpr_write_b32 a42, v186\n"
    "v_accvgpr_write_b32 a43, v187\n"
    "v_accvgpr_read_b32 v184, a44\n"
    "v_accvgpr_read_b32 v185, a45\n"
    "v_accvgpr_read_b32 v186, a46\n"
    "v_accvgpr_read_b32 v187, a47\n"
    "v_mul_f32 v184, v184, v245\n"
    "v_mul_f32 v185, v185, v245\n"
    "v_mul_f32 v186, v186, v245\n"
    "v_mul_f32 v187, v187, v245\n"
    "v_accvgpr_write_b32 a44, v184\n"
    "v_accvgpr_write_b32 a45, v185\n"
    "v_accvgpr_write_b32 a46, v186\n"
    "v_accvgpr_write_b32 a47, v187\n"
    "v_accvgpr_read_b32 v184, a48\n"
    "v_accvgpr_read_b32 v185, a49\n"
    "v_accvgpr_read_b32 v186, a50\n"
    "v_accvgpr_read_b32 v187, a51\n"
    "v_mul_f32 v184, v184, v245\n"
    "v_mul_f32 v185, v185, v245\n"
    "v_mul_f32 v186, v186, v245\n"
    "v_mul_f32 v187, v187, v245\n"
    "v_accvgpr_write_b32 a48, v184\n"
    "v_accvgpr_write_b32 a49, v185\n"
    "v_accvgpr_write_b32 a50, v186\n"
    "v_accvgpr_write_b32 a51, v187\n"
    "v_accvgpr_read_b32 v184, a52\n"
    "v_accvgpr_read_b32 v185, a53\n"
    "v_accvgpr_read_b32 v186, a54\n"
    "v_accvgpr_read_b32 v187, a55\n"
    "v_mul_f32 v184, v184, v245\n"
    "v_mul_f32 v185, v185, v245\n"
    "v_mul_f32 v186, v186, v245\n"
    "v_mul_f32 v187, v187, v245\n"
    "v_accvgpr_write_b32 a52, v184\n"
    "v_accvgpr_write_b32 a53, v185\n"
    "v_accvgpr_write_b32 a54, v186\n"
    "v_accvgpr_write_b32 a55, v187\n"
    "v_accvgpr_read_b32 v184, a56\n"
    "v_accvgpr_read_b32 v185, a57\n"
    "v_accvgpr_read_b32 v186, a58\n"
    "v_accvgpr_read_b32 v187, a59\n"
    "v_mul_f32 v184, v184, v245\n"
    "v_mul_f32 v185, v185, v245\n"
    "v_mul_f32 v186, v186, v245\n"
    "v_mul_f32 v187, v187, v245\n"
    "v_accvgpr_write_b32 a56, v184\n"
    "v_accvgpr_write_b32 a57, v185\n"
    "v_accvgpr_write_b32 a58, v186\n"
    "v_accvgpr_write_b32 a59, v187\n"
    "v_accvgpr_read_b32 v184, a60\n"
    "v_accvgpr_read_b32 v185, a61\n"
    "v_accvgpr_read_b32 v186, a62\n"
    "v_accvgpr_read_b32 v187, a63\n"
    "v_mul_f32 v184, v184, v245\n"
    "v_mul_f32 v185, v185, v245\n"
    "v_mul_f32 v186, v186, v245\n"
    "v_mul_f32 v187, v187, v245\n"
    "v_accvgpr_write_b32 a60, v184\n"
    "v_accvgpr_write_b32 a61, v185\n"
    "v_accvgpr_write_b32 a62, v186\n"
    "v_accvgpr_write_b32 a63, v187\n"
    "v_max3_f32 v243, v68, v69, v70\n"
    "v_max3_f32 v243, v243, v71, v80\n"
    "v_max3_f32 v243, v243, v81, v82\n"
    "v_max3_f32 v243, v243, v83, v92\n"
    "v_max3_f32 v243, v243, v93, v94\n"
    "v_max3_f32 v243, v243, v95, v100\n"
    "v_max3_f32 v243, v243, v101, v102\n"
    "v_max_f32 v243, v243, v103\n"
    "v_mov_b32 v245, v243\n"
    "s_nop 1\n"
    "v_permlane32_swap_b32 v243, v245\n"
    "v_max_f32 v243, v243, v245\n"
    "v_mov_b32 v245, v243\n"
    "s_nop 1\n"
    "v_permlane16_swap_b32 v243, v245\n"
    "v_max_f32 v243, v243, v245\n"
    "v_mul_f32 v243, s64, v243\n"
    "v_min_f32 v243, v243, v233\n"
    "v_sub_f32 v245, v243, v233\n"
    "v_exp_f32 v245, v245\n"
    "v_mov_b32 v233, v243\n"
    "v_mul_f32_e64 v237, v237, v245\n"
    "v_accvgpr_read_b32 v184, a64\n"
    "v_accvgpr_read_b32 v185, a65\n"
    "v_accvgpr_read_b32 v186, a66\n"
    "v_accvgpr_read_b32 v187, a67\n"
    "v_mul_f32 v184, v184, v245\n"
    "v_mul_f32 v185, v185, v245\n"
    "v_mul_f32 v186, v186, v245\n"
    "v_mul_f32 v187, v187, v245\n"
    "v_accvgpr_write_b32 a64, v184\n"
    "v_accvgpr_write_b32 a65, v185\n"
    "v_accvgpr_write_b32 a66, v186\n"
    "v_accvgpr_write_b32 a67, v187\n"
    "v_accvgpr_read_b32 v184, a68\n"
    "v_accvgpr_read_b32 v185, a69\n"
    "v_accvgpr_read_b32 v186, a70\n"
    "v_accvgpr_read_b32 v187, a71\n"
    "v_mul_f32 v184, v184, v245\n"
    "v_mul_f32 v185, v185, v245\n"
    "v_mul_f32 v186, v186, v245\n"
    "v_mul_f32 v187, v187, v245\n"
    "v_accvgpr_write_b32 a68, v184\n"
    "v_accvgpr_write_b32 a69, v185\n"
    "v_accvgpr_write_b32 a70, v186\n"
    "v_accvgpr_write_b32 a71, v187\n"
    "v_accvgpr_read_b32 v184, a72\n"
    "v_accvgpr_read_b32 v185, a73\n"
    "v_accvgpr_read_b32 v186, a74\n"
    "v_accvgpr_read_b32 v187, a75\n"
    "v_mul_f32 v184, v184, v245\n"
    "v_mul_f32 v185, v185, v245\n"
    "v_mul_f32 v186, v186, v245\n"
    "v_mul_f32 v187, v187, v245\n"
    "v_accvgpr_write_b32 a72, v184\n"
    "v_accvgpr_write_b32 a73, v185\n"
    "v_accvgpr_write_b32 a74, v186\n"
    "v_accvgpr_write_b32 a75, v187\n"
    "v_accvgpr_read_b32 v184, a76\n"
    "v_accvgpr_read_b32 v185, a77\n"
    "v_accvgpr_read_b32 v186, a78\n"
    "v_accvgpr_read_b32 v187, a79\n"
    "v_mul_f32 v184, v184, v245\n"
    "v_mul_f32 v185, v185, v245\n"
    "v_mul_f32 v186, v186, v245\n"
    "v_mul_f32 v187, v187, v245\n"
    "v_accvgpr_write_b32 a76, v184\n"
    "v_accvgpr_write_b32 a77, v185\n"
    "v_accvgpr_write_b32 a78, v186\n"
    "v_accvgpr_write_b32 a79, v187\n"
    "v_accvgpr_read_b32 v184, a80\n"
    "v_accvgpr_read_b32 v185, a81\n"
    "v_accvgpr_read_b32 v186, a82\n"
    "v_accvgpr_read_b32 v187, a83\n"
    "v_mul_f32 v184, v184, v245\n"
    "v_mul_f32 v185, v185, v245\n"
    "v_mul_f32 v186, v186, v245\n"
    "v_mul_f32 v187, v187, v245\n"
    "v_accvgpr_write_b32 a80, v184\n"
    "v_accvgpr_write_b32 a81, v185\n"
    "v_accvgpr_write_b32 a82, v186\n"
    "v_accvgpr_write_b32 a83, v187\n"
    "v_accvgpr_read_b32 v184, a84\n"
    "v_accvgpr_read_b32 v185, a85\n"
    "v_accvgpr_read_b32 v186, a86\n"
    "v_accvgpr_read_b32 v187, a87\n"
    "v_mul_f32 v184, v184, v245\n"
    "v_mul_f32 v185, v185, v245\n"
    "v_mul_f32 v186, v186, v245\n"
    "v_mul_f32 v187, v187, v245\n"
    "v_accvgpr_write_b32 a84, v184\n"
    "v_accvgpr_write_b32 a85, v185\n"
    "v_accvgpr_write_b32 a86, v186\n"
    "v_accvgpr_write_b32 a87, v187\n"
    "v_accvgpr_read_b32 v184, a88\n"
    "v_accvgpr_read_b32 v185, a89\n"
    "v_accvgpr_read_b32 v186, a90\n"
    "v_accvgpr_read_b32 v187, a91\n"
    "v_mul_f32 v184, v184, v245\n"
    "v_mul_f32 v185, v185, v245\n"
    "v_mul_f32 v186, v186, v245\n"
    "v_mul_f32 v187, v187, v245\n"
    "v_accvgpr_write_b32 a88, v184\n"
    "v_accvgpr_write_b32 a89, v185\n"
    "v_accvgpr_write_b32 a90, v186\n"
    "v_accvgpr_write_b32 a91, v187\n"
    "v_accvgpr_read_b32 v184, a92\n"
    "v_accvgpr_read_b32 v185, a93\n"
    "v_accvgpr_read_b32 v186, a94\n"
    "v_accvgpr_read_b32 v187, a95\n"
    "v_mul_f32 v184, v184, v245\n"
    "v_mul_f32 v185, v185, v245\n"
    "v_mul_f32 v186, v186, v245\n"
    "v_mul_f32 v187, v187, v245\n"
    "v_accvgpr_write_b32 a92, v184\n"
    "v_accvgpr_write_b32 a93, v185\n"
    "v_accvgpr_write_b32 a94, v186\n"
    "v_accvgpr_write_b32 a95, v187\n"
    "v_accvgpr_read_b32 v184, a96\n"
    "v_accvgpr_read_b32 v185, a97\n"
    "v_accvgpr_read_b32 v186, a98\n"
    "v_accvgpr_read_b32 v187, a99\n"
    "v_mul_f32 v184, v184, v245\n"
    "v_mul_f32 v185, v185, v245\n"
    "v_mul_f32 v186, v186, v245\n"
    "v_mul_f32 v187, v187, v245\n"
    "v_accvgpr_write_b32 a96, v184\n"
    "v_accvgpr_write_b32 a97, v185\n"
    "v_accvgpr_write_b32 a98, v186\n"
    "v_accvgpr_write_b32 a99, v187\n"
    "v_accvgpr_read_b32 v184, a100\n"
    "v_accvgpr_read_b32 v185, a101\n"
    "v_accvgpr_read_b32 v186, a102\n"
    "v_accvgpr_read_b32 v187, a103\n"
    "v_mul_f32 v184, v184, v245\n"
    "v_mul_f32 v185, v185, v245\n"
    "v_mul_f32 v186, v186, v245\n"
    "v_mul_f32 v187, v187, v245\n"
    "v_accvgpr_write_b32 a100, v184\n"
    "v_accvgpr_write_b32 a101, v185\n"
    "v_accvgpr_write_b32 a102, v186\n"
    "v_accvgpr_write_b32 a103, v187\n"
    "v_accvgpr_read_b32 v184, a104\n"
    "v_accvgpr_read_b32 v185, a105\n"
    "v_accvgpr_read_b32 v186, a106\n"
    "v_accvgpr_read_b32 v187, a107\n"
    "v_mul_f32 v184, v184, v245\n"
    "v_mul_f32 v185, v185, v245\n"
    "v_mul_f32 v186, v186, v245\n"
    "v_mul_f32 v187, v187, v245\n"
    "v_accvgpr_write_b32 a104, v184\n"
    "v_accvgpr_write_b32 a105, v185\n"
    "v_accvgpr_write_b32 a106, v186\n"
    "v_accvgpr_write_b32 a107, v187\n"
    "v_accvgpr_read_b32 v184, a108\n"
    "v_accvgpr_read_b32 v185, a109\n"
    "v_accvgpr_read_b32 v186, a110\n"
    "v_accvgpr_read_b32 v187, a111\n"
    "v_mul_f32 v184, v184, v245\n"
    "v_mul_f32 v185, v185, v245\n"
    "v_mul_f32 v186, v186, v245\n"
    "v_mul_f32 v187, v187, v245\n"
    "v_accvgpr_write_b32 a108, v184\n"
    "v_accvgpr_write_b32 a109, v185\n"
    "v_accvgpr_write_b32 a110, v186\n"
    "v_accvgpr_write_b32 a111, v187\n"
    "v_accvgpr_read_b32 v184, a112\n"
    "v_accvgpr_read_b32 v185, a113\n"
    "v_accvgpr_read_b32 v186, a114\n"
    "v_accvgpr_read_b32 v187, a115\n"
    "v_mul_f32 v184, v184, v245\n"
    "v_mul_f32 v185, v185, v245\n"
    "v_mul_f32 v186, v186, v245\n"
    "v_mul_f32 v187, v187, v245\n"
    "v_accvgpr_write_b32 a112, v184\n"
    "v_accvgpr_write_b32 a113, v185\n"
    "v_accvgpr_write_b32 a114, v186\n"
    "v_accvgpr_write_b32 a115, v187\n"
    "v_accvgpr_read_b32 v184, a116\n"
    "v_accvgpr_read_b32 v185, a117\n"
    "v_accvgpr_read_b32 v186, a118\n"
    "v_accvgpr_read_b32 v187, a119\n"
    "v_mul_f32 v184, v184, v245\n"
    "v_mul_f32 v185, v185, v245\n"
    "v_mul_f32 v186, v186, v245\n"
    "v_mul_f32 v187, v187, v245\n"
    "v_accvgpr_write_b32 a116, v184\n"
    "v_accvgpr_write_b32 a117, v185\n"
    "v_accvgpr_write_b32 a118, v186\n"
    "v_accvgpr_write_b32 a119, v187\n"
    "v_accvgpr_read_b32 v184, a120\n"
    "v_accvgpr_read_b32 v185, a121\n"
    "v_accvgpr_read_b32 v186, a122\n"
    "v_accvgpr_read_b32 v187, a123\n"
    "v_mul_f32 v184, v184, v245\n"
    "v_mul_f32 v185, v185, v245\n"
    "v_mul_f32 v186, v186, v245\n"
    "v_mul_f32 v187, v187, v245\n"
    "v_accvgpr_write_b32 a120, v184\n"
    "v_accvgpr_write_b32 a121, v185\n"
    "v_accvgpr_write_b32 a122, v186\n"
    "v_accvgpr_write_b32 a123, v187\n"
    "v_accvgpr_read_b32 v184, a124\n"
    "v_accvgpr_read_b32 v185, a125\n"
    "v_accvgpr_read_b32 v186, a126\n"
    "v_accvgpr_read_b32 v187, a127\n"
    "v_mul_f32 v184, v184, v245\n"
    "v_mul_f32 v185, v185, v245\n"
    "v_mul_f32 v186, v186, v245\n"
    "v_mul_f32 v187, v187, v245\n"
    "v_accvgpr_write_b32 a124, v184\n"
    "v_accvgpr_write_b32 a125, v185\n"
    "v_accvgpr_write_b32 a126, v186\n"
    "v_accvgpr_write_b32 a127, v187\n"
    "v_max3_f32 v243, v72, v73, v74\n"
    "v_max3_f32 v243, v243, v75, v84\n"
    "v_max3_f32 v243, v243, v85, v86\n"
    "v_max_f32 v243, v243, v87\n"
    "v_mov_b32 v245, v243\n"
    "s_nop 1\n"
    "v_permlane32_swap_b32 v243, v245\n"
    "v_max_f32 v243, v243, v245\n"
    "v_mov_b32 v245, v243\n"
    "s_nop 1\n"
    "v_permlane16_swap_b32 v243, v245\n"
    "v_max_f32 v243, v243, v245\n"
    "v_mul_f32 v243, s64, v243\n"
    "v_min_f32 v243, v243, v234\n"
    "v_sub_f32 v245, v243, v234\n"
    "v_exp_f32 v245, v245\n"
    "v_mov_b32 v234, v243\n"
    "v_mul_f32 v235, v235, v245\n"
    "v_mul_f32 v0, v0, v245\n"
    "v_mul_f32 v1, v1, v245\n"
    "v_mul_f32 v2, v2, v245\n"
    "v_mul_f32 v3, v3, v245\n"
    "v_mul_f32 v4, v4, v245\n"
    "v_mul_f32 v5, v5, v245\n"
    "v_mul_f32 v6, v6, v245\n"
    "v_mul_f32 v7, v7, v245\n"
    "v_mul_f32 v8, v8, v245\n"
    "v_mul_f32 v9, v9, v245\n"
    "v_mul_f32 v10, v10, v245\n"
    "v_mul_f32 v11, v11, v245\n"
    "v_mul_f32 v12, v12, v245\n"
    "v_mul_f32 v13, v13, v245\n"
    "v_mul_f32 v14, v14, v245\n"
    "v_mul_f32 v15, v15, v245\n"
    "v_mul_f32 v16, v16, v245\n"
    "v_mul_f32 v17, v17, v245\n"
    "v_mul_f32 v18, v18, v245\n"
    "v_mul_f32 v19, v19, v245\n"
    "v_mul_f32 v20, v20, v245\n"
    "v_mul_f32 v21, v21, v245\n"
    "v_mul_f32 v22, v22, v245\n"
    "v_mul_f32 v23, v23, v245\n"
    "v_mul_f32 v24, v24, v245\n"
    "v_mul_f32 v25, v25, v245\n"
    "v_mul_f32 v26, v26, v245\n"
    "v_mul_f32 v27, v27, v245\n"
    "v_mul_f32 v28, v28, v245\n"
    "v_mul_f32 v29, v29, v245\n"
    "v_mul_f32 v30, v30, v245\n"
    "v_mul_f32 v31, v31, v245\n"
    "v_mul_f32 v32, v32, v245\n"
    "v_mul_f32 v33, v33, v245\n"
    "v_mul_f32 v34, v34, v245\n"
    "v_mul_f32 v35, v35, v245\n"
    "v_mul_f32 v36, v36, v245\n"
    "v_mul_f32 v37, v37, v245\n"
    "v_mul_f32 v38, v38, v245\n"
    "v_mul_f32 v39, v39, v245\n"
    "v_mul_f32 v40, v40, v245\n"
    "v_mul_f32 v41, v41, v245\n"
    "v_mul_f32 v42, v42, v245\n"
    "v_mul_f32 v43, v43, v245\n"
    "v_mul_f32 v44, v44, v245\n"
    "v_mul_f32 v45, v45, v245\n"
    "v_mul_f32 v46, v46, v245\n"
    "v_mul_f32 v47, v47, v245\n"
    "v_mul_f32 v48, v48, v245\n"
    "v_mul_f32 v49, v49, v245\n"
    "v_mul_f32 v50, v50, v245\n"
    "v_mul_f32 v51, v51, v245\n"
    "v_mul_f32 v52, v52, v245\n"
    "v_mul_f32 v53, v53, v245\n"
    "v_mul_f32 v54, v54, v245\n"
    "v_mul_f32 v55, v55, v245\n"
    "v_mul_f32 v56, v56, v245\n"
    "v_mul_f32 v57, v57, v245\n"
    "v_mul_f32 v58, v58, v245\n"
    "v_mul_f32 v59, v59, v245\n"
    "v_mul_f32 v60, v60, v245\n"
    "v_mul_f32 v61, v61, v245\n"
    "v_mul_f32 v62, v62, v245\n"
    "v_mul_f32_e64 v63, v63, v245\n"
    "v_fma_f32 v184, v64, s58, v232\n"
    "v_fma_f32 v185, v65, s58, v232\n"
    "v_fma_f32 v186, v66, s58, v232\n"
    "v_fma_f32 v187, v67, s58, v232\n"
    "v_exp_f32_e64 v184, v184\n"
    "v_fma_f32 v188, v76, s58, v232\n"
    "v_exp_f32_e64 v185, v185\n"
    "v_fma_f32 v189, v77, s58, v232\n"
    "v_exp_f32_e64 v186, v186\n"
    "v_fma_f32 v190, v78, s58, v232\n"
    "v_exp_f32_e64 v187, v187\n"
    "v_fma_f32 v191, v79, s58, v232\n"
    "v_exp_f32 v188, v188\n"
    "v_add_f32 v238, v184, v185\n"
    "v_cvt_pk_bf16_f32 v144, v184, v185\n"
    "v_fma_f32 v192, v68, s58, v233\n"
    "v_exp_f32 v189, v189\n"
    "v_add_f32 v238, v238, v186\n"
    "v_fma_f32 v193, v69, s58, v233\n"
    "v_exp_f32 v190, v190\n"
    "v_add_f32 v238, v238, v187\n"
    "v_cvt_pk_bf16_f32 v145, v186, v187\n"
    "v_fma_f32 v194, v70, s58, v233\n"
    "v_exp_f32 v191, v191\n"
    "v_add_f32 v238, v238, v188\n"
    "v_fma_f32 v195, v71, s58, v233\n"
    "v_exp_f32 v192, v192\n"
    "v_add_f32 v238, v238, v189\n"
    "v_cvt_pk_bf16_f32 v146, v188, v189\n"
    "v_fma_f32 v196, v80, s58, v233\n"
    "v_exp_f32 v193, v193\n"
    "v_add_f32 v238, v238, v190\n"
    "v_fma_f32 v197, v81, s58, v233\n"
    "v_exp_f32 v194, v194\n"
    "v_add_f32 v238, v238, v191\n"
    "v_cvt_pk_bf16_f32 v147, v190, v191\n"
    "v_fma_f32 v198, v82, s58, v233\n"
    "v_exp_f32_e64 v195, v195\n"
    "v_fma_f32 v199, v83, s58, v233\n"
    "v_exp_f32 v196, v196\n"
    "v_add_f32 v239, v192, v193\n"
    "v_cvt_pk_bf16_f32 v148, v192, v193\n"
    "v_fma_f32 v184, v72, s58, v234\n"
    "v_exp_f32 v197, v197\n"
    "v_add_f32 v239, v239, v194\n"
    "v_fma_f32 v185, v73, s58, v234\n"
    "v_exp_f32 v198, v198\n"
    "v_add_f32 v239, v239, v195\n"
    "v_cvt_pk_bf16_f32 v149, v194, v195\n"
    "v_fma_f32 v186, v74, s58, v234\n"
    "v_exp_f32 v199, v199\n"
    "v_add_f32 v239, v239, v196\n"
    "v_fma_f32 v187, v75, s58, v234\n"
    "v_exp_f32 v184, v184\n"
    "v_add_f32 v239, v239, v197\n"
    "v_cvt_pk_bf16_f32 v150, v196, v197\n"
    "v_fma_f32 v188, v84, s58, v234\n"
    "v_exp_f32 v185, v185\n"
    "v_add_f32 v239, v239, v198\n"
    "v_fma_f32 v189, v85, s58, v234\n"
    "v_exp_f32 v186, v186\n"
    "v_add_f32 v239, v239, v199\n"
    "v_cvt_pk_bf16_f32 v151, v198, v199\n"
    "v_fma_f32 v190, v86, s58, v234\n"
    "v_exp_f32_e64 v187, v187\n"
    "v_fma_f32 v191, v87, s58, v234\n"
    "v_exp_f32 v188, v188\n"
    "v_add_f32 v240, v184, v185\n"
    "v_cvt_pk_bf16_f32 v152, v184, v185\n"
    "v_fma_f32 v192, v88, s58, v232\n"
    "v_exp_f32 v189, v189\n"
    "v_add_f32 v240, v240, v186\n"
    "v_fma_f32 v193, v89, s58, v232\n"
    "v_exp_f32 v190, v190\n"
    "v_add_f32 v240, v240, v187\n"
    "v_cvt_pk_bf16_f32 v153, v186, v187\n"
    "v_fma_f32 v194, v90, s58, v232\n"
    "v_exp_f32 v191, v191\n"
    "v_add_f32 v240, v240, v188\n"
    "v_fma_f32 v195, v91, s58, v232\n"
    "v_exp_f32 v192, v192\n"
    "v_add_f32 v240, v240, v189\n"
    "v_cvt_pk_bf16_f32 v154, v188, v189\n"
    "v_fma_f32 v196, v96, s58, v232\n"
    "v_exp_f32 v193, v193\n"
    "v_add_f32 v240, v240, v190\n"
    "v_fma_f32 v197, v97, s58, v232\n"
    "v_exp_f32 v194, v194\n"
    "v_add_f32 v240, v240, v191\n"
    "v_cvt_pk_bf16_f32 v155, v190, v191\n"
    "v_fma_f32 v198, v98, s58, v232\n"
    "v_exp_f32 v195, v195\n"
    "v_add_f32 v238, v238, v192\n"
    "v_fma_f32 v199, v99, s58, v232\n"
    "v_exp_f32 v196, v196\n"
    "v_add_f32 v238, v238, v193\n"
    "v_cvt_pk_bf16_f32 v156, v192, v193\n"
    "v_fma_f32 v184, v92, s58, v233\n"
    "v_exp_f32 v197, v197\n"
    "v_add_f32 v238, v238, v194\n"
    "v_fma_f32 v185, v93, s58, v233\n"
    "v_exp_f32 v198, v198\n"
    "v_add_f32 v238, v238, v195\n"
    "v_cvt_pk_bf16_f32 v157, v194, v195\n"
    "v_fma_f32 v186, v94, s58, v233\n"
    "v_exp_f32 v199, v199\n"
    "v_add_f32 v238, v238, v196\n"
    "v_fma_f32 v187, v95, s58, v233\n"
    "v_exp_f32 v184, v184\n"
    "v_add_f32 v238, v238, v197\n"
    "v_cvt_pk_bf16_f32 v158, v196, v197\n"
    "v_fma_f32 v188, v100, s58, v233\n"
    "v_exp_f32 v185, v185\n"
    "v_add_f32 v238, v238, v198\n"
    "v_fma_f32 v189, v101, s58, v233\n"
    "v_exp_f32 v186, v186\n"
    "v_add_f32 v238, v238, v199\n"
    "v_cvt_pk_bf16_f32 v159, v198, v199\n"
    "v_fma_f32 v190, v102, s58, v233\n"
    "v_exp_f32 v187, v187\n"
    "v_add_f32 v239, v239, v184\n"
    "v_fma_f32 v191, v103, s58, v233\n"
    "v_exp_f32 v188, v188\n"
    "v_add_f32 v239, v239, v185\n"
    "v_cvt_pk_bf16_f32 v160, v184, v185\n"
    "v_exp_f32 v189, v189\n"
    "v_add_f32 v239, v239, v186\n"
    "v_exp_f32 v190, v190\n"
    "v_add_f32 v239, v239, v187\n"
    "v_cvt_pk_bf16_f32 v161, v186, v187\n"
    "v_exp_f32 v191, v191\n"
    "v_add_f32 v239, v239, v188\n"
    "v_add_f32_e64 v239, v239, v189\n"
    "v_cvt_pk_bf16_f32 v162, v188, v189\n"
    "v_add_f32_e64 v239, v239, v190\n"
    "v_add_f32 v239, v239, v191\n"
    "v_cvt_pk_bf16_f32 v163, v190, v191\n"
    "s_nop 3\n"
    "s_branch LbackUo_%=\n"
    "LrareUe_%=:\n"
    "s_nop 7\n"
    "v_max3_f32 v243, v104, v105, v106\n"
    "v_max3_f32 v243, v243, v107, v116\n"
    "v_max3_f32 v243, v243, v117, v118\n"
    "v_max3_f32 v243, v243, v119, v128\n"
    "v_max3_f32 v243, v243, v129, v130\n"
    "v_max3_f32 v243, v243, v131, v136\n"
    "v_max3_f32 v243, v243, v137, v138\n"
    "v_max_f32 v243, v243, v139\n"
    "v_mov_b32 v245, v243\n"
    "s_nop 1\n"
    "v_permlane32_swap_b32 v243, v245\n"
    "v_max_f32 v243, v243, v245\n"
    "v_mov_b32 v245, v243\n"
    "s_nop 1\n"
    "v_permlane16_swap_b32 v243, v245\n"
    "v_max_f32 v243, v243, v245\n"
    "v_mul_f32 v243, s64, v243\n"
    "v_min_f32 v243, v243, v232\n"
    "v_sub_f32 v245, v243, v232\n"
    "v_exp_f32 v245, v245\n"
    "v_mov_b32 v232, v243\n"
    "v_mul_f32_e64 v236, v236, v245\n"
    "v_accvgpr_read_b32 v184, a0\n"
    "v_accvgpr_read_b32 v185, a1\n"
    "v_accvgpr_read_b32 v186, a2\n"
    "v_accvgpr_read_b32 v187, a3\n"
    "v_mul_f32 v184, v184, v245\n"
    "v_mul_f32 v185, v185, v245\n"
    "v_mul_f32 v186, v186, v245\n"
    "v_mul_f32 v187, v187, v245\n"
    "v_accvgpr_write_b32 a0, v184\n"
    "v_accvgpr_write_b32 a1, v185\n"
    "v_accvgpr_write_b32 a2, v186\n"
    "v_accvgpr_write_b32 a3, v187\n"
    "v_accvgpr_read_b32 v184, a4\n"
    "v_accvgpr_read_b32 v185, a5\n"
    "v_accvgpr_read_b32 v186, a6\n"
    "v_accvgpr_read_b32 v187, a7\n"
    "v_mul_f32 v184, v184, v245\n"
    "v_mul_f32 v185, v185, v245\n"
    "v_mul_f32 v186, v186, v245\n"
    "v_mul_f32 v187, v187, v245\n"
    "v_accvgpr_write_b32 a4, v184\n"
    "v_accvgpr_write_b32 a5, v185\n"
    "v_accvgpr_write_b32 a6, v186\n"
    "v_accvgpr_write_b32 a7, v187\n"
    "v_accvgpr_read_b32 v184, a8\n"
    "v_accvgpr_read_b32 v185, a9\n"
    "v_accvgpr_read_b32 v186, a10\n"
    "v_accvgpr_read_b32 v187, a11\n"
    "v_mul_f32 v184, v184, v245\n"
    "v_mul_f32 v185, v185, v245\n"
    "v_mul_f32 v186, v186, v245\n"
    "v_mul_f32 v187, v187, v245\n"
    "v_accvgpr_write_b32 a8, v184\n"
    "v_accvgpr_write_b32 a9, v185\n"
    "v_accvgpr_write_b32 a10, v186\n"
    "v_accvgpr_write_b32 a11, v187\n"
    "v_accvgpr_read_b32 v184, a12\n"
    "v_accvgpr_read_b32 v185, a13\n"
    "v_accvgpr_read_b32 v186, a14\n"
    "v_accvgpr_read_b32 v187, a15\n"
    "v_mul_f32 v184, v184, v245\n"
    "v_mul_f32 v185, v185, v245\n"
    "v_mul_f32 v186, v186, v245\n"
    "v_mul_f32 v187, v187, v245\n"
    "v_accvgpr_write_b32 a12, v184\n"
    "v_accvgpr_write_b32 a13, v185\n"
    "v_accvgpr_write_b32 a14, v186\n"
    "v_accvgpr_write_b32 a15, v187\n"
    "v_accvgpr_read_b32 v184, a16\n"
    "v_accvgpr_read_b32 v185, a17\n"
    "v_accvgpr_read_b32 v186, a18\n"
    "v_accvgpr_read_b32 v187, a19\n"
    "v_mul_f32 v184, v184, v245\n"
    "v_mul_f32 v185, v185, v245\n"
    "v_mul_f32 v186, v186, v245\n"
    "v_mul_f32 v187, v187, v245\n"
    "v_accvgpr_write_b32 a16, v184\n"
    "v_accvgpr_write_b32 a17, v185\n"
    "v_accvgpr_write_b32 a18, v186\n"
    "v_accvgpr_write_b32 a19, v187\n"
    "v_accvgpr_read_b32 v184, a20\n"
    "v_accvgpr_read_b32 v185, a21\n"
    "v_accvgpr_read_b32 v186, a22\n"
    "v_accvgpr_read_b32 v187, a23\n"
    "v_mul_f32 v184, v184, v245\n"
    "v_mul_f32 v185, v185, v245\n"
    "v_mul_f32 v186, v186, v245\n"
    "v_mul_f32 v187, v187, v245\n"
    "v_accvgpr_write_b32 a20, v184\n"
    "v_accvgpr_write_b32 a21, v185\n"
    "v_accvgpr_write_b32 a22, v186\n"
    "v_accvgpr_write_b32 a23, v187\n"
    "v_accvgpr_read_b32 v184, a24\n"
    "v_accvgpr_read_b32 v185, a25\n"
    "v_accvgpr_read_b32 v186, a26\n"
    "v_accvgpr_read_b32 v187, a27\n"
    "v_mul_f32 v184, v184, v245\n"
    "v_mul_f32 v185, v185, v245\n"
    "v_mul_f32 v186, v186, v245\n"
    "v_mul_f32 v187, v187, v245\n"
    "v_accvgpr_write_b32 a24, v184\n"
    "v_accvgpr_write_b32 a25, v185\n"
    "v_accvgpr_write_b32 a26, v186\n"
    "v_accvgpr_write_b32 a27, v187\n"
    "v_accvgpr_read_b32 v184, a28\n"
    "v_accvgpr_read_b32 v185, a29\n"
    "v_accvgpr_read_b32 v186, a30\n"
    "v_accvgpr_read_b32 v187, a31\n"
    "v_mul_f32 v184, v184, v245\n"
    "v_mul_f32 v185, v185, v245\n"
    "v_mul_f32 v186, v186, v245\n"
    "v_mul_f32 v187, v187, v245\n"
    "v_accvgpr_write_b32 a28, v184\n"
    "v_accvgpr_write_b32 a29, v185\n"
    "v_accvgpr_write_b32 a30, v186\n"
    "v_accvgpr_write_b32 a31, v187\n"
    "v_accvgpr_read_b32 v184, a32\n"
    "v_accvgpr_read_b32 v185, a33\n"
    "v_accvgpr_read_b32 v186, a34\n"
    "v_accvgpr_read_b32 v187, a35\n"
    "v_mul_f32 v184, v184, v245\n"
    "v_mul_f32 v185, v185, v245\n"
    "v_mul_f32 v186, v186, v245\n"
    "v_mul_f32 v187, v187, v245\n"
    "v_accvgpr_write_b32 a32, v184\n"
    "v_accvgpr_write_b32 a33, v185\n"
    "v_accvgpr_write_b32 a34, v186\n"
    "v_accvgpr_write_b32 a35, v187\n"
    "v_accvgpr_read_b32 v184, a36\n"
    "v_accvgpr_read_b32 v185, a37\n"
    "v_accvgpr_read_b32 v186, a38\n"
    "v_accvgpr_read_b32 v187, a39\n"
    "v_mul_f32 v184, v184, v245\n"
    "v_mul_f32 v185, v185, v245\n"
    "v_mul_f32 v186, v186, v245\n"
    "v_mul_f32 v187, v187, v245\n"
    "v_accvgpr_write_b32 a36, v184\n"
    "v_accvgpr_write_b32 a37, v185\n"
    "v_accvgpr_write_b32 a38, v186\n"
    "v_accvgpr_write_b32 a39, v187\n"
    "v_accvgpr_read_b32 v184, a40\n"
    "v_accvgpr_read_b32 v185, a41\n"
    "v_accvgpr_read_b32 v186, a42\n"
    "v_accvgpr_read_b32 v187, a43\n"
    "v_mul_f32 v184, v184, v245\n"
    "v_mul_f32 v185, v185, v245\n"
    "v_mul_f32 v186, v186, v245\n"
    "v_mul_f32 v187, v187, v245\n"
    "v_accvgpr_write_b32 a40, v184\n"
    "v_accvgpr_write_b32 a41, v185\n"
    "v_accvgpr_write_b32 a42, v186\n"
    "v_accvgpr_write_b32 a43, v187\n"
    "v_accvgpr_read_b32 v184, a44\n"
    "v_accvgpr_read_b32 v185, a45\n"
    "v_accvgpr_read_b32 v186, a46\n"
    "v_accvgpr_read_b32 v187, a47\n"
    "v_mul_f32 v184, v184, v245\n"
    "v_mul_f32 v185, v185, v245\n"
    "v_mul_f32 v186, v186, v245\n"
    "v_mul_f32 v187, v187, v245\n"
    "v_accvgpr_write_b32 a44, v184\n"
    "v_accvgpr_write_b32 a45, v185\n"
    "v_accvgpr_write_b32 a46, v186\n"
    "v_accvgpr_write_b32 a47, v187\n"
    "v_accvgpr_read_b32 v184, a48\n"
    "v_accvgpr_read_b32 v185, a49\n"
    "v_accvgpr_read_b32 v186, a50\n"
    "v_accvgpr_read_b32 v187, a51\n"
    "v_mul_f32 v184, v184, v245\n"
    "v_mul_f32 v185, v185, v245\n"
    "v_mul_f32 v186, v186, v245\n"
    "v_mul_f32 v187, v187, v245\n"
    "v_accvgpr_write_b32 a48, v184\n"
    "v_accvgpr_write_b32 a49, v185\n"
    "v_accvgpr_write_b32 a50, v186\n"
    "v_accvgpr_write_b32 a51, v187\n"
    "v_accvgpr_read_b32 v184, a52\n"
    "v_accvgpr_read_b32 v185, a53\n"
    "v_accvgpr_read_b32 v186, a54\n"
    "v_accvgpr_read_b32 v187, a55\n"
    "v_mul_f32 v184, v184, v245\n"
    "v_mul_f32 v185, v185, v245\n"
    "v_mul_f32 v186, v186, v245\n"
    "v_mul_f32 v187, v187, v245\n"
    "v_accvgpr_write_b32 a52, v184\n"
    "v_accvgpr_write_b32 a53, v185\n"
    "v_accvgpr_write_b32 a54, v186\n"
    "v_accvgpr_write_b32 a55, v187\n"
    "v_accvgpr_read_b32 v184, a56\n"
    "v_accvgpr_read_b32 v185, a57\n"
    "v_accvgpr_read_b32 v186, a58\n"
    "v_accvgpr_read_b32 v187, a59\n"
    "v_mul_f32 v184, v184, v245\n"
    "v_mul_f32 v185, v185, v245\n"
    "v_mul_f32 v186, v186, v245\n"
    "v_mul_f32 v187, v187, v245\n"
    "v_accvgpr_write_b32 a56, v184\n"
    "v_accvgpr_write_b32 a57, v185\n"
    "v_accvgpr_write_b32 a58, v186\n"
    "v_accvgpr_write_b32 a59, v187\n"
    "v_accvgpr_read_b32 v184, a60\n"
    "v_accvgpr_read_b32 v185, a61\n"
    "v_accvgpr_read_b32 v186, a62\n"
    "v_accvgpr_read_b32 v187, a63\n"
    "v_mul_f32 v184, v184, v245\n"
    "v_mul_f32 v185, v185, v245\n"
    "v_mul_f32 v186, v186, v245\n"
    "v_mul_f32 v187, v187, v245\n"
    "v_accvgpr_write_b32 a60, v184\n"
    "v_accvgpr_write_b32 a61, v185\n"
    "v_accvgpr_write_b32 a62, v186\n"
    "v_accvgpr_write_b32 a63, v187\n"
    "v_max3_f32 v243, v108, v109, v110\n"
    "v_max3_f32 v243, v243, v111, v120\n"
    "v_max3_f32 v243, v243, v121, v122\n"
    "v_max3_f32 v243, v243, v123, v132\n"
    "v_max3_f32 v243, v243, v133, v134\n"
    "v_max3_f32 v243, v243, v135, v140\n"
    "v_max3_f32 v243, v243, v141, v142\n"
    "v_max_f32 v243, v243, v143\n"
    "v_mov_b32 v245, v243\n"
    "s_nop 1\n"
    "v_permlane32_swap_b32 v243, v245\n"
    "v_max_f32 v243, v243, v245\n"
    "v_mov_b32 v245, v243\n"
    "s_nop 1\n"
    "v_permlane16_swap_b32 v243, v245\n"
    "v_max_f32 v243, v243, v245\n"
    "v_mul_f32 v243, s64, v243\n"
    "v_min_f32 v243, v243, v233\n"
    "v_sub_f32 v245, v243, v233\n"
    "v_exp_f32 v245, v245\n"
    "v_mov_b32 v233, v243\n"
    "v_mul_f32_e64 v237, v237, v245\n"
    "v_accvgpr_read_b32 v184, a64\n"
    "v_accvgpr_read_b32 v185, a65\n"
    "v_accvgpr_read_b32 v186, a66\n"
    "v_accvgpr_read_b32 v187, a67\n"
    "v_mul_f32 v184, v184, v245\n"
    "v_mul_f32 v185, v185, v245\n"
    "v_mul_f32 v186, v186, v245\n"
    "v_mul_f32 v187, v187, v245\n"
    "v_accvgpr_write_b32 a64, v184\n"
    "v_accvgpr_write_b32 a65, v185\n"
    "v_accvgpr_write_b32 a66, v186\n"
    "v_accvgpr_write_b32 a67, v187\n"
    "v_accvgpr_read_b32 v184, a68\n"
    "v_accvgpr_read_b32 v185, a69\n"
    "v_accvgpr_read_b32 v186, a70\n"
    "v_accvgpr_read_b32 v187, a71\n"
    "v_mul_f32 v184, v184, v245\n"
    "v_mul_f32 v185, v185, v245\n"
    "v_mul_f32 v186, v186, v245\n"
    "v_mul_f32 v187, v187, v245\n"
    "v_accvgpr_write_b32 a68, v184\n"
    "v_accvgpr_write_b32 a69, v185\n"
    "v_accvgpr_write_b32 a70, v186\n"
    "v_accvgpr_write_b32 a71, v187\n"
    "v_accvgpr_read_b32 v184, a72\n"
    "v_accvgpr_read_b32 v185, a73\n"
    "v_accvgpr_read_b32 v186, a74\n"
    "v_accvgpr_read_b32 v187, a75\n"
    "v_mul_f32 v184, v184, v245\n"
    "v_mul_f32 v185, v185, v245\n"
    "v_mul_f32 v186, v186, v245\n"
    "v_mul_f32 v187, v187, v245\n"
    "v_accvgpr_write_b32 a72, v184\n"
    "v_accvgpr_write_b32 a73, v185\n"
    "v_accvgpr_write_b32 a74, v186\n"
    "v_accvgpr_write_b32 a75, v187\n"
    "v_accvgpr_read_b32 v184, a76\n"
    "v_accvgpr_read_b32 v185, a77\n"
    "v_accvgpr_read_b32 v186, a78\n"
    "v_accvgpr_read_b32 v187, a79\n"
    "v_mul_f32 v184, v184, v245\n"
    "v_mul_f32 v185, v185, v245\n"
    "v_mul_f32 v186, v186, v245\n"
    "v_mul_f32 v187, v187, v245\n"
    "v_accvgpr_write_b32 a76, v184\n"
    "v_accvgpr_write_b32 a77, v185\n"
    "v_accvgpr_write_b32 a78, v186\n"
    "v_accvgpr_write_b32 a79, v187\n"
    "v_accvgpr_read_b32 v184, a80\n"
    "v_accvgpr_read_b32 v185, a81\n"
    "v_accvgpr_read_b32 v186, a82\n"
    "v_accvgpr_read_b32 v187, a83\n"
    "v_mul_f32 v184, v184, v245\n"
    "v_mul_f32 v185, v185, v245\n"
    "v_mul_f32 v186, v186, v245\n"
    "v_mul_f32 v187, v187, v245\n"
    "v_accvgpr_write_b32 a80, v184\n"
    "v_accvgpr_write_b32 a81, v185\n"
    "v_accvgpr_write_b32 a82, v186\n"
    "v_accvgpr_write_b32 a83, v187\n"
    "v_accvgpr_read_b32 v184, a84\n"
    "v_accvgpr_read_b32 v185, a85\n"
    "v_accvgpr_read_b32 v186, a86\n"
    "v_accvgpr_read_b32 v187, a87\n"
    "v_mul_f32 v184, v184, v245\n"
    "v_mul_f32 v185, v185, v245\n"
    "v_mul_f32 v186, v186, v245\n"
    "v_mul_f32 v187, v187, v245\n"
    "v_accvgpr_write_b32 a84, v184\n"
    "v_accvgpr_write_b32 a85, v185\n"
    "v_accvgpr_write_b32 a86, v186\n"
    "v_accvgpr_write_b32 a87, v187\n"
    "v_accvgpr_read_b32 v184, a88\n"
    "v_accvgpr_read_b32 v185, a89\n"
    "v_accvgpr_read_b32 v186, a90\n"
    "v_accvgpr_read_b32 v187, a91\n"
    "v_mul_f32 v184, v184, v245\n"
    "v_mul_f32 v185, v185, v245\n"
    "v_mul_f32 v186, v186, v245\n"
    "v_mul_f32 v187, v187, v245\n"
    "v_accvgpr_write_b32 a88, v184\n"
    "v_accvgpr_write_b32 a89, v185\n"
    "v_accvgpr_write_b32 a90, v186\n"
    "v_accvgpr_write_b32 a91, v187\n"
    "v_accvgpr_read_b32 v184, a92\n"
    "v_accvgpr_read_b32 v185, a93\n"
    "v_accvgpr_read_b32 v186, a94\n"
    "v_accvgpr_read_b32 v187, a95\n"
    "v_mul_f32 v184, v184, v245\n"
    "v_mul_f32 v185, v185, v245\n"
    "v_mul_f32 v186, v186, v245\n"
    "v_mul_f32 v187, v187, v245\n"
    "v_accvgpr_write_b32 a92, v184\n"
    "v_accvgpr_write_b32 a93, v185\n"
    "v_accvgpr_write_b32 a94, v186\n"
    "v_accvgpr_write_b32 a95, v187\n"
    "v_accvgpr_read_b32 v184, a96\n"
    "v_accvgpr_read_b32 v185, a97\n"
    "v_accvgpr_read_b32 v186, a98\n"
    "v_accvgpr_read_b32 v187, a99\n"
    "v_mul_f32 v184, v184, v245\n"
    "v_mul_f32 v185, v185, v245\n"
    "v_mul_f32 v186, v186, v245\n"
    "v_mul_f32 v187, v187, v245\n"
    "v_accvgpr_write_b32 a96, v184\n"
    "v_accvgpr_write_b32 a97, v185\n"
    "v_accvgpr_write_b32 a98, v186\n"
    "v_accvgpr_write_b32 a99, v187\n"
    "v_accvgpr_read_b32 v184, a100\n"
    "v_accvgpr_read_b32 v185, a101\n"
    "v_accvgpr_read_b32 v186, a102\n"
    "v_accvgpr_read_b32 v187, a103\n"
    "v_mul_f32 v184, v184, v245\n"
    "v_mul_f32 v185, v185, v245\n"
    "v_mul_f32 v186, v186, v245\n"
    "v_mul_f32 v187, v187, v245\n"
    "v_accvgpr_write_b32 a100, v184\n"
    "v_accvgpr_write_b32 a101, v185\n"
    "v_accvgpr_write_b32 a102, v186\n"
    "v_accvgpr_write_b32 a103, v187\n"
    "v_accvgpr_read_b32 v184, a104\n"
    "v_accvgpr_read_b32 v185, a105\n"
    "v_accvgpr_read_b32 v186, a106\n"
    "v_accvgpr_read_b32 v187, a107\n"
    "v_mul_f32 v184, v184, v245\n"
    "v_mul_f32 v185, v185, v245\n"
    "v_mul_f32 v186, v186, v245\n"
    "v_mul_f32 v187, v187, v245\n"
    "v_accvgpr_write_b32 a104, v184\n"
    "v_accvgpr_write_b32 a105, v185\n"
    "v_accvgpr_write_b32 a106, v186\n"
    "v_accvgpr_write_b32 a107, v187\n"
    "v_accvgpr_read_b32 v184, a108\n"
    "v_accvgpr_read_b32 v185, a109\n"
    "v_accvgpr_read_b32 v186, a110\n"
    "v_accvgpr_read_b32 v187, a111\n"
    "v_mul_f32 v184, v184, v245\n"
    "v_mul_f32 v185, v185, v245\n"
    "v_mul_f32 v186, v186, v245\n"
    "v_mul_f32 v187, v187, v245\n"
    "v_accvgpr_write_b32 a108, v184\n"
    "v_accvgpr_write_b32 a109, v185\n"
    "v_accvgpr_write_b32 a110, v186\n"
    "v_accvgpr_write_b32 a111, v187\n"
    "v_accvgpr_read_b32 v184, a112\n"
    "v_accvgpr_read_b32 v185, a113\n"
    "v_accvgpr_read_b32 v186, a114\n"
    "v_accvgpr_read_b32 v187, a115\n"
    "v_mul_f32 v184, v184, v245\n"
    "v_mul_f32 v185, v185, v245\n"
    "v_mul_f32 v186, v186, v245\n"
    "v_mul_f32 v187, v187, v245\n"
    "v_accvgpr_write_b32 a112, v184\n"
    "v_accvgpr_write_b32 a113, v185\n"
    "v_accvgpr_write_b32 a114, v186\n"
    "v_accvgpr_write_b32 a115, v187\n"
    "v_accvgpr_read_b32 v184, a116\n"
    "v_accvgpr_read_b32 v185, a117\n"
    "v_accvgpr_read_b32 v186, a118\n"
    "v_accvgpr_read_b32 v187, a119\n"
    "v_mul_f32 v184, v184, v245\n"
    "v_mul_f32 v185, v185, v245\n"
    "v_mul_f32 v186, v186, v245\n"
    "v_mul_f32 v187, v187, v245\n"
    "v_accvgpr_write_b32 a116, v184\n"
    "v_accvgpr_write_b32 a117, v185\n"
    "v_accvgpr_write_b32 a118, v186\n"
    "v_accvgpr_write_b32 a119, v187\n"
    "v_accvgpr_read_b32 v184, a120\n"
    "v_accvgpr_read_b32 v185, a121\n"
    "v_accvgpr_read_b32 v186, a122\n"
    "v_accvgpr_read_b32 v187, a123\n"
    "v_mul_f32 v184, v184, v245\n"
    "v_mul_f32 v185, v185, v245\n"
    "v_mul_f32 v186, v186, v245\n"
    "v_mul_f32 v187, v187, v245\n"
    "v_accvgpr_write_b32 a120, v184\n"
    "v_accvgpr_write_b32 a121, v185\n"
    "v_accvgpr_write_b32 a122, v186\n"
    "v_accvgpr_write_b32 a123, v187\n"
    "v_accvgpr_read_b32 v184, a124\n"
    "v_accvgpr_read_b32 v185, a125\n"
    "v_accvgpr_read_b32 v186, a126\n"
    "v_accvgpr_read_b32 v187, a127\n"
    "v_mul_f32 v184, v184, v245\n"
    "v_mul_f32 v185, v185, v245\n"
    "v_mul_f32 v186, v186, v245\n"
    "v_mul_f32 v187, v187, v245\n"
    "v_accvgpr_write_b32 a124, v184\n"
    "v_accvgpr_write_b32 a125, v185\n"
    "v_accvgpr_write_b32 a126, v186\n"
    "v_accvgpr_write_b32 a127, v187\n"
    "v_max3_f32 v243, v112, v113, v114\n"
    "v_max3_f32 v243, v243, v115, v124\n"
    "v_max3_f32 v243, v243, v125, v126\n"
    "v_max_f32 v243, v243, v127\n"
    "v_mov_b32 v245, v243\n"
    "s_nop 1\n"
    "v_permlane32_swap_b32 v243, v245\n"
    "v_max_f32 v243, v243, v245\n"
    "v_mov_b32 v245, v243\n"
    "s_nop 1\n"
    "v_permlane16_swap_b32 v243, v245\n"
    "v_max_f32 v243, v243, v245\n"
    "v_mul_f32 v243, s64, v243\n"
    "v_min_f32 v243, v243, v234\n"
    "v_sub_f32 v245, v243, v234\n"
    "v_exp_f32 v245, v245\n"
    "v_mov_b32 v234, v243\n"
    "v_mul_f32 v235, v235, v245\n"
    "v_mul_f32 v0, v0, v245\n"
    "v_mul_f32 v1, v1, v245\n"
    "v_mul_f32 v2, v2, v245\n"
    "v_mul_f32 v3, v3, v245\n"
    "v_mul_f32 v4, v4, v245\n"
    "v_mul_f32 v5, v5, v245\n"
    "v_mul_f32 v6, v6, v245\n"
    "v_mul_f32 v7, v7, v245\n"
    "v_mul_f32 v8, v8, v245\n"
    "v_mul_f32 v9, v9, v245\n"
    "v_mul_f32 v10, v10, v245\n"
    "v_mul_f32 v11, v11, v245\n"
    "v_mul_f32 v12, v12, v245\n"
    "v_mul_f32 v13, v13, v245\n"
    "v_mul_f32 v14, v14, v245\n"
    "v_mul_f32 v15, v15, v245\n"
    "v_mul_f32 v16, v16, v245\n"
    "v_mul_f32 v17, v17, v245\n"
    "v_mul_f32 v18, v18, v245\n"
    "v_mul_f32 v19, v19, v245\n"
    "v_mul_f32 v20, v20, v245\n"
    "v_mul_f32 v21, v21, v245\n"
    "v_mul_f32 v22, v22, v245\n"
    "v_mul_f32 v23, v23, v245\n"
    "v_mul_f32 v24, v24, v245\n"
    "v_mul_f32 v25, v25, v245\n"
    "v_mul_f32 v26, v26, v245\n"
    "v_mul_f32 v27, v27, v245\n"
    "v_mul_f32 v28, v28, v245\n"
    "v_mul_f32 v29, v29, v245\n"
    "v_mul_f32 v30, v30, v245\n"
    "v_mul_f32 v31, v31, v245\n"
    "v_mul_f32 v32, v32, v245\n"
    "v_mul_f32 v33, v33, v245\n"
    "v_mul_f32 v34, v34, v245\n"
    "v_mul_f32 v35, v35, v245\n"
    "v_mul_f32 v36, v36, v245\n"
    "v_mul_f32 v37, v37, v245\n"
    "v_mul_f32 v38, v38, v245\n"
    "v_mul_f32 v39, v39, v245\n"
    "v_mul_f32 v40, v40, v245\n"
    "v_mul_f32 v41, v41, v245\n"
    "v_mul_f32 v42, v42, v245\n"
    "v_mul_f32 v43, v43, v245\n"
    "v_mul_f32 v44, v44, v245\n"
    "v_mul_f32 v45, v45, v245\n"
    "v_mul_f32 v46, v46, v245\n"
    "v_mul_f32 v47, v47, v245\n"
    "v_mul_f32 v48, v48, v245\n"
    "v_mul_f32 v49, v49, v245\n"
    "v_mul_f32 v50, v50, v245\n"
    "v_mul_f32 v51, v51, v245\n"
    "v_mul_f32 v52, v52, v245\n"
    "v_mul_f32 v53, v53, v245\n"
    "v_mul_f32 v54, v54, v245\n"
    "v_mul_f32 v55, v55, v245\n"
    "v_mul_f32 v56, v56, v245\n"
    "v_mul_f32 v57, v57, v245\n"
    "v_mul_f32 v58, v58, v245\n"
    "v_mul_f32 v59, v59, v245\n"
    "v_mul_f32 v60, v60, v245\n"
    "v_mul_f32 v61, v61, v245\n"
    "v_mul_f32 v62, v62, v245\n"
    "v_mul_f32_e64 v63, v63, v245\n"
    "v_fma_f32 v184, v104, s58, v232\n"
    "v_fma_f32 v185, v105, s58, v232\n"
    "v_fma_f32 v186, v106, s58, v232\n"
    "v_fma_f32 v187, v107, s58, v232\n"
    "v_exp_f32_e64 v184, v184\n"
    "v_fma_f32 v188, v116, s58, v232\n"
    "v_exp_f32_e64 v185, v185\n"
    "v_fma_f32 v189, v117, s58, v232\n"
    "v_exp_f32_e64 v186, v186\n"
    "v_fma_f32 v190, v118, s58, v232\n"
    "v_exp_f32_e64 v187, v187\n"
    "v_fma_f32 v191, v119, s58, v232\n"
    "v_exp_f32 v188, v188\n"
    "v_add_f32 v238, v184, v185\n"
    "v_cvt_pk_bf16_f32 v164, v184, v185\n"
    "v_fma_f32 v192, v108, s58, v233\n"
    "v_exp_f32 v189, v189\n"
    "v_add_f32 v238, v238, v186\n"
    "v_fma_f32 v193, v109, s58, v233\n"
    "v_exp_f32 v190, v190\n"
    "v_add_f32 v238, v238, v187\n"
    "v_cvt_pk_bf16_f32 v165, v186, v187\n"
    "v_fma_f32 v194, v110, s58, v233\n"
    "v_exp_f32 v191, v191\n"
    "v_add_f32 v238, v238, v188\n"
    "v_fma_f32 v195, v111, s58, v233\n"
    "v_exp_f32 v192, v192\n"
    "v_add_f32 v238, v238, v189\n"
    "v_cvt_pk_bf16_f32 v166, v188, v189\n"
    "v_fma_f32 v196, v120, s58, v233\n"
    "v_exp_f32 v193, v193\n"
    "v_add_f32 v238, v238, v190\n"
    "v_fma_f32 v197, v121, s58, v233\n"
    "v_exp_f32 v194, v194\n"
    "v_add_f32 v238, v238, v191\n"
    "v_cvt_pk_bf16_f32 v167, v190, v191\n"
    "v_fma_f32 v198, v122, s58, v233\n"
    "v_exp_f32_e64 v195, v195\n"
    "v_fma_f32 v199, v123, s58, v233\n"
    "v_exp_f32 v196, v196\n"
    "v_add_f32 v239, v192, v193\n"
    "v_cvt_pk_bf16_f32 v168, v192, v193\n"
    "v_fma_f32 v184, v112, s58, v234\n"
    "v_exp_f32 v197, v197\n"
    "v_add_f32 v239, v239, v194\n"
    "v_fma_f32 v185, v113, s58, v234\n"
    "v_exp_f32 v198, v198\n"
    "v_add_f32 v239, v239, v195\n"
    "v_cvt_pk_bf16_f32 v169, v194, v195\n"
    "v_fma_f32 v186, v114, s58, v234\n"
    "v_exp_f32 v199, v199\n"
    "v_add_f32 v239, v239, v196\n"
    "v_fma_f32 v187, v115, s58, v234\n"
    "v_exp_f32 v184, v184\n"
    "v_add_f32 v239, v239, v197\n"
    "v_cvt_pk_bf16_f32 v170, v196, v197\n"
    "v_fma_f32 v188, v124, s58, v234\n"
    "v_exp_f32 v185, v185\n"
    "v_add_f32 v239, v239, v198\n"
    "v_fma_f32 v189, v125, s58, v234\n"
    "v_exp_f32 v186, v186\n"
    "v_add_f32 v239, v239, v199\n"
    "v_cvt_pk_bf16_f32 v171, v198, v199\n"
    "v_fma_f32 v190, v126, s58, v234\n"
    "v_exp_f32_e64 v187, v187\n"
    "v_fma_f32 v191, v127, s58, v234\n"
    "v_exp_f32 v188, v188\n"
    "v_add_f32 v240, v184, v185\n"
    "v_cvt_pk_bf16_f32 v172, v184, v185\n"
    "v_fma_f32 v192, v128, s58, v232\n"
    "v_exp_f32 v189, v189\n"
    "v_add_f32 v240, v240, v186\n"
    "v_fma_f32 v193, v129, s58, v232\n"
    "v_exp_f32 v190, v190\n"
    "v_add_f32 v240, v240, v187\n"
    "v_cvt_pk_bf16_f32 v173, v186, v187\n"
    "v_fma_f32 v194, v130, s58, v232\n"
    "v_exp_f32 v191, v191\n"
    "v_add_f32 v240, v240, v188\n"
    "v_fma_f32 v195, v131, s58, v232\n"
    "v_exp_f32 v192, v192\n"
    "v_add_f32 v240, v240, v189\n"
    "v_cvt_pk_bf16_f32 v174, v188, v189\n"
    "v_fma_f32 v196, v136, s58, v232\n"
    "v_exp_f32 v193, v193\n"
    "v_add_f32 v240, v240, v190\n"
    "v_fma_f32 v197, v137, s58, v232\n"
    "v_exp_f32 v194, v194\n"
    "v_add_f32 v240, v240, v191\n"
    "v_cvt_pk_bf16_f32 v175, v190, v191\n"
    "v_fma_f32 v198, v138, s58, v232\n"
    "v_exp_f32 v195, v195\n"
    "v_add_f32 v238, v238, v192\n"
    "v_fma_f32 v199, v139, s58, v232\n"
    "v_exp_f32 v196, v196\n"
    "v_add_f32 v238, v238, v193\n"
    "v_cvt_pk_bf16_f32 v176, v192, v193\n"
    "v_fma_f32 v184, v132, s58, v233\n"
    "v_exp_f32 v197, v197\n"
    "v_add_f32 v238, v238, v194\n"
    "v_fma_f32 v185, v133, s58, v233\n"
    "v_exp_f32 v198, v198\n"
    "v_add_f32 v238, v238, v195\n"
    "v_cvt_pk_bf16_f32 v177, v194, v195\n"
    "v_fma_f32 v186, v134, s58, v233\n"
    "v_exp_f32 v199, v199\n"
    "v_add_f32 v238, v238, v196\n"
    "v_fma_f32 v187, v135, s58, v233\n"
    "v_exp_f32 v184, v184\n"
    "v_add_f32 v238, v238, v197\n"
    "v_cvt_pk_bf16_f32 v178, v196, v197\n"
    "v_fma_f32 v188, v140, s58, v233\n"
    "v_exp_f32 v185, v185\n"
    "v_add_f32 v238, v238, v198\n"
    "v_fma_f32 v189, v141, s58, v233\n"
    "v_exp_f32 v186, v186\n"
    "v_add_f32 v238, v238, v199\n"
    "v_cvt_pk_bf16_f32 v179, v198, v199\n"
    "v_fma_f32 v190, v142, s58, v233\n"
    "v_exp_f32 v187, v187\n"
    "v_add_f32 v239, v239, v184\n"
    "v_fma_f32 v191, v143, s58, v233\n"
    "v_exp_f32 v188, v188\n"
    "v_add_f32 v239, v239, v185\n"
    "v_cvt_pk_bf16_f32 v180, v184, v185\n"
    "v_exp_f32 v189, v189\n"
    "v_add_f32 v239, v239, v186\n"
    "v_exp_f32 v190, v190\n"
    "v_add_f32 v239, v239, v187\n"
    "v_cvt_pk_bf16_f32 v181, v186, v187\n"
    "v_exp_f32 v191, v191\n"
    "v_add_f32 v239, v239, v188\n"
    "v_add_f32_e64 v239, v239, v189\n"
    "v_cvt_pk_bf16_f32 v182, v188, v189\n"
    "v_add_f32_e64 v239, v239, v190\n"
    "v_add_f32 v239, v239, v191\n"
    "v_cvt_pk_bf16_f32 v183, v190, v191\n"
    "s_nop 3\n"
    "s_branch Lloop_%=\n"
    "LrareP14t_%=:\n"
    "s_nop 7\n"
    "v_max3_f32 v243, v104, v105, v106\n"
    "v_max3_f32 v243, v243, v107, v116\n"
    "v_max3_f32 v243, v243, v117, v118\n"
    "v_max3_f32 v243, v243, v119, v128\n"
    "v_max3_f32 v243, v243, v129, v130\n"
    "v_max3_f32 v243, v243, v131, v136\n"
    "v_max3_f32 v243, v243, v137, v138\n"
    "v_max_f32 v243, v243, v139\n"
    "v_mov_b32 v245, v243\n"
    "s_nop 1\n"
    "v_permlane32_swap_b32 v243, v245\n"
    "v_max_f32 v243, v243, v245\n"
    "v_mov_b32 v245, v243\n"
    "s_nop 1\n"
    "v_permlane16_swap_b32 v243, v245\n"
    "v_max_f32 v243, v243, v245\n"
    "v_mul_f32 v243, s64, v243\n"
    "v_min_f32 v243, v243, v232\n"
    "v_sub_f32 v245, v243, v232\n"
    "v_exp_f32 v245, v245\n"
    "v_mov_b32 v232, v243\n"
    "v_mul_f32_e64 v236, v236, v245\n"
    "v_accvgpr_read_b32 v184, a0\n"
    "v_accvgpr_read_b32 v185, a1\n"
    "v_accvgpr_read_b32 v186, a2\n"
    "v_accvgpr_read_b32 v187, a3\n"
    "v_mul_f32 v184, v184, v245\n"
    "v_mul_f32 v185, v185, v245\n"
    "v_mul_f32 v186, v186, v245\n"
    "v_mul_f32 v187, v187, v245\n"
    "v_accvgpr_write_b32 a0, v184\n"
    "v_accvgpr_write_b32 a1, v185\n"
    "v_accvgpr_write_b32 a2, v186\n"
    "v_accvgpr_write_b32 a3, v187\n"
    "v_accvgpr_read_b32 v184, a4\n"
    "v_accvgpr_read_b32 v185, a5\n"
    "v_accvgpr_read_b32 v186, a6\n"
    "v_accvgpr_read_b32 v187, a7\n"
    "v_mul_f32 v184, v184, v245\n"
    "v_mul_f32 v185, v185, v245\n"
    "v_mul_f32 v186, v186, v245\n"
    "v_mul_f32 v187, v187, v245\n"
    "v_accvgpr_write_b32 a4, v184\n"
    "v_accvgpr_write_b32 a5, v185\n"
    "v_accvgpr_write_b32 a6, v186\n"
    "v_accvgpr_write_b32 a7, v187\n"
    "v_accvgpr_read_b32 v184, a8\n"
    "v_accvgpr_read_b32 v185, a9\n"
    "v_accvgpr_read_b32 v186, a10\n"
    "v_accvgpr_read_b32 v187, a11\n"
    "v_mul_f32 v184, v184, v245\n"
    "v_mul_f32 v185, v185, v245\n"
    "v_mul_f32 v186, v186, v245\n"
    "v_mul_f32 v187, v187, v245\n"
    "v_accvgpr_write_b32 a8, v184\n"
    "v_accvgpr_write_b32 a9, v185\n"
    "v_accvgpr_write_b32 a10, v186\n"
    "v_accvgpr_write_b32 a11, v187\n"
    "v_accvgpr_read_b32 v184, a12\n"
    "v_accvgpr_read_b32 v185, a13\n"
    "v_accvgpr_read_b32 v186, a14\n"
    "v_accvgpr_read_b32 v187, a15\n"
    "v_mul_f32 v184, v184, v245\n"
    "v_mul_f32 v185, v185, v245\n"
    "v_mul_f32 v186, v186, v245\n"
    "v_mul_f32 v187, v187, v245\n"
    "v_accvgpr_write_b32 a12, v184\n"
    "v_accvgpr_write_b32 a13, v185\n"
    "v_accvgpr_write_b32 a14, v186\n"
    "v_accvgpr_write_b32 a15, v187\n"
    "v_accvgpr_read_b32 v184, a16\n"
    "v_accvgpr_read_b32 v185, a17\n"
    "v_accvgpr_read_b32 v186, a18\n"
    "v_accvgpr_read_b32 v187, a19\n"
    "v_mul_f32 v184, v184, v245\n"
    "v_mul_f32 v185, v185, v245\n"
    "v_mul_f32 v186, v186, v245\n"
    "v_mul_f32 v187, v187, v245\n"
    "v_accvgpr_write_b32 a16, v184\n"
    "v_accvgpr_write_b32 a17, v185\n"
    "v_accvgpr_write_b32 a18, v186\n"
    "v_accvgpr_write_b32 a19, v187\n"
    "v_accvgpr_read_b32 v184, a20\n"
    "v_accvgpr_read_b32 v185, a21\n"
    "v_accvgpr_read_b32 v186, a22\n"
    "v_accvgpr_read_b32 v187, a23\n"
    "v_mul_f32 v184, v184, v245\n"
    "v_mul_f32 v185, v185, v245\n"
    "v_mul_f32 v186, v186, v245\n"
    "v_mul_f32 v187, v187, v245\n"
    "v_accvgpr_write_b32 a20, v184\n"
    "v_accvgpr_write_b32 a21, v185\n"
    "v_accvgpr_write_b32 a22, v186\n"
    "v_accvgpr_write_b32 a23, v187\n"
    "v_accvgpr_read_b32 v184, a24\n"
    "v_accvgpr_read_b32 v185, a25\n"
    "v_accvgpr_read_b32 v186, a26\n"
    "v_accvgpr_read_b32 v187, a27\n"
    "v_mul_f32 v184, v184, v245\n"
    "v_mul_f32 v185, v185, v245\n"
    "v_mul_f32 v186, v186, v245\n"
    "v_mul_f32 v187, v187, v245\n"
    "v_accvgpr_write_b32 a24, v184\n"
    "v_accvgpr_write_b32 a25, v185\n"
    "v_accvgpr_write_b32 a26, v186\n"
    "v_accvgpr_write_b32 a27, v187\n"
    "v_accvgpr_read_b32 v184, a28\n"
    "v_accvgpr_read_b32 v185, a29\n"
    "v_accvgpr_read_b32 v186, a30\n"
    "v_accvgpr_read_b32 v187, a31\n"
    "v_mul_f32 v184, v184, v245\n"
    "v_mul_f32 v185, v185, v245\n"
    "v_mul_f32 v186, v186, v245\n"
    "v_mul_f32 v187, v187, v245\n"
    "v_accvgpr_write_b32 a28, v184\n"
    "v_accvgpr_write_b32 a29, v185\n"
    "v_accvgpr_write_b32 a30, v186\n"
    "v_accvgpr_write_b32 a31, v187\n"
    "v_accvgpr_read_b32 v184, a32\n"
    "v_accvgpr_read_b32 v185, a33\n"
    "v_accvgpr_read_b32 v186, a34\n"
    "v_accvgpr_read_b32 v187, a35\n"
    "v_mul_f32 v184, v184, v245\n"
    "v_mul_f32 v185, v185, v245\n"
    "v_mul_f32 v186, v186, v245\n"
    "v_mul_f32 v187, v187, v245\n"
    "v_accvgpr_write_b32 a32, v184\n"
    "v_accvgpr_write_b32 a33, v185\n"
    "v_accvgpr_write_b32 a34, v186\n"
    "v_accvgpr_write_b32 a35, v187\n"
    "v_accvgpr_read_b32 v184, a36\n"
    "v_accvgpr_read_b32 v185, a37\n"
    "v_accvgpr_read_b32 v186, a38\n"
    "v_accvgpr_read_b32 v187, a39\n"
    "v_mul_f32 v184, v184, v245\n"
    "v_mul_f32 v185, v185, v245\n"
    "v_mul_f32 v186, v186, v245\n"
    "v_mul_f32 v187, v187, v245\n"
    "v_accvgpr_write_b32 a36, v184\n"
    "v_accvgpr_write_b32 a37, v185\n"
    "v_accvgpr_write_b32 a38, v186\n"
    "v_accvgpr_write_b32 a39, v187\n"
    "v_accvgpr_read_b32 v184, a40\n"
    "v_accvgpr_read_b32 v185, a41\n"
    "v_accvgpr_read_b32 v186, a42\n"
    "v_accvgpr_read_b32 v187, a43\n"
    "v_mul_f32 v184, v184, v245\n"
    "v_mul_f32 v185, v185, v245\n"
    "v_mul_f32 v186, v186, v245\n"
    "v_mul_f32 v187, v187, v245\n"
    "v_accvgpr_write_b32 a40, v184\n"
    "v_accvgpr_write_b32 a41, v185\n"
    "v_accvgpr_write_b32 a42, v186\n"
    "v_accvgpr_write_b32 a43, v187\n"
    "v_accvgpr_read_b32 v184, a44\n"
    "v_accvgpr_read_b32 v185, a45\n"
    "v_accvgpr_read_b32 v186, a46\n"
    "v_accvgpr_read_b32 v187, a47\n"
    "v_mul_f32 v184, v184, v245\n"
    "v_mul_f32 v185, v185, v245\n"
    "v_mul_f32 v186, v186, v245\n"
    "v_mul_f32 v187, v187, v245\n"
    "v_accvgpr_write_b32 a44, v184\n"
    "v_accvgpr_write_b32 a45, v185\n"
    "v_accvgpr_write_b32 a46, v186\n"
    "v_accvgpr_write_b32 a47, v187\n"
    "v_accvgpr_read_b32 v184, a48\n"
    "v_accvgpr_read_b32 v185, a49\n"
    "v_accvgpr_read_b32 v186, a50\n"
    "v_accvgpr_read_b32 v187, a51\n"
    "v_mul_f32 v184, v184, v245\n"
    "v_mul_f32 v185, v185, v245\n"
    "v_mul_f32 v186, v186, v245\n"
    "v_mul_f32 v187, v187, v245\n"
    "v_accvgpr_write_b32 a48, v184\n"
    "v_accvgpr_write_b32 a49, v185\n"
    "v_accvgpr_write_b32 a50, v186\n"
    "v_accvgpr_write_b32 a51, v187\n"
    "v_accvgpr_read_b32 v184, a52\n"
    "v_accvgpr_read_b32 v185, a53\n"
    "v_accvgpr_read_b32 v186, a54\n"
    "v_accvgpr_read_b32 v187, a55\n"
    "v_mul_f32 v184, v184, v245\n"
    "v_mul_f32 v185, v185, v245\n"
    "v_mul_f32 v186, v186, v245\n"
    "v_mul_f32 v187, v187, v245\n"
    "v_accvgpr_write_b32 a52, v184\n"
    "v_accvgpr_write_b32 a53, v185\n"
    "v_accvgpr_write_b32 a54, v186\n"
    "v_accvgpr_write_b32 a55, v187\n"
    "v_accvgpr_read_b32 v184, a56\n"
    "v_accvgpr_read_b32 v185, a57\n"
    "v_accvgpr_read_b32 v186, a58\n"
    "v_accvgpr_read_b32 v187, a59\n"
    "v_mul_f32 v184, v184, v245\n"
    "v_mul_f32 v185, v185, v245\n"
    "v_mul_f32 v186, v186, v245\n"
    "v_mul_f32 v187, v187, v245\n"
    "v_accvgpr_write_b32 a56, v184\n"
    "v_accvgpr_write_b32 a57, v185\n"
    "v_accvgpr_write_b32 a58, v186\n"
    "v_accvgpr_write_b32 a59, v187\n"
    "v_accvgpr_read_b32 v184, a60\n"
    "v_accvgpr_read_b32 v185, a61\n"
    "v_accvgpr_read_b32 v186, a62\n"
    "v_accvgpr_read_b32 v187, a63\n"
    "v_mul_f32 v184, v184, v245\n"
    "v_mul_f32 v185, v185, v245\n"
    "v_mul_f32 v186, v186, v245\n"
    "v_mul_f32 v187, v187, v245\n"
    "v_accvgpr_write_b32 a60, v184\n"
    "v_accvgpr_write_b32 a61, v185\n"
    "v_accvgpr_write_b32 a62, v186\n"
    "v_accvgpr_write_b32 a63, v187\n"
    "v_max3_f32 v243, v108, v109, v110\n"
    "v_max3_f32 v243, v243, v111, v120\n"
    "v_max3_f32 v243, v243, v121, v122\n"
    "v_max3_f32 v243, v243, v123, v132\n"
    "v_max3_f32 v243, v243, v133, v134\n"
    "v_max3_f32 v243, v243, v135, v140\n"
    "v_max3_f32 v243, v243, v141, v142\n"
    "v_max_f32 v243, v243, v143\n"
    "v_mov_b32 v245, v243\n"
    "s_nop 1\n"
    "v_permlane32_swap_b32 v243, v245\n"
    "v_max_f32 v243, v243, v245\n"
    "v_mov_b32 v245, v243\n"
    "s_nop 1\n"
    "v_permlane16_swap_b32 v243, v245\n"
    "v_max_f32 v243, v243, v245\n"
    "v_mul_f32 v243, s64, v243\n"
    "v_min_f32 v243, v243, v233\n"
    "v_sub_f32 v245, v243, v233\n"
    "v_exp_f32 v245, v245\n"
    "v_mov_b32 v233, v243\n"
    "v_mul_f32_e64 v237, v237, v245\n"
    "v_accvgpr_read_b32 v184, a64\n"
    "v_accvgpr_read_b32 v185, a65\n"
    "v_accvgpr_read_b32 v186, a66\n"
    "v_accvgpr_read_b32 v187, a67\n"
    "v_mul_f32 v184, v184, v245\n"
    "v_mul_f32 v185, v185, v245\n"
    "v_mul_f32 v186, v186, v245\n"
    "v_mul_f32 v187, v187, v245\n"
    "v_accvgpr_write_b32 a64, v184\n"
    "v_accvgpr_write_b32 a65, v185\n"
    "v_accvgpr_write_b32 a66, v186\n"
    "v_accvgpr_write_b32 a67, v187\n"
    "v_accvgpr_read_b32 v184, a68\n"
    "v_accvgpr_read_b32 v185, a69\n"
    "v_accvgpr_read_b32 v186, a70\n"
    "v_accvgpr_read_b32 v187, a71\n"
    "v_mul_f32 v184, v184, v245\n"
    "v_mul_f32 v185, v185, v245\n"
    "v_mul_f32 v186, v186, v245\n"
    "v_mul_f32 v187, v187, v245\n"
    "v_accvgpr_write_b32 a68, v184\n"
    "v_accvgpr_write_b32 a69, v185\n"
    "v_accvgpr_write_b32 a70, v186\n"
    "v_accvgpr_write_b32 a71, v187\n"
    "v_accvgpr_read_b32 v184, a72\n"
    "v_accvgpr_read_b32 v185, a73\n"
    "v_accvgpr_read_b32 v186, a74\n"
    "v_accvgpr_read_b32 v187, a75\n"
    "v_mul_f32 v184, v184, v245\n"
    "v_mul_f32 v185, v185, v245\n"
    "v_mul_f32 v186, v186, v245\n"
    "v_mul_f32 v187, v187, v245\n"
    "v_accvgpr_write_b32 a72, v184\n"
    "v_accvgpr_write_b32 a73, v185\n"
    "v_accvgpr_write_b32 a74, v186\n"
    "v_accvgpr_write_b32 a75, v187\n"
    "v_accvgpr_read_b32 v184, a76\n"
    "v_accvgpr_read_b32 v185, a77\n"
    "v_accvgpr_read_b32 v186, a78\n"
    "v_accvgpr_read_b32 v187, a79\n"
    "v_mul_f32 v184, v184, v245\n"
    "v_mul_f32 v185, v185, v245\n"
    "v_mul_f32 v186, v186, v245\n"
    "v_mul_f32 v187, v187, v245\n"
    "v_accvgpr_write_b32 a76, v184\n"
    "v_accvgpr_write_b32 a77, v185\n"
    "v_accvgpr_write_b32 a78, v186\n"
    "v_accvgpr_write_b32 a79, v187\n"
    "v_accvgpr_read_b32 v184, a80\n"
    "v_accvgpr_read_b32 v185, a81\n"
    "v_accvgpr_read_b32 v186, a82\n"
    "v_accvgpr_read_b32 v187, a83\n"
    "v_mul_f32 v184, v184, v245\n"
    "v_mul_f32 v185, v185, v245\n"
    "v_mul_f32 v186, v186, v245\n"
    "v_mul_f32 v187, v187, v245\n"
    "v_accvgpr_write_b32 a80, v184\n"
    "v_accvgpr_write_b32 a81, v185\n"
    "v_accvgpr_write_b32 a82, v186\n"
    "v_accvgpr_write_b32 a83, v187\n"
    "v_accvgpr_read_b32 v184, a84\n"
    "v_accvgpr_read_b32 v185, a85\n"
    "v_accvgpr_read_b32 v186, a86\n"
    "v_accvgpr_read_b32 v187, a87\n"
    "v_mul_f32 v184, v184, v245\n"
    "v_mul_f32 v185, v185, v245\n"
    "v_mul_f32 v186, v186, v245\n"
    "v_mul_f32 v187, v187, v245\n"
    "v_accvgpr_write_b32 a84, v184\n"
    "v_accvgpr_write_b32 a85, v185\n"
    "v_accvgpr_write_b32 a86, v186\n"
    "v_accvgpr_write_b32 a87, v187\n"
    "v_accvgpr_read_b32 v184, a88\n"
    "v_accvgpr_read_b32 v185, a89\n"
    "v_accvgpr_read_b32 v186, a90\n"
    "v_accvgpr_read_b32 v187, a91\n"
    "v_mul_f32 v184, v184, v245\n"
    "v_mul_f32 v185, v185, v245\n"
    "v_mul_f32 v186, v186, v245\n"
    "v_mul_f32 v187, v187, v245\n"
    "v_accvgpr_write_b32 a88, v184\n"
    "v_accvgpr_write_b32 a89, v185\n"
    "v_accvgpr_write_b32 a90, v186\n"
    "v_accvgpr_write_b32 a91, v187\n"
    "v_accvgpr_read_b32 v184, a92\n"
    "v_accvgpr_read_b32 v185, a93\n"
    "v_accvgpr_read_b32 v186, a94\n"
    "v_accvgpr_read_b32 v187, a95\n"
    "v_mul_f32 v184, v184, v245\n"
    "v_mul_f32 v185, v185, v245\n"
    "v_mul_f32 v186, v186, v245\n"
    "v_mul_f32 v187, v187, v245\n"
    "v_accvgpr_write_b32 a92, v184\n"
    "v_accvgpr_write_b32 a93, v185\n"
    "v_accvgpr_write_b32 a94, v186\n"
    "v_accvgpr_write_b32 a95, v187\n"
    "v_accvgpr_read_b32 v184, a96\n"
    "v_accvgpr_read_b32 v185, a97\n"
    "v_accvgpr_read_b32 v186, a98\n"
    "v_accvgpr_read_b32 v187, a99\n"
    "v_mul_f32 v184, v184, v245\n"
    "v_mul_f32 v185, v185, v245\n"
    "v_mul_f32 v186, v186, v245\n"
    "v_mul_f32 v187, v187, v245\n"
    "v_accvgpr_write_b32 a96, v184\n"
    "v_accvgpr_write_b32 a97, v185\n"
    "v_accvgpr_write_b32 a98, v186\n"
    "v_accvgpr_write_b32 a99, v187\n"
    "v_accvgpr_read_b32 v184, a100\n"
    "v_accvgpr_read_b32 v185, a101\n"
    "v_accvgpr_read_b32 v186, a102\n"
    "v_accvgpr_read_b32 v187, a103\n"
    "v_mul_f32 v184, v184, v245\n"
    "v_mul_f32 v185, v185, v245\n"
    "v_mul_f32 v186, v186, v245\n"
    "v_mul_f32 v187, v187, v245\n"
    "v_accvgpr_write_b32 a100, v184\n"
    "v_accvgpr_write_b32 a101, v185\n"
    "v_accvgpr_write_b32 a102, v186\n"
    "v_accvgpr_write_b32 a103, v187\n"
    "v_accvgpr_read_b32 v184, a104\n"
    "v_accvgpr_read_b32 v185, a105\n"
    "v_accvgpr_read_b32 v186, a106\n"
    "v_accvgpr_read_b32 v187, a107\n"
    "v_mul_f32 v184, v184, v245\n"
    "v_mul_f32 v185, v185, v245\n"
    "v_mul_f32 v186, v186, v245\n"
    "v_mul_f32 v187, v187, v245\n"
    "v_accvgpr_write_b32 a104, v184\n"
    "v_accvgpr_write_b32 a105, v185\n"
    "v_accvgpr_write_b32 a106, v186\n"
    "v_accvgpr_write_b32 a107, v187\n"
    "v_accvgpr_read_b32 v184, a108\n"
    "v_accvgpr_read_b32 v185, a109\n"
    "v_accvgpr_read_b32 v186, a110\n"
    "v_accvgpr_read_b32 v187, a111\n"
    "v_mul_f32 v184, v184, v245\n"
    "v_mul_f32 v185, v185, v245\n"
    "v_mul_f32 v186, v186, v245\n"
    "v_mul_f32 v187, v187, v245\n"
    "v_accvgpr_write_b32 a108, v184\n"
    "v_accvgpr_write_b32 a109, v185\n"
    "v_accvgpr_write_b32 a110, v186\n"
    "v_accvgpr_write_b32 a111, v187\n"
    "v_accvgpr_read_b32 v184, a112\n"
    "v_accvgpr_read_b32 v185, a113\n"
    "v_accvgpr_read_b32 v186, a114\n"
    "v_accvgpr_read_b32 v187, a115\n"
    "v_mul_f32 v184, v184, v245\n"
    "v_mul_f32 v185, v185, v245\n"
    "v_mul_f32 v186, v186, v245\n"
    "v_mul_f32 v187, v187, v245\n"
    "v_accvgpr_write_b32 a112, v184\n"
    "v_accvgpr_write_b32 a113, v185\n"
    "v_accvgpr_write_b32 a114, v186\n"
    "v_accvgpr_write_b32 a115, v187\n"
    "v_accvgpr_read_b32 v184, a116\n"
    "v_accvgpr_read_b32 v185, a117\n"
    "v_accvgpr_read_b32 v186, a118\n"
    "v_accvgpr_read_b32 v187, a119\n"
    "v_mul_f32 v184, v184, v245\n"
    "v_mul_f32 v185, v185, v245\n"
    "v_mul_f32 v186, v186, v245\n"
    "v_mul_f32 v187, v187, v245\n"
    "v_accvgpr_write_b32 a116, v184\n"
    "v_accvgpr_write_b32 a117, v185\n"
    "v_accvgpr_write_b32 a118, v186\n"
    "v_accvgpr_write_b32 a119, v187\n"
    "v_accvgpr_read_b32 v184, a120\n"
    "v_accvgpr_read_b32 v185, a121\n"
    "v_accvgpr_read_b32 v186, a122\n"
    "v_accvgpr_read_b32 v187, a123\n"
    "v_mul_f32 v184, v184, v245\n"
    "v_mul_f32 v185, v185, v245\n"
    "v_mul_f32 v186, v186, v245\n"
    "v_mul_f32 v187, v187, v245\n"
    "v_accvgpr_write_b32 a120, v184\n"
    "v_accvgpr_write_b32 a121, v185\n"
    "v_accvgpr_write_b32 a122, v186\n"
    "v_accvgpr_write_b32 a123, v187\n"
    "v_accvgpr_read_b32 v184, a124\n"
    "v_accvgpr_read_b32 v185, a125\n"
    "v_accvgpr_read_b32 v186, a126\n"
    "v_accvgpr_read_b32 v187, a127\n"
    "v_mul_f32 v184, v184, v245\n"
    "v_mul_f32 v185, v185, v245\n"
    "v_mul_f32 v186, v186, v245\n"
    "v_mul_f32 v187, v187, v245\n"
    "v_accvgpr_write_b32 a124, v184\n"
    "v_accvgpr_write_b32 a125, v185\n"
    "v_accvgpr_write_b32 a126, v186\n"
    "v_accvgpr_write_b32 a127, v187\n"
    "v_max3_f32 v243, v112, v113, v114\n"
    "v_max3_f32 v243, v243, v115, v124\n"
    "v_max3_f32 v243, v243, v125, v126\n"
    "v_max_f32 v243, v243, v127\n"
    "v_mov_b32 v245, v243\n"
    "s_nop 1\n"
    "v_permlane32_swap_b32 v243, v245\n"
    "v_max_f32 v243, v243, v245\n"
    "v_mov_b32 v245, v243\n"
    "s_nop 1\n"
    "v_permlane16_swap_b32 v243, v245\n"
    "v_max_f32 v243, v243, v245\n"
    "v_mul_f32 v243, s64, v243\n"
    "v_min_f32 v243, v243, v234\n"
    "v_sub_f32 v245, v243, v234\n"
    "v_exp_f32 v245, v245\n"
    "v_mov_b32 v234, v243\n"
    "v_mul_f32 v235, v235, v245\n"
    "v_mul_f32 v0, v0, v245\n"
    "v_mul_f32 v1, v1, v245\n"
    "v_mul_f32 v2, v2, v245\n"
    "v_mul_f32 v3, v3, v245\n"
    "v_mul_f32 v4, v4, v245\n"
    "v_mul_f32 v5, v5, v245\n"
    "v_mul_f32 v6, v6, v245\n"
    "v_mul_f32 v7, v7, v245\n"
    "v_mul_f32 v8, v8, v245\n"
    "v_mul_f32 v9, v9, v245\n"
    "v_mul_f32 v10, v10, v245\n"
    "v_mul_f32 v11, v11, v245\n"
    "v_mul_f32 v12, v12, v245\n"
    "v_mul_f32 v13, v13, v245\n"
    "v_mul_f32 v14, v14, v245\n"
    "v_mul_f32 v15, v15, v245\n"
    "v_mul_f32 v16, v16, v245\n"
    "v_mul_f32 v17, v17, v245\n"
    "v_mul_f32 v18, v18, v245\n"
    "v_mul_f32 v19, v19, v245\n"
    "v_mul_f32 v20, v20, v245\n"
    "v_mul_f32 v21, v21, v245\n"
    "v_mul_f32 v22, v22, v245\n"
    "v_mul_f32 v23, v23, v245\n"
    "v_mul_f32 v24, v24, v245\n"
    "v_mul_f32 v25, v25, v245\n"
    "v_mul_f32 v26, v26, v245\n"
    "v_mul_f32 v27, v27, v245\n"
    "v_mul_f32 v28, v28, v245\n"
    "v_mul_f32 v29, v29, v245\n"
    "v_mul_f32 v30, v30, v245\n"
    "v_mul_f32 v31, v31, v245\n"
    "v_mul_f32 v32, v32, v245\n"
    "v_mul_f32 v33, v33, v245\n"
    "v_mul_f32 v34, v34, v245\n"
    "v_mul_f32 v35, v35, v245\n"
    "v_mul_f32 v36, v36, v245\n"
    "v_mul_f32 v37, v37, v245\n"
    "v_mul_f32 v38, v38, v245\n"
    "v_mul_f32 v39, v39, v245\n"
    "v_mul_f32 v40, v40, v245\n"
    "v_mul_f32 v41, v41, v245\n"
    "v_mul_f32 v42, v42, v245\n"
    "v_mul_f32 v43, v43, v245\n"
    "v_mul_f32 v44, v44, v245\n"
    "v_mul_f32 v45, v45, v245\n"
    "v_mul_f32 v46, v46, v245\n"
    "v_mul_f32 v47, v47, v245\n"
    "v_mul_f32 v48, v48, v245\n"
    "v_mul_f32 v49, v49, v245\n"
    "v_mul_f32 v50, v50, v245\n"
    "v_mul_f32 v51, v51, v245\n"
    "v_mul_f32 v52, v52, v245\n"
    "v_mul_f32 v53, v53, v245\n"
    "v_mul_f32 v54, v54, v245\n"
    "v_mul_f32 v55, v55, v245\n"
    "v_mul_f32 v56, v56, v245\n"
    "v_mul_f32 v57, v57, v245\n"
    "v_mul_f32 v58, v58, v245\n"
    "v_mul_f32 v59, v59, v245\n"
    "v_mul_f32 v60, v60, v245\n"
    "v_mul_f32 v61, v61, v245\n"
    "v_mul_f32 v62, v62, v245\n"
    "v_mul_f32_e64 v63, v63, v245\n"
    "v_fma_f32 v184, v104, s58, v232\n"
    "v_fma_f32 v185, v105, s58, v232\n"
    "v_fma_f32 v186, v106, s58, v232\n"
    "v_fma_f32 v187, v107, s58, v232\n"
    "v_exp_f32_e64 v184, v184\n"
    "v_fma_f32 v188, v116, s58, v232\n"
    "v_exp_f32_e64 v185, v185\n"
    "v_fma_f32 v189, v117, s58, v232\n"
    "v_exp_f32_e64 v186, v186\n"
    "v_fma_f32 v190, v118, s58, v232\n"
    "v_exp_f32_e64 v187, v187\n"
    "v_fma_f32 v191, v119, s58, v232\n"
    "v_exp_f32 v188, v188\n"
    "v_add_f32 v238, v184, v185\n"
    "v_cvt_pk_bf16_f32 v164, v184, v185\n"
    "v_fma_f32 v192, v108, s58, v233\n"
    "v_exp_f32 v189, v189\n"
    "v_add_f32 v238, v238, v186\n"
    "v_fma_f32 v193, v109, s58, v233\n"
    "v_exp_f32 v190, v190\n"
    "v_add_f32 v238, v238, v187\n"
    "v_cvt_pk_bf16_f32 v165, v186, v187\n"
    "v_fma_f32 v194, v110, s58, v233\n"
    "v_exp_f32 v191, v191\n"
    "v_add_f32 v238, v238, v188\n"
    "v_fma_f32 v195, v111, s58, v233\n"
    "v_exp_f32 v192, v192\n"
    "v_add_f32 v238, v238, v189\n"
    "v_cvt_pk_bf16_f32 v166, v188, v189\n"
    "v_fma_f32 v196, v120, s58, v233\n"
    "v_exp_f32 v193, v193\n"
    "v_add_f32 v238, v238, v190\n"
    "v_fma_f32 v197, v121, s58, v233\n"
    "v_exp_f32 v194, v194\n"
    "v_add_f32 v238, v238, v191\n"
    "v_cvt_pk_bf16_f32 v167, v190, v191\n"
    "v_fma_f32 v198, v122, s58, v233\n"
    "v_exp_f32_e64 v195, v195\n"
    "v_fma_f32 v199, v123, s58, v233\n"
    "v_exp_f32 v196, v196\n"
    "v_add_f32 v239, v192, v193\n"
    "v_cvt_pk_bf16_f32 v168, v192, v193\n"
    "v_fma_f32 v184, v112, s58, v234\n"
    "v_exp_f32 v197, v197\n"
    "v_add_f32 v239, v239, v194\n"
    "v_fma_f32 v185, v113, s58, v234\n"
    "v_exp_f32 v198, v198\n"
    "v_add_f32 v239, v239, v195\n"
    "v_cvt_pk_bf16_f32 v169, v194, v195\n"
    "v_fma_f32 v186, v114, s58, v234\n"
    "v_exp_f32 v199, v199\n"
    "v_add_f32 v239, v239, v196\n"
    "v_fma_f32 v187, v115, s58, v234\n"
    "v_exp_f32 v184, v184\n"
    "v_add_f32 v239, v239, v197\n"
    "v_cvt_pk_bf16_f32 v170, v196, v197\n"
    "v_fma_f32 v188, v124, s58, v234\n"
    "v_exp_f32 v185, v185\n"
    "v_add_f32 v239, v239, v198\n"
    "v_fma_f32 v189, v125, s58, v234\n"
    "v_exp_f32 v186, v186\n"
    "v_add_f32 v239, v239, v199\n"
    "v_cvt_pk_bf16_f32 v171, v198, v199\n"
    "v_fma_f32 v190, v126, s58, v234\n"
    "v_exp_f32_e64 v187, v187\n"
    "v_fma_f32 v191, v127, s58, v234\n"
    "v_exp_f32 v188, v188\n"
    "v_add_f32 v240, v184, v185\n"
    "v_cvt_pk_bf16_f32 v172, v184, v185\n"
    "v_fma_f32 v192, v128, s58, v232\n"
    "v_exp_f32 v189, v189\n"
    "v_add_f32 v240, v240, v186\n"
    "v_fma_f32 v193, v129, s58, v232\n"
    "v_exp_f32 v190, v190\n"
    "v_add_f32 v240, v240, v187\n"
    "v_cvt_pk_bf16_f32 v173, v186, v187\n"
    "v_fma_f32 v194, v130, s58, v232\n"
    "v_exp_f32 v191, v191\n"
    "v_add_f32 v240, v240, v188\n"
    "v_fma_f32 v195, v131, s58, v232\n"
    "v_exp_f32 v192, v192\n"
    "v_add_f32 v240, v240, v189\n"
    "v_cvt_pk_bf16_f32 v174, v188, v189\n"
    "v_fma_f32 v196, v136, s58, v232\n"
    "v_exp_f32 v193, v193\n"
    "v_add_f32 v240, v240, v190\n"
    "v_fma_f32 v197, v137, s58, v232\n"
    "v_exp_f32 v194, v194\n"
    "v_add_f32 v240, v240, v191\n"
    "v_cvt_pk_bf16_f32 v175, v190, v191\n"
    "v_fma_f32 v198, v138, s58, v232\n"
    "v_exp_f32 v195, v195\n"
    "v_add_f32 v238, v238, v192\n"
    "v_fma_f32 v199, v139, s58, v232\n"
    "v_exp_f32 v196, v196\n"
    "v_add_f32 v238, v238, v193\n"
    "v_cvt_pk_bf16_f32 v176, v192, v193\n"
    "v_fma_f32 v184, v132, s58, v233\n"
    "v_exp_f32 v197, v197\n"
    "v_add_f32 v238, v238, v194\n"
    "v_fma_f32 v185, v133, s58, v233\n"
    "v_exp_f32 v198, v198\n"
    "v_add_f32 v238, v238, v195\n"
    "v_cvt_pk_bf16_f32 v177, v194, v195\n"
    "v_fma_f32 v186, v134, s58, v233\n"
    "v_exp_f32 v199, v199\n"
    "v_add_f32 v238, v238, v196\n"
    "v_fma_f32 v187, v135, s58, v233\n"
    "v_exp_f32 v184, v184\n"
    "v_add_f32 v238, v238, v197\n"
    "v_cvt_pk_bf16_f32 v178, v196, v197\n"
    "v_fma_f32 v188, v140, s58, v233\n"
    "v_exp_f32 v185, v185\n"
    "v_add_f32 v238, v238, v198\n"
    "v_fma_f32 v189, v141, s58, v233\n"
    "v_exp_f32 v186, v186\n"
    "v_add_f32 v238, v238, v199\n"
    "v_cvt_pk_bf16_f32 v179, v198, v199\n"
    "v_fma_f32 v190, v142, s58, v233\n"
    "v_exp_f32 v187, v187\n"
    "v_add_f32 v239, v239, v184\n"
    "v_fma_f32 v191, v143, s58, v233\n"
    "v_exp_f32 v188, v188\n"
    "v_add_f32 v239, v239, v185\n"
    "v_cvt_pk_bf16_f32 v180, v184, v185\n"
    "v_exp_f32 v189, v189\n"
    "v_add_f32 v239, v239, v186\n"
    "v_exp_f32 v190, v190\n"
    "v_add_f32 v239, v239, v187\n"
    "v_cvt_pk_bf16_f32 v181, v186, v187\n"
    "v_exp_f32 v191, v191\n"
    "v_add_f32 v239, v239, v188\n"
    "v_add_f32_e64 v239, v239, v189\n"
    "v_cvt_pk_bf16_f32 v182, v188, v189\n"
    "v_add_f32_e64 v239, v239, v190\n"
    "v_add_f32 v239, v239, v191\n"
    "v_cvt_pk_bf16_f32 v183, v190, v191\n"
    "s_nop 3\n"
    "s_branch LbackP14t_%=\n"
    "LrareP14f_%=:\n"
    "s_nop 7\n"
    "v_max3_f32 v243, v104, v105, v106\n"
    "v_max3_f32 v243, v243, v107, v116\n"
    "v_max3_f32 v243, v243, v117, v118\n"
    "v_max3_f32 v243, v243, v119, v128\n"
    "v_max3_f32 v243, v243, v129, v130\n"
    "v_max3_f32 v243, v243, v131, v136\n"
    "v_max3_f32 v243, v243, v137, v138\n"
    "v_max_f32 v243, v243, v139\n"
    "v_mov_b32 v245, v243\n"
    "s_nop 1\n"
    "v_permlane32_swap_b32 v243, v245\n"
    "v_max_f32 v243, v243, v245\n"
    "v_mov_b32 v245, v243\n"
    "s_nop 1\n"
    "v_permlane16_swap_b32 v243, v245\n"
    "v_max_f32 v243, v243, v245\n"
    "v_mul_f32 v243, s64, v243\n"
    "v_min_f32 v243, v243, v232\n"
    "v_sub_f32 v245, v243, v232\n"
    "v_exp_f32 v245, v245\n"
    "v_mov_b32 v232, v243\n"
    "v_mul_f32_e64 v236, v236, v245\n"
    "v_accvgpr_read_b32 v184, a0\n"
    "v_accvgpr_read_b32 v185, a1\n"
    "v_accvgpr_read_b32 v186, a2\n"
    "v_accvgpr_read_b32 v187, a3\n"
    "v_mul_f32 v184, v184, v245\n"
    "v_mul_f32 v185, v185, v245\n"
    "v_mul_f32 v186, v186, v245\n"
    "v_mul_f32 v187, v187, v245\n"
    "v_accvgpr_write_b32 a0, v184\n"
    "v_accvgpr_write_b32 a1, v185\n"
    "v_accvgpr_write_b32 a2, v186\n"
    "v_accvgpr_write_b32 a3, v187\n"
    "v_accvgpr_read_b32 v184, a4\n"
    "v_accvgpr_read_b32 v185, a5\n"
    "v_accvgpr_read_b32 v186, a6\n"
    "v_accvgpr_read_b32 v187, a7\n"
    "v_mul_f32 v184, v184, v245\n"
    "v_mul_f32 v185, v185, v245\n"
    "v_mul_f32 v186, v186, v245\n"
    "v_mul_f32 v187, v187, v245\n"
    "v_accvgpr_write_b32 a4, v184\n"
    "v_accvgpr_write_b32 a5, v185\n"
    "v_accvgpr_write_b32 a6, v186\n"
    "v_accvgpr_write_b32 a7, v187\n"
    "v_accvgpr_read_b32 v184, a8\n"
    "v_accvgpr_read_b32 v185, a9\n"
    "v_accvgpr_read_b32 v186, a10\n"
    "v_accvgpr_read_b32 v187, a11\n"
    "v_mul_f32 v184, v184, v245\n"
    "v_mul_f32 v185, v185, v245\n"
    "v_mul_f32 v186, v186, v245\n"
    "v_mul_f32 v187, v187, v245\n"
    "v_accvgpr_write_b32 a8, v184\n"
    "v_accvgpr_write_b32 a9, v185\n"
    "v_accvgpr_write_b32 a10, v186\n"
    "v_accvgpr_write_b32 a11, v187\n"
    "v_accvgpr_read_b32 v184, a12\n"
    "v_accvgpr_read_b32 v185, a13\n"
    "v_accvgpr_read_b32 v186, a14\n"
    "v_accvgpr_read_b32 v187, a15\n"
    "v_mul_f32 v184, v184, v245\n"
    "v_mul_f32 v185, v185, v245\n"
    "v_mul_f32 v186, v186, v245\n"
    "v_mul_f32 v187, v187, v245\n"
    "v_accvgpr_write_b32 a12, v184\n"
    "v_accvgpr_write_b32 a13, v185\n"
    "v_accvgpr_write_b32 a14, v186\n"
    "v_accvgpr_write_b32 a15, v187\n"
    "v_accvgpr_read_b32 v184, a16\n"
    "v_accvgpr_read_b32 v185, a17\n"
    "v_accvgpr_read_b32 v186, a18\n"
    "v_accvgpr_read_b32 v187, a19\n"
    "v_mul_f32 v184, v184, v245\n"
    "v_mul_f32 v185, v185, v245\n"
    "v_mul_f32 v186, v186, v245\n"
    "v_mul_f32 v187, v187, v245\n"
    "v_accvgpr_write_b32 a16, v184\n"
    "v_accvgpr_write_b32 a17, v185\n"
    "v_accvgpr_write_b32 a18, v186\n"
    "v_accvgpr_write_b32 a19, v187\n"
    "v_accvgpr_read_b32 v184, a20\n"
    "v_accvgpr_read_b32 v185, a21\n"
    "v_accvgpr_read_b32 v186, a22\n"
    "v_accvgpr_read_b32 v187, a23\n"
    "v_mul_f32 v184, v184, v245\n"
    "v_mul_f32 v185, v185, v245\n"
    "v_mul_f32 v186, v186, v245\n"
    "v_mul_f32 v187, v187, v245\n"
    "v_accvgpr_write_b32 a20, v184\n"
    "v_accvgpr_write_b32 a21, v185\n"
    "v_accvgpr_write_b32 a22, v186\n"
    "v_accvgpr_write_b32 a23, v187\n"
    "v_accvgpr_read_b32 v184, a24\n"
    "v_accvgpr_read_b32 v185, a25\n"
    "v_accvgpr_read_b32 v186, a26\n"
    "v_accvgpr_read_b32 v187, a27\n"
    "v_mul_f32 v184, v184, v245\n"
    "v_mul_f32 v185, v185, v245\n"
    "v_mul_f32 v186, v186, v245\n"
    "v_mul_f32 v187, v187, v245\n"
    "v_accvgpr_write_b32 a24, v184\n"
    "v_accvgpr_write_b32 a25, v185\n"
    "v_accvgpr_write_b32 a26, v186\n"
    "v_accvgpr_write_b32 a27, v187\n"
    "v_accvgpr_read_b32 v184, a28\n"
    "v_accvgpr_read_b32 v185, a29\n"
    "v_accvgpr_read_b32 v186, a30\n"
    "v_accvgpr_read_b32 v187, a31\n"
    "v_mul_f32 v184, v184, v245\n"
    "v_mul_f32 v185, v185, v245\n"
    "v_mul_f32 v186, v186, v245\n"
    "v_mul_f32 v187, v187, v245\n"
    "v_accvgpr_write_b32 a28, v184\n"
    "v_accvgpr_write_b32 a29, v185\n"
    "v_accvgpr_write_b32 a30, v186\n"
    "v_accvgpr_write_b32 a31, v187\n"
    "v_accvgpr_read_b32 v184, a32\n"
    "v_accvgpr_read_b32 v185, a33\n"
    "v_accvgpr_read_b32 v186, a34\n"
    "v_accvgpr_read_b32 v187, a35\n"
    "v_mul_f32 v184, v184, v245\n"
    "v_mul_f32 v185, v185, v245\n"
    "v_mul_f32 v186, v186, v245\n"
    "v_mul_f32 v187, v187, v245\n"
    "v_accvgpr_write_b32 a32, v184\n"
    "v_accvgpr_write_b32 a33, v185\n"
    "v_accvgpr_write_b32 a34, v186\n"
    "v_accvgpr_write_b32 a35, v187\n"
    "v_accvgpr_read_b32 v184, a36\n"
    "v_accvgpr_read_b32 v185, a37\n"
    "v_accvgpr_read_b32 v186, a38\n"
    "v_accvgpr_read_b32 v187, a39\n"
    "v_mul_f32 v184, v184, v245\n"
    "v_mul_f32 v185, v185, v245\n"
    "v_mul_f32 v186, v186, v245\n"
    "v_mul_f32 v187, v187, v245\n"
    "v_accvgpr_write_b32 a36, v184\n"
    "v_accvgpr_write_b32 a37, v185\n"
    "v_accvgpr_write_b32 a38, v186\n"
    "v_accvgpr_write_b32 a39, v187\n"
    "v_accvgpr_read_b32 v184, a40\n"
    "v_accvgpr_read_b32 v185, a41\n"
    "v_accvgpr_read_b32 v186, a42\n"
    "v_accvgpr_read_b32 v187, a43\n"
    "v_mul_f32 v184, v184, v245\n"
    "v_mul_f32 v185, v185, v245\n"
    "v_mul_f32 v186, v186, v245\n"
    "v_mul_f32 v187, v187, v245\n"
    "v_accvgpr_write_b32 a40, v184\n"
    "v_accvgpr_write_b32 a41, v185\n"
    "v_accvgpr_write_b32 a42, v186\n"
    "v_accvgpr_write_b32 a43, v187\n"
    "v_accvgpr_read_b32 v184, a44\n"
    "v_accvgpr_read_b32 v185, a45\n"
    "v_accvgpr_read_b32 v186, a46\n"
    "v_accvgpr_read_b32 v187, a47\n"
    "v_mul_f32 v184, v184, v245\n"
    "v_mul_f32 v185, v185, v245\n"
    "v_mul_f32 v186, v186, v245\n"
    "v_mul_f32 v187, v187, v245\n"
    "v_accvgpr_write_b32 a44, v184\n"
    "v_accvgpr_write_b32 a45, v185\n"
    "v_accvgpr_write_b32 a46, v186\n"
    "v_accvgpr_write_b32 a47, v187\n"
    "v_accvgpr_read_b32 v184, a48\n"
    "v_accvgpr_read_b32 v185, a49\n"
    "v_accvgpr_read_b32 v186, a50\n"
    "v_accvgpr_read_b32 v187, a51\n"
    "v_mul_f32 v184, v184, v245\n"
    "v_mul_f32 v185, v185, v245\n"
    "v_mul_f32 v186, v186, v245\n"
    "v_mul_f32 v187, v187, v245\n"
    "v_accvgpr_write_b32 a48, v184\n"
    "v_accvgpr_write_b32 a49, v185\n"
    "v_accvgpr_write_b32 a50, v186\n"
    "v_accvgpr_write_b32 a51, v187\n"
    "v_accvgpr_read_b32 v184, a52\n"
    "v_accvgpr_read_b32 v185, a53\n"
    "v_accvgpr_read_b32 v186, a54\n"
    "v_accvgpr_read_b32 v187, a55\n"
    "v_mul_f32 v184, v184, v245\n"
    "v_mul_f32 v185, v185, v245\n"
    "v_mul_f32 v186, v186, v245\n"
    "v_mul_f32 v187, v187, v245\n"
    "v_accvgpr_write_b32 a52, v184\n"
    "v_accvgpr_write_b32 a53, v185\n"
    "v_accvgpr_write_b32 a54, v186\n"
    "v_accvgpr_write_b32 a55, v187\n"
    "v_accvgpr_read_b32 v184, a56\n"
    "v_accvgpr_read_b32 v185, a57\n"
    "v_accvgpr_read_b32 v186, a58\n"
    "v_accvgpr_read_b32 v187, a59\n"
    "v_mul_f32 v184, v184, v245\n"
    "v_mul_f32 v185, v185, v245\n"
    "v_mul_f32 v186, v186, v245\n"
    "v_mul_f32 v187, v187, v245\n"
    "v_accvgpr_write_b32 a56, v184\n"
    "v_accvgpr_write_b32 a57, v185\n"
    "v_accvgpr_write_b32 a58, v186\n"
    "v_accvgpr_write_b32 a59, v187\n"
    "v_accvgpr_read_b32 v184, a60\n"
    "v_accvgpr_read_b32 v185, a61\n"
    "v_accvgpr_read_b32 v186, a62\n"
    "v_accvgpr_read_b32 v187, a63\n"
    "v_mul_f32 v184, v184, v245\n"
    "v_mul_f32 v185, v185, v245\n"
    "v_mul_f32 v186, v186, v245\n"
    "v_mul_f32 v187, v187, v245\n"
    "v_accvgpr_write_b32 a60, v184\n"
    "v_accvgpr_write_b32 a61, v185\n"
    "v_accvgpr_write_b32 a62, v186\n"
    "v_accvgpr_write_b32 a63, v187\n"
    "v_max3_f32 v243, v108, v109, v110\n"
    "v_max3_f32 v243, v243, v111, v120\n"
    "v_max3_f32 v243, v243, v121, v122\n"
    "v_max3_f32 v243, v243, v123, v132\n"
    "v_max3_f32 v243, v243, v133, v134\n"
    "v_max3_f32 v243, v243, v135, v140\n"
    "v_max3_f32 v243, v243, v141, v142\n"
    "v_max_f32 v243, v243, v143\n"
    "v_mov_b32 v245, v243\n"
    "s_nop 1\n"
    "v_permlane32_swap_b32 v243, v245\n"
    "v_max_f32 v243, v243, v245\n"
    "v_mov_b32 v245, v243\n"
    "s_nop 1\n"
    "v_permlane16_swap_b32 v243, v245\n"
    "v_max_f32 v243, v243, v245\n"
    "v_mul_f32 v243, s64, v243\n"
    "v_min_f32 v243, v243, v233\n"
    "v_sub_f32 v245, v243, v233\n"
    "v_exp_f32 v245, v245\n"
    "v_mov_b32 v233, v243\n"
    "v_mul_f32_e64 v237, v237, v245\n"
    "v_accvgpr_read_b32 v184, a64\n"
    "v_accvgpr_read_b32 v185, a65\n"
    "v_accvgpr_read_b32 v186, a66\n"
    "v_accvgpr_read_b32 v187, a67\n"
    "v_mul_f32 v184, v184, v245\n"
    "v_mul_f32 v185, v185, v245\n"
    "v_mul_f32 v186, v186, v245\n"
    "v_mul_f32 v187, v187, v245\n"
    "v_accvgpr_write_b32 a64, v184\n"
    "v_accvgpr_write_b32 a65, v185\n"
    "v_accvgpr_write_b32 a66, v186\n"
    "v_accvgpr_write_b32 a67, v187\n"
    "v_accvgpr_read_b32 v184, a68\n"
    "v_accvgpr_read_b32 v185, a69\n"
    "v_accvgpr_read_b32 v186, a70\n"
    "v_accvgpr_read_b32 v187, a71\n"
    "v_mul_f32 v184, v184, v245\n"
    "v_mul_f32 v185, v185, v245\n"
    "v_mul_f32 v186, v186, v245\n"
    "v_mul_f32 v187, v187, v245\n"
    "v_accvgpr_write_b32 a68, v184\n"
    "v_accvgpr_write_b32 a69, v185\n"
    "v_accvgpr_write_b32 a70, v186\n"
    "v_accvgpr_write_b32 a71, v187\n"
    "v_accvgpr_read_b32 v184, a72\n"
    "v_accvgpr_read_b32 v185, a73\n"
    "v_accvgpr_read_b32 v186, a74\n"
    "v_accvgpr_read_b32 v187, a75\n"
    "v_mul_f32 v184, v184, v245\n"
    "v_mul_f32 v185, v185, v245\n"
    "v_mul_f32 v186, v186, v245\n"
    "v_mul_f32 v187, v187, v245\n"
    "v_accvgpr_write_b32 a72, v184\n"
    "v_accvgpr_write_b32 a73, v185\n"
    "v_accvgpr_write_b32 a74, v186\n"
    "v_accvgpr_write_b32 a75, v187\n"
    "v_accvgpr_read_b32 v184, a76\n"
    "v_accvgpr_read_b32 v185, a77\n"
    "v_accvgpr_read_b32 v186, a78\n"
    "v_accvgpr_read_b32 v187, a79\n"
    "v_mul_f32 v184, v184, v245\n"
    "v_mul_f32 v185, v185, v245\n"
    "v_mul_f32 v186, v186, v245\n"
    "v_mul_f32 v187, v187, v245\n"
    "v_accvgpr_write_b32 a76, v184\n"
    "v_accvgpr_write_b32 a77, v185\n"
    "v_accvgpr_write_b32 a78, v186\n"
    "v_accvgpr_write_b32 a79, v187\n"
    "v_accvgpr_read_b32 v184, a80\n"
    "v_accvgpr_read_b32 v185, a81\n"
    "v_accvgpr_read_b32 v186, a82\n"
    "v_accvgpr_read_b32 v187, a83\n"
    "v_mul_f32 v184, v184, v245\n"
    "v_mul_f32 v185, v185, v245\n"
    "v_mul_f32 v186, v186, v245\n"
    "v_mul_f32 v187, v187, v245\n"
    "v_accvgpr_write_b32 a80, v184\n"
    "v_accvgpr_write_b32 a81, v185\n"
    "v_accvgpr_write_b32 a82, v186\n"
    "v_accvgpr_write_b32 a83, v187\n"
    "v_accvgpr_read_b32 v184, a84\n"
    "v_accvgpr_read_b32 v185, a85\n"
    "v_accvgpr_read_b32 v186, a86\n"
    "v_accvgpr_read_b32 v187, a87\n"
    "v_mul_f32 v184, v184, v245\n"
    "v_mul_f32 v185, v185, v245\n"
    "v_mul_f32 v186, v186, v245\n"
    "v_mul_f32 v187, v187, v245\n"
    "v_accvgpr_write_b32 a84, v184\n"
    "v_accvgpr_write_b32 a85, v185\n"
    "v_accvgpr_write_b32 a86, v186\n"
    "v_accvgpr_write_b32 a87, v187\n"
    "v_accvgpr_read_b32 v184, a88\n"
    "v_accvgpr_read_b32 v185, a89\n"
    "v_accvgpr_read_b32 v186, a90\n"
    "v_accvgpr_read_b32 v187, a91\n"
    "v_mul_f32 v184, v184, v245\n"
    "v_mul_f32 v185, v185, v245\n"
    "v_mul_f32 v186, v186, v245\n"
    "v_mul_f32 v187, v187, v245\n"
    "v_accvgpr_write_b32 a88, v184\n"
    "v_accvgpr_write_b32 a89, v185\n"
    "v_accvgpr_write_b32 a90, v186\n"
    "v_accvgpr_write_b32 a91, v187\n"
    "v_accvgpr_read_b32 v184, a92\n"
    "v_accvgpr_read_b32 v185, a93\n"
    "v_accvgpr_read_b32 v186, a94\n"
    "v_accvgpr_read_b32 v187, a95\n"
    "v_mul_f32 v184, v184, v245\n"
    "v_mul_f32 v185, v185, v245\n"
    "v_mul_f32 v186, v186, v245\n"
    "v_mul_f32 v187, v187, v245\n"
    "v_accvgpr_write_b32 a92, v184\n"
    "v_accvgpr_write_b32 a93, v185\n"
    "v_accvgpr_write_b32 a94, v186\n"
    "v_accvgpr_write_b32 a95, v187\n"
    "v_accvgpr_read_b32 v184, a96\n"
    "v_accvgpr_read_b32 v185, a97\n"
    "v_accvgpr_read_b32 v186, a98\n"
    "v_accvgpr_read_b32 v187, a99\n"
    "v_mul_f32 v184, v184, v245\n"
    "v_mul_f32 v185, v185, v245\n"
    "v_mul_f32 v186, v186, v245\n"
    "v_mul_f32 v187, v187, v245\n"
    "v_accvgpr_write_b32 a96, v184\n"
    "v_accvgpr_write_b32 a97, v185\n"
    "v_accvgpr_write_b32 a98, v186\n"
    "v_accvgpr_write_b32 a99, v187\n"
    "v_accvgpr_read_b32 v184, a100\n"
    "v_accvgpr_read_b32 v185, a101\n"
    "v_accvgpr_read_b32 v186, a102\n"
    "v_accvgpr_read_b32 v187, a103\n"
    "v_mul_f32 v184, v184, v245\n"
    "v_mul_f32 v185, v185, v245\n"
    "v_mul_f32 v186, v186, v245\n"
    "v_mul_f32 v187, v187, v245\n"
    "v_accvgpr_write_b32 a100, v184\n"
    "v_accvgpr_write_b32 a101, v185\n"
    "v_accvgpr_write_b32 a102, v186\n"
    "v_accvgpr_write_b32 a103, v187\n"
    "v_accvgpr_read_b32 v184, a104\n"
    "v_accvgpr_read_b32 v185, a105\n"
    "v_accvgpr_read_b32 v186, a106\n"
    "v_accvgpr_read_b32 v187, a107\n"
    "v_mul_f32 v184, v184, v245\n"
    "v_mul_f32 v185, v185, v245\n"
    "v_mul_f32 v186, v186, v245\n"
    "v_mul_f32 v187, v187, v245\n"
    "v_accvgpr_write_b32 a104, v184\n"
    "v_accvgpr_write_b32 a105, v185\n"
    "v_accvgpr_write_b32 a106, v186\n"
    "v_accvgpr_write_b32 a107, v187\n"
    "v_accvgpr_read_b32 v184, a108\n"
    "v_accvgpr_read_b32 v185, a109\n"
    "v_accvgpr_read_b32 v186, a110\n"
    "v_accvgpr_read_b32 v187, a111\n"
    "v_mul_f32 v184, v184, v245\n"
    "v_mul_f32 v185, v185, v245\n"
    "v_mul_f32 v186, v186, v245\n"
    "v_mul_f32 v187, v187, v245\n"
    "v_accvgpr_write_b32 a108, v184\n"
    "v_accvgpr_write_b32 a109, v185\n"
    "v_accvgpr_write_b32 a110, v186\n"
    "v_accvgpr_write_b32 a111, v187\n"
    "v_accvgpr_read_b32 v184, a112\n"
    "v_accvgpr_read_b32 v185, a113\n"
    "v_accvgpr_read_b32 v186, a114\n"
    "v_accvgpr_read_b32 v187, a115\n"
    "v_mul_f32 v184, v184, v245\n"
    "v_mul_f32 v185, v185, v245\n"
    "v_mul_f32 v186, v186, v245\n"
    "v_mul_f32 v187, v187, v245\n"
    "v_accvgpr_write_b32 a112, v184\n"
    "v_accvgpr_write_b32 a113, v185\n"
    "v_accvgpr_write_b32 a114, v186\n"
    "v_accvgpr_write_b32 a115, v187\n"
    "v_accvgpr_read_b32 v184, a116\n"
    "v_accvgpr_read_b32 v185, a117\n"
    "v_accvgpr_read_b32 v186, a118\n"
    "v_accvgpr_read_b32 v187, a119\n"
    "v_mul_f32 v184, v184, v245\n"
    "v_mul_f32 v185, v185, v245\n"
    "v_mul_f32 v186, v186, v245\n"
    "v_mul_f32 v187, v187, v245\n"
    "v_accvgpr_write_b32 a116, v184\n"
    "v_accvgpr_write_b32 a117, v185\n"
    "v_accvgpr_write_b32 a118, v186\n"
    "v_accvgpr_write_b32 a119, v187\n"
    "v_accvgpr_read_b32 v184, a120\n"
    "v_accvgpr_read_b32 v185, a121\n"
    "v_accvgpr_read_b32 v186, a122\n"
    "v_accvgpr_read_b32 v187, a123\n"
    "v_mul_f32 v184, v184, v245\n"
    "v_mul_f32 v185, v185, v245\n"
    "v_mul_f32 v186, v186, v245\n"
    "v_mul_f32 v187, v187, v245\n"
    "v_accvgpr_write_b32 a120, v184\n"
    "v_accvgpr_write_b32 a121, v185\n"
    "v_accvgpr_write_b32 a122, v186\n"
    "v_accvgpr_write_b32 a123, v187\n"
    "v_accvgpr_read_b32 v184, a124\n"
    "v_accvgpr_read_b32 v185, a125\n"
    "v_accvgpr_read_b32 v186, a126\n"
    "v_accvgpr_read_b32 v187, a127\n"
    "v_mul_f32 v184, v184, v245\n"
    "v_mul_f32 v185, v185, v245\n"
    "v_mul_f32 v186, v186, v245\n"
    "v_mul_f32 v187, v187, v245\n"
    "v_accvgpr_write_b32 a124, v184\n"
    "v_accvgpr_write_b32 a125, v185\n"
    "v_accvgpr_write_b32 a126, v186\n"
    "v_accvgpr_write_b32 a127, v187\n"
    "v_max3_f32 v243, v112, v113, v114\n"
    "v_max3_f32 v243, v243, v115, v124\n"
    "v_max3_f32 v243, v243, v125, v126\n"
    "v_max_f32 v243, v243, v127\n"
    "v_mov_b32 v245, v243\n"
    "s_nop 1\n"
    "v_permlane32_swap_b32 v243, v245\n"
    "v_max_f32 v243, v243, v245\n"
    "v_mov_b32 v245, v243\n"
    "s_nop 1\n"
    "v_permlane16_swap_b32 v243, v245\n"
    "v_max_f32 v243, v243, v245\n"
    "v_mul_f32 v243, s64, v243\n"
    "v_min_f32 v243, v243, v234\n"
    "v_sub_f32 v245, v243, v234\n"
    "v_exp_f32 v245, v245\n"
    "v_mov_b32 v234, v243\n"
    "v_mul_f32 v235, v235, v245\n"
    "v_mul_f32 v0, v0, v245\n"
    "v_mul_f32 v1, v1, v245\n"
    "v_mul_f32 v2, v2, v245\n"
    "v_mul_f32 v3, v3, v245\n"
    "v_mul_f32 v4, v4, v245\n"
    "v_mul_f32 v5, v5, v245\n"
    "v_mul_f32 v6, v6, v245\n"
    "v_mul_f32 v7, v7, v245\n"
    "v_mul_f32 v8, v8, v245\n"
    "v_mul_f32 v9, v9, v245\n"
    "v_mul_f32 v10, v10, v245\n"
    "v_mul_f32 v11, v11, v245\n"
    "v_mul_f32 v12, v12, v245\n"
    "v_mul_f32 v13, v13, v245\n"
    "v_mul_f32 v14, v14, v245\n"
    "v_mul_f32 v15, v15, v245\n"
    "v_mul_f32 v16, v16, v245\n"
    "v_mul_f32 v17, v17, v245\n"
    "v_mul_f32 v18, v18, v245\n"
    "v_mul_f32 v19, v19, v245\n"
    "v_mul_f32 v20, v20, v245\n"
    "v_mul_f32 v21, v21, v245\n"
    "v_mul_f32 v22, v22, v245\n"
    "v_mul_f32 v23, v23, v245\n"
    "v_mul_f32 v24, v24, v245\n"
    "v_mul_f32 v25, v25, v245\n"
    "v_mul_f32 v26, v26, v245\n"
    "v_mul_f32 v27, v27, v245\n"
    "v_mul_f32 v28, v28, v245\n"
    "v_mul_f32 v29, v29, v245\n"
    "v_mul_f32 v30, v30, v245\n"
    "v_mul_f32 v31, v31, v245\n"
    "v_mul_f32 v32, v32, v245\n"
    "v_mul_f32 v33, v33, v245\n"
    "v_mul_f32 v34, v34, v245\n"
    "v_mul_f32 v35, v35, v245\n"
    "v_mul_f32 v36, v36, v245\n"
    "v_mul_f32 v37, v37, v245\n"
    "v_mul_f32 v38, v38, v245\n"
    "v_mul_f32 v39, v39, v245\n"
    "v_mul_f32 v40, v40, v245\n"
    "v_mul_f32 v41, v41, v245\n"
    "v_mul_f32 v42, v42, v245\n"
    "v_mul_f32 v43, v43, v245\n"
    "v_mul_f32 v44, v44, v245\n"
    "v_mul_f32 v45, v45, v245\n"
    "v_mul_f32 v46, v46, v245\n"
    "v_mul_f32 v47, v47, v245\n"
    "v_mul_f32 v48, v48, v245\n"
    "v_mul_f32 v49, v49, v245\n"
    "v_mul_f32 v50, v50, v245\n"
    "v_mul_f32 v51, v51, v245\n"
    "v_mul_f32 v52, v52, v245\n"
    "v_mul_f32 v53, v53, v245\n"
    "v_mul_f32 v54, v54, v245\n"
    "v_mul_f32 v55, v55, v245\n"
    "v_mul_f32 v56, v56, v245\n"
    "v_mul_f32 v57, v57, v245\n"
    "v_mul_f32 v58, v58, v245\n"
    "v_mul_f32 v59, v59, v245\n"
    "v_mul_f32 v60, v60, v245\n"
    "v_mul_f32 v61, v61, v245\n"
    "v_mul_f32 v62, v62, v245\n"
    "v_mul_f32_e64 v63, v63, v245\n"
    "v_fma_f32 v184, v104, s58, v232\n"
    "v_fma_f32 v185, v105, s58, v232\n"
    "v_fma_f32 v186, v106, s58, v232\n"
    "v_fma_f32 v187, v107, s58, v232\n"
    "v_exp_f32_e64 v184, v184\n"
    "v_fma_f32 v188, v116, s58, v232\n"
    "v_exp_f32_e64 v185, v185\n"
    "v_fma_f32 v189, v117, s58, v232\n"
    "v_exp_f32_e64 v186, v186\n"
    "v_fma_f32 v190, v118, s58, v232\n"
    "v_exp_f32_e64 v187, v187\n"
    "v_fma_f32 v191, v119, s58, v232\n"
    "v_exp_f32 v188, v188\n"
    "v_add_f32 v238, v184, v185\n"
    "v_cvt_pk_bf16_f32 v164, v184, v185\n"
    "v_fma_f32 v192, v108, s58, v233\n"
    "v_exp_f32 v189, v189\n"
    "v_add_f32 v238, v238, v186\n"
    "v_fma_f32 v193, v109, s58, v233\n"
    "v_exp_f32 v190, v190\n"
    "v_add_f32 v238, v238, v187\n"
    "v_cvt_pk_bf16_f32 v165, v186, v187\n"
    "v_fma_f32 v194, v110, s58, v233\n"
    "v_exp_f32 v191, v191\n"
    "v_add_f32 v238, v238, v188\n"
    "v_fma_f32 v195, v111, s58, v233\n"
    "v_exp_f32 v192, v192\n"
    "v_add_f32 v238, v238, v189\n"
    "v_cvt_pk_bf16_f32 v166, v188, v189\n"
    "v_fma_f32 v196, v120, s58, v233\n"
    "v_exp_f32 v193, v193\n"
    "v_add_f32 v238, v238, v190\n"
    "v_fma_f32 v197, v121, s58, v233\n"
    "v_exp_f32 v194, v194\n"
    "v_add_f32 v238, v238, v191\n"
    "v_cvt_pk_bf16_f32 v167, v190, v191\n"
    "v_fma_f32 v198, v122, s58, v233\n"
    "v_exp_f32_e64 v195, v195\n"
    "v_fma_f32 v199, v123, s58, v233\n"
    "v_exp_f32 v196, v196\n"
    "v_add_f32 v239, v192, v193\n"
    "v_cvt_pk_bf16_f32 v168, v192, v193\n"
    "v_fma_f32 v184, v112, s58, v234\n"
    "v_exp_f32 v197, v197\n"
    "v_add_f32 v239, v239, v194\n"
    "v_fma_f32 v185, v113, s58, v234\n"
    "v_exp_f32 v198, v198\n"
    "v_add_f32 v239, v239, v195\n"
    "v_cvt_pk_bf16_f32 v169, v194, v195\n"
    "v_fma_f32 v186, v114, s58, v234\n"
    "v_exp_f32 v199, v199\n"
    "v_add_f32 v239, v239, v196\n"
    "v_fma_f32 v187, v115, s58, v234\n"
    "v_exp_f32 v184, v184\n"
    "v_add_f32 v239, v239, v197\n"
    "v_cvt_pk_bf16_f32 v170, v196, v197\n"
    "v_fma_f32 v188, v124, s58, v234\n"
    "v_exp_f32 v185, v185\n"
    "v_add_f32 v239, v239, v198\n"
    "v_fma_f32 v189, v125, s58, v234\n"
    "v_exp_f32 v186, v186\n"
    "v_add_f32 v239, v239, v199\n"
    "v_cvt_pk_bf16_f32 v171, v198, v199\n"
    "v_fma_f32 v190, v126, s58, v234\n"
    "v_exp_f32_e64 v187, v187\n"
    "v_fma_f32 v191, v127, s58, v234\n"
    "v_exp_f32 v188, v188\n"
    "v_add_f32 v240, v184, v185\n"
    "v_cvt_pk_bf16_f32 v172, v184, v185\n"
    "v_fma_f32 v192, v128, s58, v232\n"
    "v_exp_f32 v189, v189\n"
    "v_add_f32 v240, v240, v186\n"
    "v_fma_f32 v193, v129, s58, v232\n"
    "v_exp_f32 v190, v190\n"
    "v_add_f32 v240, v240, v187\n"
    "v_cvt_pk_bf16_f32 v173, v186, v187\n"
    "v_fma_f32 v194, v130, s58, v232\n"
    "v_exp_f32 v191, v191\n"
    "v_add_f32 v240, v240, v188\n"
    "v_fma_f32 v195, v131, s58, v232\n"
    "v_exp_f32 v192, v192\n"
    "v_add_f32 v240, v240, v189\n"
    "v_cvt_pk_bf16_f32 v174, v188, v189\n"
    "v_fma_f32 v196, v136, s58, v232\n"
    "v_exp_f32 v193, v193\n"
    "v_add_f32 v240, v240, v190\n"
    "v_fma_f32 v197, v137, s58, v232\n"
    "v_exp_f32 v194, v194\n"
    "v_add_f32 v240, v240, v191\n"
    "v_cvt_pk_bf16_f32 v175, v190, v191\n"
    "v_fma_f32 v198, v138, s58, v232\n"
    "v_exp_f32 v195, v195\n"
    "v_add_f32 v238, v238, v192\n"
    "v_fma_f32 v199, v139, s58, v232\n"
    "v_exp_f32 v196, v196\n"
    "v_add_f32 v238, v238, v193\n"
    "v_cvt_pk_bf16_f32 v176, v192, v193\n"
    "v_fma_f32 v184, v132, s58, v233\n"
    "v_exp_f32 v197, v197\n"
    "v_add_f32 v238, v238, v194\n"
    "v_fma_f32 v185, v133, s58, v233\n"
    "v_exp_f32 v198, v198\n"
    "v_add_f32 v238, v238, v195\n"
    "v_cvt_pk_bf16_f32 v177, v194, v195\n"
    "v_fma_f32 v186, v134, s58, v233\n"
    "v_exp_f32 v199, v199\n"
    "v_add_f32 v238, v238, v196\n"
    "v_fma_f32 v187, v135, s58, v233\n"
    "v_exp_f32 v184, v184\n"
    "v_add_f32 v238, v238, v197\n"
    "v_cvt_pk_bf16_f32 v178, v196, v197\n"
    "v_fma_f32 v188, v140, s58, v233\n"
    "v_exp_f32 v185, v185\n"
    "v_add_f32 v238, v238, v198\n"
    "v_fma_f32 v189, v141, s58, v233\n"
    "v_exp_f32 v186, v186\n"
    "v_add_f32 v238, v238, v199\n"
    "v_cvt_pk_bf16_f32 v179, v198, v199\n"
    "v_fma_f32 v190, v142, s58, v233\n"
    "v_exp_f32 v187, v187\n"
    "v_add_f32 v239, v239, v184\n"
    "v_fma_f32 v191, v143, s58, v233\n"
    "v_exp_f32 v188, v188\n"
    "v_add_f32 v239, v239, v185\n"
    "v_cvt_pk_bf16_f32 v180, v184, v185\n"
    "v_exp_f32 v189, v189\n"
    "v_add_f32 v239, v239, v186\n"
    "v_exp_f32 v190, v190\n"
    "v_add_f32 v239, v239, v187\n"
    "v_cvt_pk_bf16_f32 v181, v186, v187\n"
    "v_exp_f32 v191, v191\n"
    "v_add_f32 v239, v239, v188\n"
    "v_add_f32_e64 v239, v239, v189\n"
    "v_cvt_pk_bf16_f32 v182, v188, v189\n"
    "v_add_f32 v239, v239, v190\n"
    "v_add_f32 v239, v239, v191\n"
    "v_cvt_pk_bf16_f32 v183, v190, v191\n"
    "s_nop 3\n"
    "s_branch LbackP14f_%=\n"
    "Lend_%=:\n"
    :
    : [ka] "s"(__builtin_amdgcn_kernarg_segment_ptr()),
      [wid] "s"(wid), [qd] "s"(qd), [lds] "s"(ldsb), [grp] "s"(grp), [loc] "s"(g >> 5)
    : "v0","v1","v2","v3","v4","v5","v6","v7","v8","v9","v10","v11","v12","v13","v14","v15","v16","v17","v18","v19","v20","v21","v22","v23","v24","v25","v26","v27","v28","v29","v30","v31","v32","v33","v34","v35","v36","v37","v38","v39","v40","v41","v42","v43","v44","v45","v46","v47","v48","v49","v50","v51","v52","v53","v54","v55","v56","v57","v58","v59","v60","v61","v62","v63","v64","v65","v66","v67","v68","v69","v70","v71","v72","v73","v74","v75","v76","v77","v78","v79","v80","v81","v82","v83","v84","v85","v86","v87","v88","v89","v90","v91","v92","v93","v94","v95","v96","v97","v98","v99","v100","v101","v102","v103","v104","v105","v106","v107","v108","v109","v110","v111","v112","v113","v114","v115","v116","v117","v118","v119","v120","v121","v122","v123","v124","v125","v126","v127","v128","v129","v130","v131","v132","v133","v134","v135","v136","v137","v138","v139","v140","v141","v142","v143","v144","v145","v146","v147","v148","v149","v150","v151","v152","v153","v154","v155","v156","v157","v158","v159","v160","v161","v162","v163","v164","v165","v166","v167","v168","v169","v170","v171","v172","v173","v174","v175","v176","v177","v178","v179","v180","v181","v182","v183","v184","v185","v186","v187","v188","v189","v190","v191","v192","v193","v194","v195","v196","v197","v198","v199","v200","v201","v202","v203","v204","v205","v206","v207","v208","v209","v210","v211","v212","v213","v214","v215","v216","v217","v218","v219","v220","v221","v222","v223","v224","v225","v226","v227","v228","v229","v230","v231","v232","v233","v234","v235","v236","v237","v238","v239","v240","v241","v242","v243","v244","v245","v246","v247","v248","v249","v250","v251","v252","v253","v254","v255","a0","a1","a2","a3","a4","a5","a6","a7","a8","a9","a10","a11","a12","a13","a14","a15","a16","a17","a18","a19","a20","a21","a22","a23","a24","a25","a26","a27","a28","a29","a30","a31","a32","a33","a34","a35","a36","a37","a38","a39","a40","a41","a42","a43","a44","a45","a46","a47","a48","a49","a50","a51","a52","a53","a54","a55","a56","a57","a58","a59","a60","a61","a62","a63","a64","a65","a66","a67","a68","a69","a70","a71","a72","a73","a74","a75","a76","a77","a78","a79","a80","a81","a82","a83","a84","a85","a86","a87","a88","a89","a90","a91","a92","a93","a94","a95","a96","a97","a98","a99","a100","a101","a102","a103","a104","a105","a106","a107","a108","a109","a110","a111","a112","a113","a114","a115","a116","a117","a118","a119","a120","a121","a122","a123","a124","a125","a126","a127","a128","a129","a130","a131","a132","a133","a134","a135","a136","a137","a138","a139","a140","a141","a142","a143","a144","a145","a146","a147","a148","a149","a150","a151","a152","a153","a154","a155","a156","a157","a158","a159","a160","a161","a162","a163","a164","a165","a166","a167","a168","a169","a170","a171","a172","a173","a174","a175","a176","a177","a178","a179","a180","a181","a182","a183","a184","a185","a186","a187","a188","a189","a190","a191","a192","a193","a194","a195","a196","a197","a198","a199","a200","a201","a202","a203","a204","a205","a206","a207","a208","a209","a210","a211","a212","a213","a214","a215","a216","a217","a218","a219","a220","a221","a222","a223","a224","a225","a226","a227","a228","a229","a230","a231","a232","a233","a234","a235","a236","a237","a238","a239","a240","a241","a242","a243","a244","a245","a246","a247","a248","a249","a250","a251","a252","a253","a254","a255","s12","s13","s14","s15","s16","s17","s18","s19","s20","s21","s22","s23","s24","s25","s26","s27","s28","s29","s30","s31","s36","s37","s38","s39","s40","s41","s42","s43","s44","s45","s46","s47","s48","s49","s50","s51","s52","s53","s54","s55","s56","s57","s58","s59","s60","s61","s62","s63","s64","s65","s66","s67","s68","s69","s70","s71","vcc","scc","memory");
}


extern "C" void kernel_launch(void* const* d_in, const int* in_sizes, int n_in, void* d_out, int out_size, void* d_ws, size_t ws_size, hipStream_t stream) {

  hipLaunchKernelGGL(mha_fwd_kernel, dim3(256), dim3(256), 0, stream, (const char*)d_in[0], (const char*)d_in[1], (const char*)d_in[2], (char*)d_out);

}
```
